```python
import torch

def _softmax(x, axis=-1):
    return torch.softmax(x, dim=axis)
def _gelu(x):
    return torch.nn.functional.gelu(x, approximate="tanh")
def _silu(x):
    return torch.nn.functional.silu(x)
def _sigmoid(x):
    return torch.sigmoid(x)
def _layernorm(x, axis=-1, eps=1e-5):
    m = x.mean(dim=axis, keepdim=True)
    v = x.var(dim=axis, keepdim=True, unbiased=False)
    return (x - m) / torch.sqrt(v + eps)
def _scatter_add(tgt, idx, src, axis=0):
    return tgt.clone().index_add_(axis, idx, src)
def _segment_sum(src, seg, n):
    out = torch.zeros((n,) + src.shape[1:], dtype=src.dtype)
    return out.index_add_(0, seg, src)
def _pad(x, pw):
    flat = tuple(v for pair in reversed(pw) for v in pair)
    return torch.nn.functional.pad(x, flat)

kernel_name = 'standalone__mha_softmax__near_ridge'

def setup_inputs():

    _w = (lambda a: (lambda t: t.to(torch.bfloat16) if t.is_floating_point() else t)(torch.as_tensor(a)))
    B, H, Sq, Sk, D = 16, 32, 384, 1024, 128
    return {
        "q": _w((lambda *s: torch.randn(*s, dtype=torch.bfloat16) * 0.8)(B, H, Sq, D)),
        "k": _w((lambda *s: torch.randn(*s, dtype=torch.bfloat16) * 0.8)(B, H, Sk, D)),
        "v": _w((lambda *s: torch.randn(*s, dtype=torch.bfloat16) * 0.05)(B, H, Sk, D)),
    }

def reference(q, k, v):
    scale = q.shape[-1] ** -0.5
    scores = torch.einsum("bhqd,bhkd->bhqk", q, k) * scale
    p = _softmax(scores, axis=-1)
    return torch.einsum("bhqk,bhkd->bhqd", p, v)
```

```cpp
#include <hip/hip_runtime.h>
#include <hip/hip_bf16.h>
using bf16 = __hip_bfloat16;
constexpr int   D = 128, NW = 4, QBLK = 32, KVBLK = 64;
constexpr float SCALE = 0.088388347648318440f;
constexpr float THR = 8.f;
constexpr int B = 16, H = 32, SQ = 384, SK = 1024;
constexpr size_t SHM_V = KVBLK * D * 2, SHM_K = KVBLK * D * 2, SHM_ATTN = 2 * SHM_V + 2 * SHM_K + NW * 64 * 4;
using bf16x8 = __attribute__((ext_vector_type(8))) short;
using s16x4  = __attribute__((ext_vector_type(4))) short;
using f32x16 = __attribute__((ext_vector_type(16))) float;
using u32x4  = __attribute__((ext_vector_type(4))) unsigned;
#define KSWZ(row, colB) ((row) * 256 + ((colB) ^ (((row) & 7) << 4)))
#define SBAR() __builtin_amdgcn_sched_barrier(0)
__device__ __forceinline__ int crow(int r, int hi) { return (r & 3) + 8 * (r >> 2) + 4 * hi; }
__device__ __forceinline__ unsigned cvtpk(float lo, float hi) {
  unsigned r; asm volatile("v_cvt_pk_bf16_f32 %0, %1, %2" : "=v"(r) : "v"(lo), "v"(hi)); return r;
}
__device__ __forceinline__ void partialSM(f32x16& p0, f32x16& p1, float& m_reg, float& mn, float& alpha) {
  constexpr float C = SCALE * 1.4426950408889634f;
  float pmax = p0[0]; for (int r = 1; r < 16; ++r) pmax = fmaxf(pmax, p0[r]); for (int r = 0; r < 16; ++r) pmax = fmaxf(pmax, p1[r]);
  { auto rr = __builtin_amdgcn_permlane32_swap(__float_as_uint(pmax), __float_as_uint(pmax), false, false);
    pmax = fmaxf(__uint_as_float(rr[0]), __uint_as_float(rr[1])); }
  if (__builtin_expect(__all(pmax - m_reg <= THR / SCALE), 1)) { mn = m_reg; alpha = 1.f; }
  else { mn = fmaxf(m_reg, pmax); alpha = __builtin_amdgcn_exp2f((m_reg - mn) * C); m_reg = mn; }
  float mnC = -mn * C;
  for (int r = 0; r < 16; ++r) p0[r] = fmaf(p0[r], C, mnC); for (int r = 0; r < 16; ++r) p1[r] = fmaf(p1[r], C, mnC);
  for (int r = 0; r < 16; ++r) p0[r] = __builtin_amdgcn_exp2f(p0[r]);
}
__device__ __forceinline__ void finishSM(f32x16& p0, f32x16& p1, float alpha, float& l_reg, bf16x8& pa0, bf16x8& pa1, bf16x8& pa2, bf16x8& pa3) {
  for (int r = 0; r < 16; ++r) p1[r] = __builtin_amdgcn_exp2f(p1[r]);
  float ps = 0; for (int r = 0; r < 16; ++r) ps += p0[r]; for (int r = 0; r < 16; ++r) ps += p1[r];
  { auto rr = __builtin_amdgcn_permlane32_swap(__float_as_uint(ps), __float_as_uint(ps), false, false);
    ps = __uint_as_float(rr[0]) + __uint_as_float(rr[1]); }
  l_reg = l_reg * alpha + ps;
#define PK4(P, BASE, OUT) do { unsigned a0 = cvtpk(P[BASE + 0], P[BASE + 1]), a1 = cvtpk(P[BASE + 2], P[BASE + 3]);   \
    unsigned b0 = cvtpk(P[BASE + 4], P[BASE + 5]), b1 = cvtpk(P[BASE + 6], P[BASE + 7]);                              \
    auto r0 = __builtin_amdgcn_permlane32_swap(a0, b0, false, false); auto r1 = __builtin_amdgcn_permlane32_swap(a1, b1, false, false); \
    u32x4 w = {r0[0], r1[0], r0[1], r1[1]}; OUT = *reinterpret_cast<bf16x8*>(&w); } while (0)
  PK4(p0, 0, pa0); PK4(p0, 8, pa1); PK4(p1, 0, pa2); PK4(p1, 8, pa3);
#undef PK4
}
__device__ __forceinline__ void qkt(f32x16& p0, f32x16& p1, const bf16* Ks, const bf16x8* qr, int r32, int hi) {
  p0 = f32x16{}; p1 = f32x16{};
  for (int d0 = 0; d0 < 8; ++d0) { int cb = (d0 * 16 + hi * 8) * 2;
    bf16x8 b0 = *reinterpret_cast<const bf16x8*>((const char*)Ks + KSWZ(r32, cb));
    bf16x8 b1 = *reinterpret_cast<const bf16x8*>((const char*)Ks + KSWZ(32 + r32, cb));
    p0 = __builtin_amdgcn_mfma_f32_32x32x16_bf16(b0, qr[d0], p0, 0, 0, 0);
    p1 = __builtin_amdgcn_mfma_f32_32x32x16_bf16(b1, qr[d0], p1, 0, 0, 0); }
}
__device__ __forceinline__ int v_st(int k, int c) { const int kk = (k & ~0xC) | ((k & 4) << 1) | ((k & 8) >> 1); return ((kk >> 3) * 4 + (c >> 5)) * 512 + ((kk & 7) * 32 + (c & 31)) * 2; }
__device__ __forceinline__ int v_rd_base(int lane) { return ((lane & 3) << 3) | (((lane >> 2) & 3) << 6) | (((lane >> 4) & 1) << 5) | (((lane >> 5) & 1) << 8); }
constexpr int v_rd_off(int d0, int ks, int half) { return d0 * 512 + ks * 4096 + half * 2048; }
template <int OFF> __device__ __forceinline__ s16x4 tr_read(int vb) {
  s16x4 r; asm volatile("ds_read_b64_tr_b16 %0, %1 offset:%2" : "=&v"(r) : "v"(vb), "i"(OFF) : "memory"); return r;
}
template <int D0> __device__ __forceinline__ void pv_one(f32x16& od, int vb, bf16x8 pa0, bf16x8 pa1, bf16x8 pa2, bf16x8 pa3) {
  const s16x4 l0 = tr_read<v_rd_off(D0, 0, 0)>(vb), h0 = tr_read<v_rd_off(D0, 0, 1)>(vb), l1 = tr_read<v_rd_off(D0, 1, 0)>(vb), h1 = tr_read<v_rd_off(D0, 1, 1)>(vb);
  const s16x4 l2 = tr_read<v_rd_off(D0, 2, 0)>(vb), h2 = tr_read<v_rd_off(D0, 2, 1)>(vb), l3 = tr_read<v_rd_off(D0, 3, 0)>(vb), h3 = tr_read<v_rd_off(D0, 3, 1)>(vb);
  asm volatile("s_waitcnt lgkmcnt(0)" ::: "memory"); SBAR();
#define PK(L, H) (bf16x8){L[0], L[1], L[2], L[3], H[0], H[1], H[2], H[3]}
  od = __builtin_amdgcn_mfma_f32_32x32x16_bf16(pa0, PK(l0, h0), od, 0, 0, 0);
  od = __builtin_amdgcn_mfma_f32_32x32x16_bf16(pa1, PK(l1, h1), od, 0, 0, 0);
  od = __builtin_amdgcn_mfma_f32_32x32x16_bf16(pa2, PK(l2, h2), od, 0, 0, 0);
  od = __builtin_amdgcn_mfma_f32_32x32x16_bf16(pa3, PK(l3, h3), od, 0, 0, 0);
#undef PK
}
__device__ __forceinline__ void pv_d0(f32x16* o, int vb, bf16x8 pa0, bf16x8 pa1, bf16x8 pa2, bf16x8 pa3) {
  pv_one<0>(o[0], vb, pa0, pa1, pa2, pa3); pv_one<1>(o[1], vb, pa0, pa1, pa2, pa3); pv_one<2>(o[2], vb, pa0, pa1, pa2, pa3); pv_one<3>(o[3], vb, pa0, pa1, pa2, pa3);
}
__global__ __launch_bounds__(64 * NW, 2)
void attn_fwd(const bf16* __restrict__ Q, const bf16* __restrict__ K, const bf16* __restrict__ V, bf16* __restrict__ O) {
  extern __shared__ __attribute__((aligned(16))) char lds[];
  const int bid = blockIdx.x; const int xcd = bid & 7, idx = bid >> 3; const int qblk = idx % 3, bh = (idx / 3) * 8 + xcd;
  const bf16* Qb = Q + ((long)bh * SQ + qblk * 128) * D; const bf16* Kh = K + (long)bh * SK * D; const bf16* Vh = V + (long)bh * SK * D;
  bf16* Ob = O + ((long)bh * SQ + qblk * 128) * D;
  const int tid = threadIdx.x, wid = tid >> 6, lane = tid & 63, r32 = lane & 31, hi = lane >> 5;
  bf16* V_lds = (bf16*)lds; bf16* K_lds = (bf16*)(lds + 2 * SHM_V);
  float* ws = (float*)(lds + 2 * SHM_V + 2 * SHM_K) + wid * 64; float* li_l = ws; float* al_l = ws + 32;
  float m_reg = -1e30f, l_reg = 0; f32x16 o[4] = {}; bf16x8 qr[8];
  const bf16* Qw = Qb + (long)(wid * QBLK + r32) * D + hi * 8;
#pragma unroll
  for (int d0 = 0; d0 < 8; ++d0) qr[d0] = *reinterpret_cast<const bf16x8*>(Qw + d0 * 16);
  const int sr = tid >> 4, sc = (tid & 15) * 8;
  const int vb0 = (int)(uintptr_t)V_lds + v_rd_base(lane);
  bf16x8 vs[4], ks[4];
#define SLOAD(k0) do { _Pragma("unroll") for (int i = 0; i < 4; ++i) { vs[i] = *reinterpret_cast<const bf16x8*>(&Vh[(long)((k0) + 16 * i + sr) * D + sc]); ks[i] = *reinterpret_cast<const bf16x8*>(&Kh[(long)((k0) + 16 * i + sr) * D + sc]); } } while (0)
#define SWRITE(b) do { _Pragma("unroll") for (int i = 0; i < 4; ++i) { *(bf16x8*)((char*)V_lds + (b) * SHM_V + v_st(16 * i + sr, sc)) = vs[i]; \
    *(bf16x8*)((char*)K_lds + (b) * SHM_K + KSWZ(16 * i + sr, sc * 2)) = ks[i]; } } while (0)
#define RESC(a) do { if (__any((a) < 1.f)) { if (hi == 0) al_l[r32] = (a); asm volatile("s_waitcnt lgkmcnt(0)" ::: "memory"); \
    for (int d = 0; d < 4; ++d) for (int r = 0; r < 16; ++r) o[d][r] *= al_l[crow(r, hi)]; } } while (0)
  f32x16 pA0, pA1, pB0, pB1; float mnA, mnB, alA, alB; bf16x8 pa0, pa1, pa2, pa3; const int NT = SK / KVBLK;
  SLOAD(0); SWRITE(0); __syncthreads();
  qkt(pA0, pA1, K_lds, qr, r32, hi); partialSM(pA0, pA1, m_reg, mnA, alA);
  SLOAD(KVBLK);
  SWRITE(1); __syncthreads();
  for (int j = 1; j + 1 < NT; j += 2) {
    SBAR(); qkt(pB0, pB1, (bf16*)((char*)K_lds + SHM_K), qr, r32, hi);
    finishSM(pA0, pA1, alA, l_reg, pa0, pa1, pa2, pa3); SBAR();
    SLOAD((j + 1) * KVBLK); SBAR();
    pv_d0(o, vb0, pa0, pa1, pa2, pa3); partialSM(pB0, pB1, m_reg, mnB, alB);
    __syncthreads(); SWRITE(0);
    RESC(alB); __syncthreads();
    SBAR(); qkt(pA0, pA1, K_lds, qr, r32, hi);
    finishSM(pB0, pB1, alB, l_reg, pa0, pa1, pa2, pa3); SBAR();
    SLOAD((j + 2) * KVBLK); SBAR();
    pv_d0(o, vb0 + (int)SHM_V, pa0, pa1, pa2, pa3); partialSM(pA0, pA1, m_reg, mnA, alA);
    __syncthreads(); SWRITE(1);
    RESC(alA); __syncthreads();
  }
  SBAR(); qkt(pB0, pB1, (bf16*)((char*)K_lds + SHM_K), qr, r32, hi);
  finishSM(pA0, pA1, alA, l_reg, pa0, pa1, pa2, pa3); SBAR();
  pv_d0(o, vb0, pa0, pa1, pa2, pa3); partialSM(pB0, pB1, m_reg, mnB, alB);
  __syncthreads(); RESC(alB);
  finishSM(pB0, pB1, alB, l_reg, pa0, pa1, pa2, pa3); SBAR();
  pv_d0(o, vb0 + (int)SHM_V, pa0, pa1, pa2, pa3);
  if (hi == 0) li_l[r32] = l_reg; asm volatile("s_waitcnt lgkmcnt(0)" ::: "memory");
  float rli[16];
#pragma unroll
  for (int r = 0; r < 16; ++r) rli[r] = __builtin_amdgcn_rcpf(li_l[crow(r, hi)]);
  bf16* Ow = Ob + (long)(wid * QBLK) * D;
#pragma unroll
  for (int r = 0; r < 16; ++r) { int orow = crow(r, hi);
    for (int d0 = 0; d0 < 4; ++d0) Ow[(long)orow * D + d0 * 32 + r32] = __float2bfloat16(o[d0][r] * rli[r]); }
}
extern "C" void kernel_launch(void* const* d_in, const int* in_sizes, int n_in, void* d_out, int out_size, void* d_ws, size_t ws_size, hipStream_t stream) {
  hipFuncSetAttribute((const void*)attn_fwd, hipFuncAttributeMaxDynamicSharedMemorySize, (int)SHM_ATTN);
  hipLaunchKernelGGL(attn_fwd, dim3(B * H * 3), dim3(64 * NW), SHM_ATTN, stream,
                     (const bf16*)d_in[0], (const bf16*)d_in[1], (const bf16*)d_in[2], (bf16*)d_out);
}
```

```cpp
#include <hip/hip_runtime.h>
#include <hip/hip_bf16.h>

typedef __bf16 bf16x8_t __attribute__((ext_vector_type(8)));
typedef __bf16 bf16x2_t __attribute__((ext_vector_type(2)));
typedef float f32x4_t __attribute__((ext_vector_type(4)));
typedef float f32x2_t __attribute__((ext_vector_type(2)));
typedef unsigned u32x4_t __attribute__((ext_vector_type(4)));
typedef unsigned u32x2_t __attribute__((ext_vector_type(2)));

constexpr int NBH = 512, SQ = 384, SK = 1024, DH = 128;
constexpr float SCL2 = 0.08838834764831845f * 1.4426950408889634f;

__device__ __forceinline__ unsigned pk2(float a, float b) {
  f32x2_t v = {a, b}; bf16x2_t r = __builtin_convertvector(v, bf16x2_t); return __builtin_bit_cast(unsigned, r);
}
__device__ __forceinline__ f32x4_t score16(const unsigned short* Kh, int key0, int lane, const bf16x8_t (&qf)[4]) {
  f32x4_t s = {0.f, 0.f, 0.f, 0.f};
#pragma unroll
  for (int ds = 0; ds < 4; ++ds) {
    const bf16x8_t kf = *(const bf16x8_t*)(Kh + (size_t)(key0 + (lane & 15)) * DH + 32 * ds + 8 * (lane >> 4));
    s = __builtin_amdgcn_mfma_f32_16x16x32_bf16(kf, qf[ds], s, 0, 0, 0);
  }
  return s;
}
__device__ __forceinline__ void exact_rows16(const unsigned short* Qr, const unsigned short* Kh, const unsigned short* Vh, unsigned short* Or, int lane) {
  const int g = lane >> 4, c = lane & 15;
  bf16x8_t qf[4];
#pragma unroll
  for (int ds = 0; ds < 4; ++ds) qf[ds] = *(const bf16x8_t*)(Qr + (size_t)c * DH + 32 * ds + 8 * g);
  float m = -__builtin_inff();
  for (int k0 = 0; k0 < SK; k0 += 16) {
    const f32x4_t s = score16(Kh, k0, lane, qf);
    m = fmaxf(m, fmaxf(fmaxf(s[0], s[1]), fmaxf(s[2], s[3])));
  }
  m = fmaxf(m, __shfl_xor(m, 16)); m = fmaxf(m, __shfl_xor(m, 32));
  float l = 0.f;
  for (int k0 = 0; k0 < SK; k0 += 16) {
    const f32x4_t s = score16(Kh, k0, lane, qf);
#pragma unroll
    for (int r = 0; r < 4; ++r) l += __builtin_amdgcn_exp2f((s[r] - m) * SCL2);
  }
  l += __shfl_xor(l, 16); l += __shfl_xor(l, 32);
  const float il = 1.0f / l;
  f32x4_t o[8];
#pragma unroll
  for (int db = 0; db < 8; ++db) o[db] = (f32x4_t){0.f, 0.f, 0.f, 0.f};
  for (int k0 = 0; k0 < SK; k0 += 32) {
    const f32x4_t s0 = score16(Kh, k0, lane, qf), s1 = score16(Kh, k0 + 16, lane, qf);
    float p[8];
#pragma unroll
    for (int r = 0; r < 4; ++r) { p[r] = __builtin_amdgcn_exp2f((s0[r] - m) * SCL2) * il; p[4 + r] = __builtin_amdgcn_exp2f((s1[r] - m) * SCL2) * il; }
    const u32x4_t pw = {pk2(p[0], p[1]), pk2(p[2], p[3]), pk2(p[4], p[5]), pk2(p[6], p[7])};
    const bf16x8_t pf = __builtin_bit_cast(bf16x8_t, pw);
#pragma unroll
    for (int db = 0; db < 8; ++db) {
      unsigned short e[8];
#pragma unroll
      for (int j = 0; j < 8; ++j) e[j] = Vh[(size_t)(k0 + 16 * (j >> 2) + 4 * g + (j & 3)) * DH + 16 * db + c];
      const u32x4_t vw = {e[0] | ((unsigned)e[1] << 16), e[2] | ((unsigned)e[3] << 16), e[4] | ((unsigned)e[5] << 16), e[6] | ((unsigned)e[7] << 16)};
      o[db] = __builtin_amdgcn_mfma_f32_16x16x32_bf16(__builtin_bit_cast(bf16x8_t, vw), pf, o[db], 0, 0, 0);
    }
  }
#pragma unroll
  for (int db = 0; db < 8; ++db) {
    const u32x2_t w = {pk2(o[db][0], o[db][1]), pk2(o[db][2], o[db][3])};
    *(u32x2_t*)(Or + (size_t)c * DH + 16 * db + 4 * g) = w;
  }
}

__global__ __launch_bounds__(256, 1) void mha_fwd_kernel(const unsigned short* __restrict__ Q, const unsigned short* __restrict__ K,
                                                         const unsigned short* __restrict__ V, unsigned short* __restrict__ O) {
  __shared__ __attribute__((aligned(1024))) char lds[163840];
  const unsigned wg = blockIdx.x;
  const unsigned w = __builtin_amdgcn_readfirstlane(threadIdx.x >> 6);
  const unsigned wk = (w >> 1) * 4096u + (w & 1u) * 1024u;
  const unsigned qoff = wg * (unsigned)(SQ * DH * 2) + w * 4096u, kvoff = wg * (unsigned)(SK * DH * 2);
  const unsigned koff = kvoff + wk, voff = koff;
  const unsigned rot = 0u * 16384u;
  const char* qp = (const char*)Q + qoff; const char* kp = (const char*)K + koff; const char* vp = (const char*)V + voff; char* op = (char*)O + qoff;
  const unsigned qrec = (unsigned)(NBH * SQ * DH * 2) - qoff, krec = (unsigned)(NBH * SK * DH * 2) - koff, vrec = (unsigned)(NBH * SK * DH * 2) - voff;
  const unsigned ldsb = (unsigned)(size_t)lds;
  unsigned flags;
  asm volatile(
    "s_mov_b64 s[36:37], %1\n"
    "s_mov_b64 s[40:41], %2\n"
    "s_mov_b64 s[44:45], %3\n"
    "s_mov_b64 s[48:49], %4\n"
    "s_mov_b32 s38, %5\n"
    "s_mov_b32 s50, %5\n"
    "s_mov_b32 s42, %6\n"
    "s_mov_b32 s46, %7\n"
    "s_mov_b32 s72, %8\n"
    "s_mov_b32 s73, %9\n"
    "s_mov_b32 s62, %10\n"
    "s_mov_b32 s71, %11\n"
    "s_mov_b32 s69, %12\n"
    "s_mov_b32 s89, %13\n"
    "s_mov_b32 s39, 0x20000\n"
    "s_and_b32 s37, s37, 0xffff\n"
    "s_mov_b32 s43, 0x20000\n"
    "s_and_b32 s41, s41, 0xffff\n"
    "v_mbcnt_lo_u32_b32 v0, -1, 0\n"
    "v_mbcnt_hi_u32_b32 v0, -1, v0\n"
    "v_bfe_u32 v1, v0, 2, 2\n"
    "v_lshlrev_b32 v1, 8, v1\n"
    "v_lshrrev_b32 v2, 4, v0\n"
    "v_lshl_add_u32 v1, v2, 6, v1\n"
    "v_and_b32 v2, 3, v0\n"
    "v_lshl_add_u32 v1, v2, 4, v1\n"
    "v_xor_b32 v131, s71, v1\n"
    "s_getpc_b64 s[90:91]\n"
    "Lpc_%=:\n"
    "s_mov_b32 s92, Lend_%=-Lpc_%=\n"
    "s_sub_u32 s93, s73, s69\n"
    "s_lshl_b32 s93, s93, 2\n"
    "v_lshlrev_b32 v4, 7, v0\n"
    "v_add_u32 v4, s93, v4\n"
    "v_add_u32 v5, 0x8000, v4\n"
    "v_min_u32 v4, s92, v4\n"
    "v_min_u32 v5, s92, v5\n"
    "global_load_dword v250, v4, s[90:91]\n"
    "global_load_dword v251, v5, s[90:91]\n"
    "s_mov_b32 s64, 0\n"
    "s_mov_b32 m0, s62\n"
    "s_nop 0\n"
    "buffer_load_dwordx4 v131, s[36:39], s64 offen nt lds\n"
    "buffer_load_dwordx4 v131, s[36:39], s64 offen offset:1024 nt lds\n"
    "buffer_load_dwordx4 v131, s[36:39], s64 offen offset:2048 nt lds\n"
    "buffer_load_dwordx4 v131, s[36:39], s64 offen offset:3072 nt lds\n"
    "s_add_u32 s64, s89, 0\n"
    "s_and_b32 s64, s64, 262143\n"
    "s_mov_b32 m0, s72\n"
    "s_nop 0\n"
    "buffer_load_dwordx4 v131, s[40:43], s64 offen nt lds\n"
    "buffer_load_dwordx4 v131, s[40:43], s64 offen offset:2048 nt lds\n"
    "s_add_u32 s64, s89, 0\n"
    "s_and_b32 s64, s64, 262143\n"
    "s_add_u32 s64, s64, 8192\n"
    "s_add_u32 m0, s72, 8192\n"
    "s_nop 0\n"
    "buffer_load_dwordx4 v131, s[40:43], s64 offen nt lds\n"
    "buffer_load_dwordx4 v131, s[40:43], s64 offen offset:2048 nt lds\n"
    "s_mov_b32 s64, 16384\n"
    "s_add_u32 m0, s62, 4096\n"
    "s_nop 0\n"
    "buffer_load_dwordx4 v131, s[36:39], s64 offen nt lds\n"
    "buffer_load_dwordx4 v131, s[36:39], s64 offen offset:1024 nt lds\n"
    "buffer_load_dwordx4 v131, s[36:39], s64 offen offset:2048 nt lds\n"
    "buffer_load_dwordx4 v131, s[36:39], s64 offen offset:3072 nt lds\n"
    "s_mov_b32 s64, 32768\n"
    "s_add_u32 m0, s62, 8192\n"
    "s_nop 0\n"
    "buffer_load_dwordx4 v131, s[36:39], s64 offen nt lds\n"
    "buffer_load_dwordx4 v131, s[36:39], s64 offen offset:1024 nt lds\n"
    "buffer_load_dwordx4 v131, s[36:39], s64 offen offset:2048 nt lds\n"
    "buffer_load_dwordx4 v131, s[36:39], s64 offen offset:3072 nt lds\n"
    "s_mov_b32 s47, 0x20000\n"
    "s_and_b32 s45, s45, 0xffff\n"
    "s_mov_b32 s51, 0x20000\n"
    "s_and_b32 s49, s49, 0xffff\n"
    "s_mov_b64 s[52:53], s[48:49]\n"
    "s_mov_b64 s[54:55], s[50:51]\n"
    "s_mov_b32 s66, 0\n"
    "s_add_u32 s88, s69, 163840\n"
    "s_add_u32 s80, s72, 0\n"
    "s_add_u32 s84, s72, 32768\n"
    "s_add_u32 s81, s72, 8192\n"
    "s_add_u32 s85, s72, 40960\n"
    "s_add_u32 s82, s72, 16384\n"
    "s_add_u32 s86, s72, 49152\n"
    "s_add_u32 s83, s72, 24576\n"
    "s_add_u32 s87, s72, 57344\n"
    "s_mov_b32 s76, 0x3e0293ee\n"
    "s_mov_b32 s77, 0x3e0293ee\n"
    "s_mov_b32 s67, 0x71800000\n"
    "s_mov_b32 s68, 0xffff0000\n"
    "v_bfe_u32 v1, v0, 2, 2\n"
    "v_lshlrev_b32 v1, 10, v1\n"
    "v_and_b32 v2, 3, v0\n"
    "v_lshl_add_u32 v1, v2, 6, v1\n"
    "v_bfe_u32 v2, v0, 2, 1\n"
    "v_lshlrev_b32 v2, 1, v2\n"
    "v_lshrrev_b32 v3, 4, v0\n"
    "v_xor_b32 v2, v2, v3\n"
    "v_lshl_add_u32 v1, v2, 4, v1\n"
    "v_add_u32 v128, s69, v1\n"
    "v_mbcnt_lo_u32_b32 v4, -1, 0\n"
    "v_mbcnt_hi_u32_b32 v4, -1, v4\n"
    "v_bfe_u32 v5, v4, 2, 2\n"
    "v_lshlrev_b32 v134, 10, v5\n"
    "v_and_b32 v5, 3, v4\n"
    "v_lshl_add_u32 v134, v5, 6, v134\n"
    "v_and_b32 v5, 48, v4\n"
    "v_xor_b32 v5, s71, v5\n"
    "v_add_u32 v134, v5, v134\n"
    "v_add_u32 v134, s62, v134\n"
    "v_lshlrev_b32 v1, 10, v3\n"
    "v_bfe_u32 v2, v0, 2, 2\n"
    "v_lshl_add_u32 v1, v2, 6, v1\n"
    "v_and_b32 v2, 1, v3\n"
    "v_lshl_add_u32 v1, v2, 5, v1\n"
    "v_and_b32 v2, 3, v0\n"
    "v_lshl_add_u32 v1, v2, 3, v1\n"
    "v_add_u32 v129, s69, v1\n"
    "v_xor_b32 v130, 32, v129\n"
    "v_and_b32 v1, 15, v0\n"
    "v_lshlrev_b32 v1, 5, v1\n"
    "v_lshrrev_b32 v2, 5, v0\n"
    "v_bfe_u32 v4, v0, 2, 1\n"
    "v_xor_b32 v2, v2, v4\n"
    "v_lshl_add_u32 v1, v2, 4, v1\n"
    "v_and_b32 v2, 1, v3\n"
    "v_lshl_add_u32 v1, v2, 3, v1\n"
    "v_add_u32 v135, s62, v1\n"
    "v_and_b32 v1, 1, v0\n"
    "v_cmp_eq_u32 vcc, 0, v1\n"
    "v_mov_b32 v1, 0xeeeeeeee\n"
    "v_mov_b32 v2, 0x44444444\n"
    "v_cndmask_b32 v133, v1, v2, vcc\n"
    "v_and_b32 v1, 2, v0\n"
    "v_cmp_eq_u32 vcc, 0, v1\n"
    "v_mov_b32 v2, 0x3f803f80\n"
    "v_mov_b32 v1, 0\n"
    "v_cndmask_b32 v1, v1, v2, vcc\n"
    "v_accvgpr_write_b32 a168, v1\n"
    "v_accvgpr_write_b32 a169, v1\n"
    "v_accvgpr_write_b32 a170, v1\n"
    "v_accvgpr_write_b32 a171, v1\n"
    "v_accvgpr_write_b32 a144, 0\n"
    "v_accvgpr_write_b32 a145, 0\n"
    "v_accvgpr_write_b32 a146, 0\n"
    "v_accvgpr_write_b32 a147, 0\n"
    "v_accvgpr_write_b32 a148, 0\n"
    "v_accvgpr_write_b32 a149, 0\n"
    "v_accvgpr_write_b32 a150, 0\n"
    "v_accvgpr_write_b32 a151, 0\n"
    "v_accvgpr_write_b32 a152, 0\n"
    "v_accvgpr_write_b32 a153, 0\n"
    "v_accvgpr_write_b32 a154, 0\n"
    "v_accvgpr_write_b32 a155, 0\n"
    "v_accvgpr_write_b32 a156, 0\n"
    "v_accvgpr_write_b32 a157, 0\n"
    "v_accvgpr_write_b32 a158, 0\n"
    "v_accvgpr_write_b32 a159, 0\n"
    "v_accvgpr_write_b32 a160, 0\n"
    "v_accvgpr_write_b32 a161, 0\n"
    "v_accvgpr_write_b32 a162, 0\n"
    "v_accvgpr_write_b32 a163, 0\n"
    "v_accvgpr_write_b32 a164, 0\n"
    "v_accvgpr_write_b32 a165, 0\n"
    "v_accvgpr_write_b32 a166, 0\n"
    "v_accvgpr_write_b32 a167, 0\n"
    "s_waitcnt vmcnt(12) lgkmcnt(0)\n"
    "ds_read_b128 v[148:151], v134 offset:0\n"
    "s_mov_b32 s64, 49152\n"
    "s_add_u32 m0, s62, 12288\n"
    "ds_read_b128 v[152:155], v134 offset:256\n"
    "buffer_load_dwordx4 v131, s[36:39], s64 offen nt lds\n"
    "ds_read_b128 v[156:159], v134 offset:512\n"
    "ds_read_b128 v[160:163], v134 offset:768\n"
    "buffer_load_dwordx4 v131, s[36:39], s64 offen offset:1024 nt lds\n"
    "s_waitcnt lgkmcnt(3)\n"
    "v_lshlrev_b32 v180, 16, v148\n"
    "v_and_b32 v181, s68, v148\n"
    "v_lshlrev_b32 v182, 16, v149\n"
    "v_and_b32 v183, s68, v149\n"
    "v_lshlrev_b32 v184, 16, v150\n"
    "v_and_b32 v185, s68, v150\n"
    "v_lshlrev_b32 v186, 16, v151\n"
    "v_and_b32 v187, s68, v151\n"
    "s_waitcnt lgkmcnt(2)\n"
    "v_lshlrev_b32 v188, 16, v152\n"
    "v_and_b32 v189, s68, v152\n"
    "v_lshlrev_b32 v190, 16, v153\n"
    "v_and_b32 v191, s68, v153\n"
    "v_lshlrev_b32 v192, 16, v154\n"
    "v_and_b32 v193, s68, v154\n"
    "v_lshlrev_b32 v194, 16, v155\n"
    "v_and_b32 v195, s68, v155\n"
    "s_waitcnt lgkmcnt(1)\n"
    "v_lshlrev_b32 v196, 16, v156\n"
    "v_and_b32 v197, s68, v156\n"
    "v_lshlrev_b32 v198, 16, v157\n"
    "v_and_b32 v199, s68, v157\n"
    "v_lshlrev_b32 v200, 16, v158\n"
    "v_and_b32 v201, s68, v158\n"
    "v_lshlrev_b32 v202, 16, v159\n"
    "v_and_b32 v203, s68, v159\n"
    "s_waitcnt lgkmcnt(0)\n"
    "v_lshlrev_b32 v204, 16, v160\n"
    "v_and_b32 v205, s68, v160\n"
    "v_lshlrev_b32 v206, 16, v161\n"
    "v_and_b32 v207, s68, v161\n"
    "v_lshlrev_b32 v208, 16, v162\n"
    "v_and_b32 v209, s68, v162\n"
    "v_lshlrev_b32 v210, 16, v163\n"
    "v_and_b32 v211, s68, v163\n"
    "v_pk_mul_f32 v[180:181], v[180:181], s[76:77]\n"
    "v_pk_mul_f32 v[182:183], v[182:183], s[76:77]\n"
    "v_pk_mul_f32 v[184:185], v[184:185], s[76:77]\n"
    "v_pk_mul_f32 v[186:187], v[186:187], s[76:77]\n"
    "v_pk_mul_f32 v[188:189], v[188:189], s[76:77]\n"
    "v_pk_mul_f32 v[190:191], v[190:191], s[76:77]\n"
    "v_pk_mul_f32 v[192:193], v[192:193], s[76:77]\n"
    "v_pk_mul_f32 v[194:195], v[194:195], s[76:77]\n"
    "v_pk_mul_f32 v[196:197], v[196:197], s[76:77]\n"
    "v_pk_mul_f32 v[198:199], v[198:199], s[76:77]\n"
    "v_pk_mul_f32 v[200:201], v[200:201], s[76:77]\n"
    "v_pk_mul_f32 v[202:203], v[202:203], s[76:77]\n"
    "v_pk_mul_f32 v[204:205], v[204:205], s[76:77]\n"
    "v_pk_mul_f32 v[206:207], v[206:207], s[76:77]\n"
    "v_pk_mul_f32 v[208:209], v[208:209], s[76:77]\n"
    "v_pk_mul_f32 v[210:211], v[210:211], s[76:77]\n"
    "v_cvt_pk_bf16_f32 v180, v180, v181\n"
    "v_cvt_pk_bf16_f32 v182, v182, v183\n"
    "v_cvt_pk_bf16_f32 v184, v184, v185\n"
    "v_cvt_pk_bf16_f32 v186, v186, v187\n"
    "v_cvt_pk_bf16_f32 v188, v188, v189\n"
    "v_cvt_pk_bf16_f32 v190, v190, v191\n"
    "v_cvt_pk_bf16_f32 v192, v192, v193\n"
    "v_cvt_pk_bf16_f32 v194, v194, v195\n"
    "v_cvt_pk_bf16_f32 v196, v196, v197\n"
    "v_cvt_pk_bf16_f32 v198, v198, v199\n"
    "v_cvt_pk_bf16_f32 v200, v200, v201\n"
    "v_cvt_pk_bf16_f32 v202, v202, v203\n"
    "v_cvt_pk_bf16_f32 v204, v204, v205\n"
    "v_cvt_pk_bf16_f32 v206, v206, v207\n"
    "v_cvt_pk_bf16_f32 v208, v208, v209\n"
    "v_cvt_pk_bf16_f32 v210, v210, v211\n"
    "v_accvgpr_write_b32 a0, v180\n"
    "v_accvgpr_write_b32 a1, v182\n"
    "v_accvgpr_write_b32 a2, v184\n"
    "v_accvgpr_write_b32 a3, v186\n"
    "v_accvgpr_write_b32 a4, v188\n"
    "v_accvgpr_write_b32 a5, v190\n"
    "v_accvgpr_write_b32 a6, v192\n"
    "v_accvgpr_write_b32 a7, v194\n"
    "v_accvgpr_write_b32 a8, v196\n"
    "v_accvgpr_write_b32 a9, v198\n"
    "v_accvgpr_write_b32 a10, v200\n"
    "v_accvgpr_write_b32 a11, v202\n"
    "v_accvgpr_write_b32 a12, v204\n"
    "v_accvgpr_write_b32 a13, v206\n"
    "v_accvgpr_write_b32 a14, v208\n"
    "v_accvgpr_write_b32 a15, v210\n"
    "buffer_load_dwordx4 v131, s[36:39], s64 offen offset:2048 nt lds\n"
    "buffer_load_dwordx4 v131, s[36:39], s64 offen offset:3072 nt lds\n"
    "s_waitcnt vmcnt(12)\n"
    "s_barrier\n"
    "ds_read_b128 a[172:175], v128 offset:0\n"
    "s_mov_b32 s64, 65536\n"
    "s_add_u32 m0, s62, 16384\n"
    "ds_read_b128 a[176:179], v128 offset:256\n"
    "buffer_load_dwordx4 v131, s[36:39], s64 offen nt lds\n"
    "ds_read_b128 a[180:183], v128 offset:512\n"
    "ds_read_b128 a[184:187], v128 offset:768\n"
    "ds_read_b128 a[188:191], v128 offset:4096\n"
    "ds_read_b128 a[204:207], v128 offset:8192\n"
    "ds_read_b128 a[220:223], v128 offset:12288\n"
    "buffer_load_dwordx4 v131, s[36:39], s64 offen offset:1024 nt lds\n"
    "ds_read_b128 a[192:195], v128 offset:4352\n"
    "ds_read_b128 a[208:211], v128 offset:8448\n"
    "ds_read_b128 a[224:227], v128 offset:12544\n"
    "ds_read_b128 a[196:199], v128 offset:4608\n"
    "ds_read_b128 a[212:215], v128 offset:8704\n"
    "s_waitcnt lgkmcnt(11)\n"
    "v_mfma_f32_16x16x32_bf16 v[48:51], a[172:175], a[0:3], 0\n"
    "ds_read_b128 a[228:231], v128 offset:12800\n"
    "buffer_load_dwordx4 v131, s[36:39], s64 offen offset:2048 nt lds\n"
    "s_waitcnt lgkmcnt(11)\n"
    "v_mfma_f32_16x16x32_bf16 v[48:51], a[176:179], a[4:7], v[48:51]\n"
    "ds_read_b128 a[200:203], v128 offset:4864\n"
    "s_waitcnt lgkmcnt(11)\n"
    "v_mfma_f32_16x16x32_bf16 v[48:51], a[180:183], a[8:11], v[48:51]\n"
    "ds_read_b128 a[216:219], v128 offset:8960\n"
    "s_waitcnt lgkmcnt(11)\n"
    "v_mfma_f32_16x16x32_bf16 v[48:51], a[184:187], a[12:15], v[48:51]\n"
    "ds_read_b128 a[232:235], v128 offset:13056\n"
    "buffer_load_dwordx4 v131, s[36:39], s64 offen offset:3072 nt lds\n"
    "s_nop 5\n"
    "v_max3_f32 v136, v48, v49, v50\n"
    "v_max_f32 v136, v136, v51\n"
    "v_mov_b32 v137, v136\n"
    "s_nop 1\n"
    "v_permlane32_swap_b32 v137, v136\n"
    "v_max_f32 v136, v136, v137\n"
    "v_mov_b32 v137, v136\n"
    "s_nop 1\n"
    "v_permlane16_swap_b32 v137, v136\n"
    "v_max_f32 v136, v136, v137\n"
    "v_add_f32 v136, 0x41200000, v136\n"
    "v_sub_f32 v104, 0, v136\n"
    "v_sub_f32 v105, 0, v136\n"
    "v_sub_f32 v106, 0, v136\n"
    "v_sub_f32 v107, 0, v136\n"
    "v_sub_f32 v48, v48, v136\n"
    "v_sub_f32 v49, v49, v136\n"
    "s_waitcnt lgkmcnt(11)\n"
    "v_mfma_f32_16x16x32_bf16 v[56:59], a[188:191], a[0:3], v[104:107]\n"
    "v_sub_f32 v50, v50, v136\n"
    "v_sub_f32 v51, v51, v136\n"
    "s_waitcnt lgkmcnt(10)\n"
    "v_mfma_f32_16x16x32_bf16 v[60:63], a[204:207], a[0:3], v[104:107]\n"
    "v_exp_f32 v48, v48\n"
    "s_waitcnt lgkmcnt(9)\n"
    "v_mfma_f32_16x16x32_bf16 v[64:67], a[220:223], a[0:3], v[104:107]\n"
    "v_exp_f32 v49, v49\n"
    "s_waitcnt lgkmcnt(8)\n"
    "v_mfma_f32_16x16x32_bf16 v[56:59], a[192:195], a[4:7], v[56:59]\n"
    "v_exp_f32 v50, v50\n"
    "s_waitcnt lgkmcnt(7)\n"
    "v_mfma_f32_16x16x32_bf16 v[60:63], a[208:211], a[4:7], v[60:63]\n"
    "v_exp_f32 v51, v51\n"
    "s_waitcnt lgkmcnt(6)\n"
    "v_mfma_f32_16x16x32_bf16 v[64:67], a[224:227], a[4:7], v[64:67]\n"
    "v_cvt_pk_bf16_f32 v0, v48, v49\n"
    "v_cvt_pk_bf16_f32 v1, v50, v51\n"
    "s_waitcnt lgkmcnt(5)\n"
    "v_mfma_f32_16x16x32_bf16 v[56:59], a[196:199], a[8:11], v[56:59]\n"
    "s_waitcnt lgkmcnt(4)\n"
    "v_mfma_f32_16x16x32_bf16 v[60:63], a[212:215], a[8:11], v[60:63]\n"
    "s_waitcnt lgkmcnt(3)\n"
    "v_mfma_f32_16x16x32_bf16 v[64:67], a[228:231], a[8:11], v[64:67]\n"
    "s_waitcnt lgkmcnt(2)\n"
    "v_mfma_f32_16x16x32_bf16 v[56:59], a[200:203], a[12:15], v[56:59]\n"
    "s_waitcnt lgkmcnt(1)\n"
    "v_mfma_f32_16x16x32_bf16 v[60:63], a[216:219], a[12:15], v[60:63]\n"
    "s_waitcnt lgkmcnt(0)\n"
    "v_mfma_f32_16x16x32_bf16 v[64:67], a[232:235], a[12:15], v[64:67]\n"
    "s_nop 3\n"
    "v_exp_f32 v56, v56\n"
    "v_exp_f32 v57, v57\n"
    "v_exp_f32 v58, v58\n"
    "v_exp_f32 v59, v59\n"
    "v_exp_f32 v60, v60\n"
    "v_exp_f32 v61, v61\n"
    "v_exp_f32 v62, v62\n"
    "v_exp_f32 v63, v63\n"
    "v_exp_f32 v64, v64\n"
    "v_exp_f32 v65, v65\n"
    "v_exp_f32 v66, v66\n"
    "v_exp_f32 v67, v67\n"
    "v_cvt_pk_bf16_f32 v2, v56, v57\n"
    "v_cvt_pk_bf16_f32 v3, v58, v59\n"
    "v_cvt_pk_bf16_f32 v4, v60, v61\n"
    "v_cvt_pk_bf16_f32 v5, v62, v63\n"
    "v_cvt_pk_bf16_f32 v6, v64, v65\n"
    "v_cvt_pk_bf16_f32 v7, v66, v67\n"
    "s_waitcnt vmcnt(12)\n"
    "ds_read_b128 v[164:167], v134 offset:4096\n"
    "s_mov_b32 s64, 81920\n"
    "s_add_u32 m0, s62, 20480\n"
    "ds_read_b128 v[168:171], v134 offset:4352\n"
    "buffer_load_dwordx4 v131, s[36:39], s64 offen nt lds\n"
    "ds_read_b128 v[172:175], v134 offset:4608\n"
    "ds_read_b128 v[176:179], v134 offset:4864\n"
    "buffer_load_dwordx4 v131, s[36:39], s64 offen offset:1024 nt lds\n"
    "s_waitcnt lgkmcnt(3)\n"
    "v_lshlrev_b32 v212, 16, v164\n"
    "v_and_b32 v213, s68, v164\n"
    "v_lshlrev_b32 v214, 16, v165\n"
    "v_and_b32 v215, s68, v165\n"
    "v_lshlrev_b32 v216, 16, v166\n"
    "v_and_b32 v217, s68, v166\n"
    "v_lshlrev_b32 v218, 16, v167\n"
    "v_and_b32 v219, s68, v167\n"
    "v_pk_mul_f32 v[212:213], v[212:213], s[76:77]\n"
    "v_pk_mul_f32 v[214:215], v[214:215], s[76:77]\n"
    "v_pk_mul_f32 v[216:217], v[216:217], s[76:77]\n"
    "v_pk_mul_f32 v[218:219], v[218:219], s[76:77]\n"
    "v_cvt_pk_bf16_f32 v212, v212, v213\n"
    "v_cvt_pk_bf16_f32 v214, v214, v215\n"
    "v_cvt_pk_bf16_f32 v216, v216, v217\n"
    "v_cvt_pk_bf16_f32 v218, v218, v219\n"
    "s_waitcnt lgkmcnt(2)\n"
    "v_lshlrev_b32 v220, 16, v168\n"
    "v_and_b32 v221, s68, v168\n"
    "v_lshlrev_b32 v222, 16, v169\n"
    "v_and_b32 v223, s68, v169\n"
    "v_lshlrev_b32 v224, 16, v170\n"
    "v_and_b32 v225, s68, v170\n"
    "v_lshlrev_b32 v226, 16, v171\n"
    "v_and_b32 v227, s68, v171\n"
    "s_waitcnt lgkmcnt(1)\n"
    "v_lshlrev_b32 v228, 16, v172\n"
    "v_and_b32 v229, s68, v172\n"
    "v_lshlrev_b32 v230, 16, v173\n"
    "v_and_b32 v231, s68, v173\n"
    "v_lshlrev_b32 v232, 16, v174\n"
    "v_and_b32 v233, s68, v174\n"
    "v_lshlrev_b32 v234, 16, v175\n"
    "v_and_b32 v235, s68, v175\n"
    "s_waitcnt lgkmcnt(0)\n"
    "v_lshlrev_b32 v236, 16, v176\n"
    "v_and_b32 v237, s68, v176\n"
    "v_lshlrev_b32 v238, 16, v177\n"
    "v_and_b32 v239, s68, v177\n"
    "v_lshlrev_b32 v240, 16, v178\n"
    "v_and_b32 v241, s68, v178\n"
    "v_lshlrev_b32 v242, 16, v179\n"
    "v_and_b32 v243, s68, v179\n"
    "v_accvgpr_write_b32 a16, v212\n"
    "v_accvgpr_write_b32 a17, v214\n"
    "v_accvgpr_write_b32 a18, v216\n"
    "v_accvgpr_write_b32 a19, v218\n"
    "v_pk_mul_f32 v[220:221], v[220:221], s[76:77]\n"
    "v_pk_mul_f32 v[222:223], v[222:223], s[76:77]\n"
    "v_pk_mul_f32 v[224:225], v[224:225], s[76:77]\n"
    "v_pk_mul_f32 v[226:227], v[226:227], s[76:77]\n"
    "v_pk_mul_f32 v[228:229], v[228:229], s[76:77]\n"
    "v_pk_mul_f32 v[230:231], v[230:231], s[76:77]\n"
    "v_pk_mul_f32 v[232:233], v[232:233], s[76:77]\n"
    "v_pk_mul_f32 v[234:235], v[234:235], s[76:77]\n"
    "v_pk_mul_f32 v[236:237], v[236:237], s[76:77]\n"
    "v_pk_mul_f32 v[238:239], v[238:239], s[76:77]\n"
    "v_pk_mul_f32 v[240:241], v[240:241], s[76:77]\n"
    "v_pk_mul_f32 v[242:243], v[242:243], s[76:77]\n"
    "v_mfma_f32_16x16x32_bf16 v[52:55], a[172:175], a[16:19], 0\n"
    "buffer_load_dwordx4 v131, s[36:39], s64 offen offset:2048 nt lds\n"
    "v_cvt_pk_bf16_f32 v220, v220, v221\n"
    "v_cvt_pk_bf16_f32 v222, v222, v223\n"
    "v_cvt_pk_bf16_f32 v224, v224, v225\n"
    "v_cvt_pk_bf16_f32 v226, v226, v227\n"
    "v_accvgpr_write_b32 a20, v220\n"
    "v_accvgpr_write_b32 a21, v222\n"
    "v_accvgpr_write_b32 a22, v224\n"
    "v_accvgpr_write_b32 a23, v226\n"
    "v_cvt_pk_bf16_f32 v228, v228, v229\n"
    "v_cvt_pk_bf16_f32 v230, v230, v231\n"
    "v_mfma_f32_16x16x32_bf16 v[52:55], a[176:179], a[20:23], v[52:55]\n"
    "v_cvt_pk_bf16_f32 v232, v232, v233\n"
    "v_cvt_pk_bf16_f32 v234, v234, v235\n"
    "v_accvgpr_write_b32 a24, v228\n"
    "v_accvgpr_write_b32 a25, v230\n"
    "v_accvgpr_write_b32 a26, v232\n"
    "v_accvgpr_write_b32 a27, v234\n"
    "v_cvt_pk_bf16_f32 v236, v236, v237\n"
    "v_cvt_pk_bf16_f32 v238, v238, v239\n"
    "v_mfma_f32_16x16x32_bf16 v[52:55], a[180:183], a[24:27], v[52:55]\n"
    "buffer_load_dwordx4 v131, s[36:39], s64 offen offset:3072 nt lds\n"
    "v_cvt_pk_bf16_f32 v240, v240, v241\n"
    "v_cvt_pk_bf16_f32 v242, v242, v243\n"
    "v_accvgpr_write_b32 a28, v236\n"
    "v_accvgpr_write_b32 a29, v238\n"
    "v_accvgpr_write_b32 a30, v240\n"
    "v_accvgpr_write_b32 a31, v242\n"
    "s_nop 1\n"
    "v_mfma_f32_16x16x32_bf16 v[52:55], a[184:187], a[28:31], v[52:55]\n"
    "s_nop 7\n"
    "v_max3_f32 v136, v52, v53, v54\n"
    "v_max_f32 v136, v136, v55\n"
    "v_mov_b32 v137, v136\n"
    "s_nop 1\n"
    "v_permlane32_swap_b32 v137, v136\n"
    "v_max_f32 v136, v136, v137\n"
    "v_mov_b32 v137, v136\n"
    "s_nop 1\n"
    "v_permlane16_swap_b32 v137, v136\n"
    "v_max_f32 v136, v136, v137\n"
    "v_add_f32 v136, 0x41200000, v136\n"
    "v_sub_f32 v108, 0, v136\n"
    "v_sub_f32 v109, 0, v136\n"
    "v_sub_f32 v110, 0, v136\n"
    "v_sub_f32 v111, 0, v136\n"
    "v_sub_f32 v52, v52, v136\n"
    "v_sub_f32 v53, v53, v136\n"
    "v_mfma_f32_16x16x32_bf16 v[68:71], a[188:191], a[16:19], v[108:111]\n"
    "v_sub_f32 v54, v54, v136\n"
    "v_sub_f32 v55, v55, v136\n"
    "v_mfma_f32_16x16x32_bf16 v[72:75], a[204:207], a[16:19], v[108:111]\n"
    "v_exp_f32 v52, v52\n"
    "v_mfma_f32_16x16x32_bf16 v[76:79], a[220:223], a[16:19], v[108:111]\n"
    "v_exp_f32 v53, v53\n"
    "v_mfma_f32_16x16x32_bf16 v[68:71], a[192:195], a[20:23], v[68:71]\n"
    "v_exp_f32 v54, v54\n"
    "v_mfma_f32_16x16x32_bf16 v[72:75], a[208:211], a[20:23], v[72:75]\n"
    "v_exp_f32 v55, v55\n"
    "v_mfma_f32_16x16x32_bf16 v[76:79], a[224:227], a[20:23], v[76:79]\n"
    "v_cvt_pk_bf16_f32 v8, v52, v53\n"
    "v_cvt_pk_bf16_f32 v9, v54, v55\n"
    "v_mfma_f32_16x16x32_bf16 v[68:71], a[196:199], a[24:27], v[68:71]\n"
    "v_mfma_f32_16x16x32_bf16 v[72:75], a[212:215], a[24:27], v[72:75]\n"
    "v_mfma_f32_16x16x32_bf16 v[76:79], a[228:231], a[24:27], v[76:79]\n"
    "v_mfma_f32_16x16x32_bf16 v[68:71], a[200:203], a[28:31], v[68:71]\n"
    "v_mfma_f32_16x16x32_bf16 v[72:75], a[216:219], a[28:31], v[72:75]\n"
    "v_mfma_f32_16x16x32_bf16 v[76:79], a[232:235], a[28:31], v[76:79]\n"
    "s_nop 5\n"
    "v_exp_f32 v68, v68\n"
    "v_exp_f32 v69, v69\n"
    "v_exp_f32 v70, v70\n"
    "v_exp_f32 v71, v71\n"
    "v_exp_f32 v72, v72\n"
    "v_exp_f32 v73, v73\n"
    "v_exp_f32 v74, v74\n"
    "v_exp_f32 v75, v75\n"
    "v_exp_f32 v76, v76\n"
    "v_exp_f32 v77, v77\n"
    "v_exp_f32 v78, v78\n"
    "v_exp_f32 v79, v79\n"
    "v_cvt_pk_bf16_f32 v10, v68, v69\n"
    "v_cvt_pk_bf16_f32 v11, v70, v71\n"
    "v_cvt_pk_bf16_f32 v12, v72, v73\n"
    "v_cvt_pk_bf16_f32 v13, v74, v75\n"
    "v_cvt_pk_bf16_f32 v14, v76, v77\n"
    "v_cvt_pk_bf16_f32 v15, v78, v79\n"
    "s_waitcnt vmcnt(12)\n"
    "ds_read_b128 v[148:151], v134 offset:8192\n"
    "s_add_u32 s64, s89, 0\n"
    "s_and_b32 s64, s64, 262143\n"
    "ds_read_b128 v[152:155], v134 offset:8448\n"
    "s_add_u32 m0, s72, 32768\n"
    "ds_read_b128 v[156:159], v134 offset:8704\n"
    "buffer_load_dwordx4 v131, s[44:47], s64 offen nt lds\n"
    "ds_read_b128 v[160:163], v134 offset:8960\n"
    "buffer_load_dwordx4 v131, s[44:47], s64 offen offset:2048 nt lds\n"
    "s_add_u32 s64, s89, 0\n"
    "s_and_b32 s64, s64, 262143\n"
    "s_add_u32 s64, s64, 8192\n"
    "s_add_u32 m0, s72, 40960\n"
    "s_waitcnt lgkmcnt(3)\n"
    "v_lshlrev_b32 v180, 16, v148\n"
    "v_and_b32 v181, s68, v148\n"
    "v_lshlrev_b32 v182, 16, v149\n"
    "v_and_b32 v183, s68, v149\n"
    "v_lshlrev_b32 v184, 16, v150\n"
    "v_and_b32 v185, s68, v150\n"
    "v_lshlrev_b32 v186, 16, v151\n"
    "v_and_b32 v187, s68, v151\n"
    "v_pk_mul_f32 v[180:181], v[180:181], s[76:77]\n"
    "v_pk_mul_f32 v[182:183], v[182:183], s[76:77]\n"
    "v_pk_mul_f32 v[184:185], v[184:185], s[76:77]\n"
    "v_pk_mul_f32 v[186:187], v[186:187], s[76:77]\n"
    "v_cvt_pk_bf16_f32 v180, v180, v181\n"
    "v_cvt_pk_bf16_f32 v182, v182, v183\n"
    "v_cvt_pk_bf16_f32 v184, v184, v185\n"
    "v_cvt_pk_bf16_f32 v186, v186, v187\n"
    "s_waitcnt lgkmcnt(2)\n"
    "v_lshlrev_b32 v188, 16, v152\n"
    "v_and_b32 v189, s68, v152\n"
    "v_lshlrev_b32 v190, 16, v153\n"
    "v_and_b32 v191, s68, v153\n"
    "v_lshlrev_b32 v192, 16, v154\n"
    "v_and_b32 v193, s68, v154\n"
    "v_lshlrev_b32 v194, 16, v155\n"
    "v_and_b32 v195, s68, v155\n"
    "s_waitcnt lgkmcnt(1)\n"
    "v_lshlrev_b32 v196, 16, v156\n"
    "v_and_b32 v197, s68, v156\n"
    "v_lshlrev_b32 v198, 16, v157\n"
    "v_and_b32 v199, s68, v157\n"
    "v_lshlrev_b32 v200, 16, v158\n"
    "v_and_b32 v201, s68, v158\n"
    "v_lshlrev_b32 v202, 16, v159\n"
    "v_and_b32 v203, s68, v159\n"
    "s_waitcnt lgkmcnt(0)\n"
    "v_lshlrev_b32 v204, 16, v160\n"
    "v_and_b32 v205, s68, v160\n"
    "v_lshlrev_b32 v206, 16, v161\n"
    "v_and_b32 v207, s68, v161\n"
    "v_lshlrev_b32 v208, 16, v162\n"
    "v_and_b32 v209, s68, v162\n"
    "v_lshlrev_b32 v210, 16, v163\n"
    "v_and_b32 v211, s68, v163\n"
    "v_accvgpr_write_b32 a32, v180\n"
    "v_accvgpr_write_b32 a33, v182\n"
    "v_accvgpr_write_b32 a34, v184\n"
    "v_accvgpr_write_b32 a35, v186\n"
    "v_pk_mul_f32 v[188:189], v[188:189], s[76:77]\n"
    "v_pk_mul_f32 v[190:191], v[190:191], s[76:77]\n"
    "v_pk_mul_f32 v[192:193], v[192:193], s[76:77]\n"
    "v_pk_mul_f32 v[194:195], v[194:195], s[76:77]\n"
    "v_pk_mul_f32 v[196:197], v[196:197], s[76:77]\n"
    "v_pk_mul_f32 v[198:199], v[198:199], s[76:77]\n"
    "v_pk_mul_f32 v[200:201], v[200:201], s[76:77]\n"
    "v_pk_mul_f32 v[202:203], v[202:203], s[76:77]\n"
    "v_pk_mul_f32 v[204:205], v[204:205], s[76:77]\n"
    "v_pk_mul_f32 v[206:207], v[206:207], s[76:77]\n"
    "v_pk_mul_f32 v[208:209], v[208:209], s[76:77]\n"
    "v_pk_mul_f32 v[210:211], v[210:211], s[76:77]\n"
    "v_mfma_f32_16x16x32_bf16 v[48:51], a[172:175], a[32:35], 0\n"
    "buffer_load_dwordx4 v131, s[44:47], s64 offen nt lds\n"
    "v_cvt_pk_bf16_f32 v188, v188, v189\n"
    "v_cvt_pk_bf16_f32 v190, v190, v191\n"
    "v_cvt_pk_bf16_f32 v192, v192, v193\n"
    "v_cvt_pk_bf16_f32 v194, v194, v195\n"
    "v_accvgpr_write_b32 a36, v188\n"
    "v_accvgpr_write_b32 a37, v190\n"
    "v_accvgpr_write_b32 a38, v192\n"
    "v_accvgpr_write_b32 a39, v194\n"
    "v_cvt_pk_bf16_f32 v196, v196, v197\n"
    "v_cvt_pk_bf16_f32 v198, v198, v199\n"
    "v_mfma_f32_16x16x32_bf16 v[48:51], a[176:179], a[36:39], v[48:51]\n"
    "v_cvt_pk_bf16_f32 v200, v200, v201\n"
    "v_cvt_pk_bf16_f32 v202, v202, v203\n"
    "v_accvgpr_write_b32 a40, v196\n"
    "v_accvgpr_write_b32 a41, v198\n"
    "v_accvgpr_write_b32 a42, v200\n"
    "v_accvgpr_write_b32 a43, v202\n"
    "v_cvt_pk_bf16_f32 v204, v204, v205\n"
    "v_cvt_pk_bf16_f32 v206, v206, v207\n"
    "v_mfma_f32_16x16x32_bf16 v[48:51], a[180:183], a[40:43], v[48:51]\n"
    "buffer_load_dwordx4 v131, s[44:47], s64 offen offset:2048 nt lds\n"
    "v_cvt_pk_bf16_f32 v208, v208, v209\n"
    "v_cvt_pk_bf16_f32 v210, v210, v211\n"
    "v_accvgpr_write_b32 a44, v204\n"
    "v_accvgpr_write_b32 a45, v206\n"
    "v_accvgpr_write_b32 a46, v208\n"
    "v_accvgpr_write_b32 a47, v210\n"
    "s_nop 1\n"
    "v_mfma_f32_16x16x32_bf16 v[48:51], a[184:187], a[44:47], v[48:51]\n"
    "s_nop 7\n"
    "v_max3_f32 v136, v48, v49, v50\n"
    "v_max_f32 v136, v136, v51\n"
    "v_mov_b32 v137, v136\n"
    "s_nop 1\n"
    "v_permlane32_swap_b32 v137, v136\n"
    "v_max_f32 v136, v136, v137\n"
    "v_mov_b32 v137, v136\n"
    "s_nop 1\n"
    "v_permlane16_swap_b32 v137, v136\n"
    "v_max_f32 v136, v136, v137\n"
    "v_add_f32 v136, 0x41200000, v136\n"
    "v_sub_f32 v112, 0, v136\n"
    "v_sub_f32 v113, 0, v136\n"
    "v_sub_f32 v114, 0, v136\n"
    "v_sub_f32 v115, 0, v136\n"
    "v_sub_f32 v48, v48, v136\n"
    "v_sub_f32 v49, v49, v136\n"
    "v_mfma_f32_16x16x32_bf16 v[80:83], a[188:191], a[32:35], v[112:115]\n"
    "v_sub_f32 v50, v50, v136\n"
    "v_sub_f32 v51, v51, v136\n"
    "v_mfma_f32_16x16x32_bf16 v[84:87], a[204:207], a[32:35], v[112:115]\n"
    "v_exp_f32 v48, v48\n"
    "v_mfma_f32_16x16x32_bf16 v[88:91], a[220:223], a[32:35], v[112:115]\n"
    "v_exp_f32 v49, v49\n"
    "v_mfma_f32_16x16x32_bf16 v[80:83], a[192:195], a[36:39], v[80:83]\n"
    "v_exp_f32 v50, v50\n"
    "v_mfma_f32_16x16x32_bf16 v[84:87], a[208:211], a[36:39], v[84:87]\n"
    "v_exp_f32 v51, v51\n"
    "v_mfma_f32_16x16x32_bf16 v[88:91], a[224:227], a[36:39], v[88:91]\n"
    "v_cvt_pk_bf16_f32 v16, v48, v49\n"
    "v_cvt_pk_bf16_f32 v17, v50, v51\n"
    "v_mfma_f32_16x16x32_bf16 v[80:83], a[196:199], a[40:43], v[80:83]\n"
    "v_mfma_f32_16x16x32_bf16 v[84:87], a[212:215], a[40:43], v[84:87]\n"
    "v_mfma_f32_16x16x32_bf16 v[88:91], a[228:231], a[40:43], v[88:91]\n"
    "v_mfma_f32_16x16x32_bf16 v[80:83], a[200:203], a[44:47], v[80:83]\n"
    "v_mfma_f32_16x16x32_bf16 v[84:87], a[216:219], a[44:47], v[84:87]\n"
    "v_mfma_f32_16x16x32_bf16 v[88:91], a[232:235], a[44:47], v[88:91]\n"
    "s_nop 5\n"
    "v_exp_f32 v80, v80\n"
    "v_exp_f32 v81, v81\n"
    "v_exp_f32 v82, v82\n"
    "v_exp_f32 v83, v83\n"
    "v_exp_f32 v84, v84\n"
    "v_exp_f32 v85, v85\n"
    "v_exp_f32 v86, v86\n"
    "v_exp_f32 v87, v87\n"
    "v_exp_f32 v88, v88\n"
    "v_exp_f32 v89, v89\n"
    "v_exp_f32 v90, v90\n"
    "v_exp_f32 v91, v91\n"
    "v_cvt_pk_bf16_f32 v18, v80, v81\n"
    "v_cvt_pk_bf16_f32 v19, v82, v83\n"
    "v_cvt_pk_bf16_f32 v20, v84, v85\n"
    "v_cvt_pk_bf16_f32 v21, v86, v87\n"
    "v_cvt_pk_bf16_f32 v22, v88, v89\n"
    "v_cvt_pk_bf16_f32 v23, v90, v91\n"
    "s_waitcnt vmcnt(12)\n"
    "ds_read_b128 v[164:167], v134 offset:12288\n"
    "s_add_u32 s64, s89, 16384\n"
    "s_and_b32 s64, s64, 262143\n"
    "ds_read_b128 v[168:171], v134 offset:12544\n"
    "s_add_u32 m0, s72, 16384\n"
    "ds_read_b128 v[172:175], v134 offset:12800\n"
    "buffer_load_dwordx4 v131, s[40:43], s64 offen nt lds\n"
    "ds_read_b128 v[176:179], v134 offset:13056\n"
    "buffer_load_dwordx4 v131, s[40:43], s64 offen offset:2048 nt lds\n"
    "s_add_u32 s64, s89, 16384\n"
    "s_and_b32 s64, s64, 262143\n"
    "s_add_u32 s64, s64, 8192\n"
    "s_add_u32 m0, s72, 24576\n"
    "s_waitcnt lgkmcnt(3)\n"
    "v_lshlrev_b32 v212, 16, v164\n"
    "v_and_b32 v213, s68, v164\n"
    "v_lshlrev_b32 v214, 16, v165\n"
    "v_and_b32 v215, s68, v165\n"
    "v_lshlrev_b32 v216, 16, v166\n"
    "v_and_b32 v217, s68, v166\n"
    "v_lshlrev_b32 v218, 16, v167\n"
    "v_and_b32 v219, s68, v167\n"
    "v_pk_mul_f32 v[212:213], v[212:213], s[76:77]\n"
    "v_pk_mul_f32 v[214:215], v[214:215], s[76:77]\n"
    "v_pk_mul_f32 v[216:217], v[216:217], s[76:77]\n"
    "v_pk_mul_f32 v[218:219], v[218:219], s[76:77]\n"
    "v_cvt_pk_bf16_f32 v212, v212, v213\n"
    "v_cvt_pk_bf16_f32 v214, v214, v215\n"
    "v_cvt_pk_bf16_f32 v216, v216, v217\n"
    "v_cvt_pk_bf16_f32 v218, v218, v219\n"
    "s_waitcnt lgkmcnt(2)\n"
    "v_lshlrev_b32 v220, 16, v168\n"
    "v_and_b32 v221, s68, v168\n"
    "v_lshlrev_b32 v222, 16, v169\n"
    "v_and_b32 v223, s68, v169\n"
    "v_lshlrev_b32 v224, 16, v170\n"
    "v_and_b32 v225, s68, v170\n"
    "v_lshlrev_b32 v226, 16, v171\n"
    "v_and_b32 v227, s68, v171\n"
    "s_waitcnt lgkmcnt(1)\n"
    "v_lshlrev_b32 v228, 16, v172\n"
    "v_and_b32 v229, s68, v172\n"
    "v_lshlrev_b32 v230, 16, v173\n"
    "v_and_b32 v231, s68, v173\n"
    "v_lshlrev_b32 v232, 16, v174\n"
    "v_and_b32 v233, s68, v174\n"
    "v_lshlrev_b32 v234, 16, v175\n"
    "v_and_b32 v235, s68, v175\n"
    "s_waitcnt lgkmcnt(0)\n"
    "v_lshlrev_b32 v236, 16, v176\n"
    "v_and_b32 v237, s68, v176\n"
    "v_lshlrev_b32 v238, 16, v177\n"
    "v_and_b32 v239, s68, v177\n"
    "v_lshlrev_b32 v240, 16, v178\n"
    "v_and_b32 v241, s68, v178\n"
    "v_lshlrev_b32 v242, 16, v179\n"
    "v_and_b32 v243, s68, v179\n"
    "v_accvgpr_write_b32 a48, v212\n"
    "v_accvgpr_write_b32 a49, v214\n"
    "v_accvgpr_write_b32 a50, v216\n"
    "v_accvgpr_write_b32 a51, v218\n"
    "v_pk_mul_f32 v[220:221], v[220:221], s[76:77]\n"
    "v_pk_mul_f32 v[222:223], v[222:223], s[76:77]\n"
    "v_pk_mul_f32 v[224:225], v[224:225], s[76:77]\n"
    "v_pk_mul_f32 v[226:227], v[226:227], s[76:77]\n"
    "v_pk_mul_f32 v[228:229], v[228:229], s[76:77]\n"
    "v_pk_mul_f32 v[230:231], v[230:231], s[76:77]\n"
    "v_pk_mul_f32 v[232:233], v[232:233], s[76:77]\n"
    "v_pk_mul_f32 v[234:235], v[234:235], s[76:77]\n"
    "v_pk_mul_f32 v[236:237], v[236:237], s[76:77]\n"
    "v_pk_mul_f32 v[238:239], v[238:239], s[76:77]\n"
    "v_pk_mul_f32 v[240:241], v[240:241], s[76:77]\n"
    "v_pk_mul_f32 v[242:243], v[242:243], s[76:77]\n"
    "v_mfma_f32_16x16x32_bf16 v[52:55], a[172:175], a[48:51], 0\n"
    "buffer_load_dwordx4 v131, s[40:43], s64 offen nt lds\n"
    "v_cvt_pk_bf16_f32 v220, v220, v221\n"
    "v_cvt_pk_bf16_f32 v222, v222, v223\n"
    "v_cvt_pk_bf16_f32 v224, v224, v225\n"
    "v_cvt_pk_bf16_f32 v226, v226, v227\n"
    "v_accvgpr_write_b32 a52, v220\n"
    "v_accvgpr_write_b32 a53, v222\n"
    "v_accvgpr_write_b32 a54, v224\n"
    "v_accvgpr_write_b32 a55, v226\n"
    "v_cvt_pk_bf16_f32 v228, v228, v229\n"
    "v_cvt_pk_bf16_f32 v230, v230, v231\n"
    "v_mfma_f32_16x16x32_bf16 v[52:55], a[176:179], a[52:55], v[52:55]\n"
    "v_cvt_pk_bf16_f32 v232, v232, v233\n"
    "v_cvt_pk_bf16_f32 v234, v234, v235\n"
    "v_accvgpr_write_b32 a56, v228\n"
    "v_accvgpr_write_b32 a57, v230\n"
    "v_accvgpr_write_b32 a58, v232\n"
    "v_accvgpr_write_b32 a59, v234\n"
    "v_cvt_pk_bf16_f32 v236, v236, v237\n"
    "v_cvt_pk_bf16_f32 v238, v238, v239\n"
    "v_mfma_f32_16x16x32_bf16 v[52:55], a[180:183], a[56:59], v[52:55]\n"
    "buffer_load_dwordx4 v131, s[40:43], s64 offen offset:2048 nt lds\n"
    "v_cvt_pk_bf16_f32 v240, v240, v241\n"
    "v_cvt_pk_bf16_f32 v242, v242, v243\n"
    "v_accvgpr_write_b32 a60, v236\n"
    "v_accvgpr_write_b32 a61, v238\n"
    "v_accvgpr_write_b32 a62, v240\n"
    "v_accvgpr_write_b32 a63, v242\n"
    "s_nop 1\n"
    "v_mfma_f32_16x16x32_bf16 v[52:55], a[184:187], a[60:63], v[52:55]\n"
    "s_nop 7\n"
    "v_max3_f32 v136, v52, v53, v54\n"
    "v_max_f32 v136, v136, v55\n"
    "v_mov_b32 v137, v136\n"
    "s_nop 1\n"
    "v_permlane32_swap_b32 v137, v136\n"
    "v_max_f32 v136, v136, v137\n"
    "v_mov_b32 v137, v136\n"
    "s_nop 1\n"
    "v_permlane16_swap_b32 v137, v136\n"
    "v_max_f32 v136, v136, v137\n"
    "v_add_f32 v136, 0x41200000, v136\n"
    "v_sub_f32 v116, 0, v136\n"
    "v_sub_f32 v117, 0, v136\n"
    "v_sub_f32 v118, 0, v136\n"
    "v_sub_f32 v119, 0, v136\n"
    "v_sub_f32 v52, v52, v136\n"
    "v_sub_f32 v53, v53, v136\n"
    "v_mfma_f32_16x16x32_bf16 v[56:59], a[188:191], a[48:51], v[116:119]\n"
    "v_sub_f32 v54, v54, v136\n"
    "v_sub_f32 v55, v55, v136\n"
    "v_mfma_f32_16x16x32_bf16 v[60:63], a[204:207], a[48:51], v[116:119]\n"
    "v_exp_f32 v52, v52\n"
    "v_mfma_f32_16x16x32_bf16 v[64:67], a[220:223], a[48:51], v[116:119]\n"
    "v_exp_f32 v53, v53\n"
    "v_mfma_f32_16x16x32_bf16 v[56:59], a[192:195], a[52:55], v[56:59]\n"
    "v_exp_f32 v54, v54\n"
    "v_mfma_f32_16x16x32_bf16 v[60:63], a[208:211], a[52:55], v[60:63]\n"
    "v_exp_f32 v55, v55\n"
    "v_mfma_f32_16x16x32_bf16 v[64:67], a[224:227], a[52:55], v[64:67]\n"
    "v_cvt_pk_bf16_f32 v24, v52, v53\n"
    "v_cvt_pk_bf16_f32 v25, v54, v55\n"
    "v_mfma_f32_16x16x32_bf16 v[56:59], a[196:199], a[56:59], v[56:59]\n"
    "v_mfma_f32_16x16x32_bf16 v[60:63], a[212:215], a[56:59], v[60:63]\n"
    "v_mfma_f32_16x16x32_bf16 v[64:67], a[228:231], a[56:59], v[64:67]\n"
    "v_mfma_f32_16x16x32_bf16 v[56:59], a[200:203], a[60:63], v[56:59]\n"
    "v_mfma_f32_16x16x32_bf16 v[60:63], a[216:219], a[60:63], v[60:63]\n"
    "v_mfma_f32_16x16x32_bf16 v[64:67], a[232:235], a[60:63], v[64:67]\n"
    "s_nop 5\n"
    "v_exp_f32 v56, v56\n"
    "v_exp_f32 v57, v57\n"
    "v_exp_f32 v58, v58\n"
    "v_exp_f32 v59, v59\n"
    "v_exp_f32 v60, v60\n"
    "v_exp_f32 v61, v61\n"
    "v_exp_f32 v62, v62\n"
    "v_exp_f32 v63, v63\n"
    "v_exp_f32 v64, v64\n"
    "v_exp_f32 v65, v65\n"
    "v_exp_f32 v66, v66\n"
    "v_exp_f32 v67, v67\n"
    "v_cvt_pk_bf16_f32 v26, v56, v57\n"
    "v_cvt_pk_bf16_f32 v27, v58, v59\n"
    "v_cvt_pk_bf16_f32 v28, v60, v61\n"
    "v_cvt_pk_bf16_f32 v29, v62, v63\n"
    "v_cvt_pk_bf16_f32 v30, v64, v65\n"
    "v_cvt_pk_bf16_f32 v31, v66, v67\n"
    "s_waitcnt vmcnt(12)\n"
    "ds_read_b128 v[148:151], v134 offset:16384\n"
    "s_add_u32 s64, s89, 16384\n"
    "s_and_b32 s64, s64, 262143\n"
    "ds_read_b128 v[152:155], v134 offset:16640\n"
    "s_add_u32 m0, s72, 49152\n"
    "ds_read_b128 v[156:159], v134 offset:16896\n"
    "buffer_load_dwordx4 v131, s[44:47], s64 offen nt lds\n"
    "ds_read_b128 v[160:163], v134 offset:17152\n"
    "buffer_load_dwordx4 v131, s[44:47], s64 offen offset:2048 nt lds\n"
    "s_add_u32 s64, s89, 16384\n"
    "s_and_b32 s64, s64, 262143\n"
    "s_add_u32 s64, s64, 8192\n"
    "s_add_u32 m0, s72, 57344\n"
    "s_waitcnt lgkmcnt(3)\n"
    "v_lshlrev_b32 v180, 16, v148\n"
    "v_and_b32 v181, s68, v148\n"
    "v_lshlrev_b32 v182, 16, v149\n"
    "v_and_b32 v183, s68, v149\n"
    "v_lshlrev_b32 v184, 16, v150\n"
    "v_and_b32 v185, s68, v150\n"
    "v_lshlrev_b32 v186, 16, v151\n"
    "v_and_b32 v187, s68, v151\n"
    "v_pk_mul_f32 v[180:181], v[180:181], s[76:77]\n"
    "v_pk_mul_f32 v[182:183], v[182:183], s[76:77]\n"
    "v_pk_mul_f32 v[184:185], v[184:185], s[76:77]\n"
    "v_pk_mul_f32 v[186:187], v[186:187], s[76:77]\n"
    "v_cvt_pk_bf16_f32 v180, v180, v181\n"
    "v_cvt_pk_bf16_f32 v182, v182, v183\n"
    "v_cvt_pk_bf16_f32 v184, v184, v185\n"
    "v_cvt_pk_bf16_f32 v186, v186, v187\n"
    "s_waitcnt lgkmcnt(2)\n"
    "v_lshlrev_b32 v188, 16, v152\n"
    "v_and_b32 v189, s68, v152\n"
    "v_lshlrev_b32 v190, 16, v153\n"
    "v_and_b32 v191, s68, v153\n"
    "v_lshlrev_b32 v192, 16, v154\n"
    "v_and_b32 v193, s68, v154\n"
    "v_lshlrev_b32 v194, 16, v155\n"
    "v_and_b32 v195, s68, v155\n"
    "s_waitcnt lgkmcnt(1)\n"
    "v_lshlrev_b32 v196, 16, v156\n"
    "v_and_b32 v197, s68, v156\n"
    "v_lshlrev_b32 v198, 16, v157\n"
    "v_and_b32 v199, s68, v157\n"
    "v_lshlrev_b32 v200, 16, v158\n"
    "v_and_b32 v201, s68, v158\n"
    "v_lshlrev_b32 v202, 16, v159\n"
    "v_and_b32 v203, s68, v159\n"
    "s_waitcnt lgkmcnt(0)\n"
    "v_lshlrev_b32 v204, 16, v160\n"
    "v_and_b32 v205, s68, v160\n"
    "v_lshlrev_b32 v206, 16, v161\n"
    "v_and_b32 v207, s68, v161\n"
    "v_lshlrev_b32 v208, 16, v162\n"
    "v_and_b32 v209, s68, v162\n"
    "v_lshlrev_b32 v210, 16, v163\n"
    "v_and_b32 v211, s68, v163\n"
    "v_accvgpr_write_b32 a64, v180\n"
    "v_accvgpr_write_b32 a65, v182\n"
    "v_accvgpr_write_b32 a66, v184\n"
    "v_accvgpr_write_b32 a67, v186\n"
    "v_pk_mul_f32 v[188:189], v[188:189], s[76:77]\n"
    "v_pk_mul_f32 v[190:191], v[190:191], s[76:77]\n"
    "v_pk_mul_f32 v[192:193], v[192:193], s[76:77]\n"
    "v_pk_mul_f32 v[194:195], v[194:195], s[76:77]\n"
    "v_pk_mul_f32 v[196:197], v[196:197], s[76:77]\n"
    "v_pk_mul_f32 v[198:199], v[198:199], s[76:77]\n"
    "v_pk_mul_f32 v[200:201], v[200:201], s[76:77]\n"
    "v_pk_mul_f32 v[202:203], v[202:203], s[76:77]\n"
    "v_pk_mul_f32 v[204:205], v[204:205], s[76:77]\n"
    "v_pk_mul_f32 v[206:207], v[206:207], s[76:77]\n"
    "v_pk_mul_f32 v[208:209], v[208:209], s[76:77]\n"
    "v_pk_mul_f32 v[210:211], v[210:211], s[76:77]\n"
    "v_mfma_f32_16x16x32_bf16 v[48:51], a[172:175], a[64:67], 0\n"
    "buffer_load_dwordx4 v131, s[44:47], s64 offen nt lds\n"
    "v_cvt_pk_bf16_f32 v188, v188, v189\n"
    "v_cvt_pk_bf16_f32 v190, v190, v191\n"
    "v_cvt_pk_bf16_f32 v192, v192, v193\n"
    "v_cvt_pk_bf16_f32 v194, v194, v195\n"
    "v_accvgpr_write_b32 a68, v188\n"
    "v_accvgpr_write_b32 a69, v190\n"
    "v_accvgpr_write_b32 a70, v192\n"
    "v_accvgpr_write_b32 a71, v194\n"
    "v_cvt_pk_bf16_f32 v196, v196, v197\n"
    "v_cvt_pk_bf16_f32 v198, v198, v199\n"
    "v_mfma_f32_16x16x32_bf16 v[48:51], a[176:179], a[68:71], v[48:51]\n"
    "v_cvt_pk_bf16_f32 v200, v200, v201\n"
    "v_cvt_pk_bf16_f32 v202, v202, v203\n"
    "v_accvgpr_write_b32 a72, v196\n"
    "v_accvgpr_write_b32 a73, v198\n"
    "v_accvgpr_write_b32 a74, v200\n"
    "v_accvgpr_write_b32 a75, v202\n"
    "v_cvt_pk_bf16_f32 v204, v204, v205\n"
    "v_cvt_pk_bf16_f32 v206, v206, v207\n"
    "v_mfma_f32_16x16x32_bf16 v[48:51], a[180:183], a[72:75], v[48:51]\n"
    "buffer_load_dwordx4 v131, s[44:47], s64 offen offset:2048 nt lds\n"
    "v_cvt_pk_bf16_f32 v208, v208, v209\n"
    "v_cvt_pk_bf16_f32 v210, v210, v211\n"
    "v_accvgpr_write_b32 a76, v204\n"
    "v_accvgpr_write_b32 a77, v206\n"
    "v_accvgpr_write_b32 a78, v208\n"
    "v_accvgpr_write_b32 a79, v210\n"
    "s_nop 1\n"
    "v_mfma_f32_16x16x32_bf16 v[48:51], a[184:187], a[76:79], v[48:51]\n"
    "s_nop 7\n"
    "v_max3_f32 v136, v48, v49, v50\n"
    "v_max_f32 v136, v136, v51\n"
    "v_mov_b32 v137, v136\n"
    "s_nop 1\n"
    "v_permlane32_swap_b32 v137, v136\n"
    "v_max_f32 v136, v136, v137\n"
    "v_mov_b32 v137, v136\n"
    "s_nop 1\n"
    "v_permlane16_swap_b32 v137, v136\n"
    "v_max_f32 v136, v136, v137\n"
    "v_add_f32 v136, 0x41200000, v136\n"
    "v_sub_f32 v120, 0, v136\n"
    "v_sub_f32 v121, 0, v136\n"
    "v_sub_f32 v122, 0, v136\n"
    "v_sub_f32 v123, 0, v136\n"
    "v_sub_f32 v48, v48, v136\n"
    "v_sub_f32 v49, v49, v136\n"
    "v_mfma_f32_16x16x32_bf16 v[68:71], a[188:191], a[64:67], v[120:123]\n"
    "v_sub_f32 v50, v50, v136\n"
    "v_sub_f32 v51, v51, v136\n"
    "v_mfma_f32_16x16x32_bf16 v[72:75], a[204:207], a[64:67], v[120:123]\n"
    "v_exp_f32 v48, v48\n"
    "v_mfma_f32_16x16x32_bf16 v[76:79], a[220:223], a[64:67], v[120:123]\n"
    "v_exp_f32 v49, v49\n"
    "v_mfma_f32_16x16x32_bf16 v[68:71], a[192:195], a[68:71], v[68:71]\n"
    "v_exp_f32 v50, v50\n"
    "v_mfma_f32_16x16x32_bf16 v[72:75], a[208:211], a[68:71], v[72:75]\n"
    "v_exp_f32 v51, v51\n"
    "v_mfma_f32_16x16x32_bf16 v[76:79], a[224:227], a[68:71], v[76:79]\n"
    "v_cvt_pk_bf16_f32 v32, v48, v49\n"
    "v_cvt_pk_bf16_f32 v33, v50, v51\n"
    "v_mfma_f32_16x16x32_bf16 v[68:71], a[196:199], a[72:75], v[68:71]\n"
    "v_mfma_f32_16x16x32_bf16 v[72:75], a[212:215], a[72:75], v[72:75]\n"
    "v_mfma_f32_16x16x32_bf16 v[76:79], a[228:231], a[72:75], v[76:79]\n"
    "v_mfma_f32_16x16x32_bf16 v[68:71], a[200:203], a[76:79], v[68:71]\n"
    "v_mfma_f32_16x16x32_bf16 v[72:75], a[216:219], a[76:79], v[72:75]\n"
    "v_mfma_f32_16x16x32_bf16 v[76:79], a[232:235], a[76:79], v[76:79]\n"
    "s_nop 5\n"
    "v_exp_f32 v68, v68\n"
    "v_exp_f32 v69, v69\n"
    "v_exp_f32 v70, v70\n"
    "v_exp_f32 v71, v71\n"
    "v_exp_f32 v72, v72\n"
    "v_exp_f32 v73, v73\n"
    "v_exp_f32 v74, v74\n"
    "v_exp_f32 v75, v75\n"
    "v_exp_f32 v76, v76\n"
    "v_exp_f32 v77, v77\n"
    "v_exp_f32 v78, v78\n"
    "v_exp_f32 v79, v79\n"
    "v_cvt_pk_bf16_f32 v34, v68, v69\n"
    "v_cvt_pk_bf16_f32 v35, v70, v71\n"
    "v_cvt_pk_bf16_f32 v36, v72, v73\n"
    "v_cvt_pk_bf16_f32 v37, v74, v75\n"
    "v_cvt_pk_bf16_f32 v38, v76, v77\n"
    "v_cvt_pk_bf16_f32 v39, v78, v79\n"
    "s_waitcnt vmcnt(12)\n"
    "ds_read_b128 v[164:167], v134 offset:20480\n"
    "ds_read_b128 v[168:171], v134 offset:20736\n"
    "ds_read_b128 v[172:175], v134 offset:20992\n"
    "ds_read_b128 v[176:179], v134 offset:21248\n"
    "s_waitcnt lgkmcnt(3)\n"
    "v_lshlrev_b32 v212, 16, v164\n"
    "v_and_b32 v213, s68, v164\n"
    "v_lshlrev_b32 v214, 16, v165\n"
    "v_and_b32 v215, s68, v165\n"
    "v_lshlrev_b32 v216, 16, v166\n"
    "v_and_b32 v217, s68, v166\n"
    "v_lshlrev_b32 v218, 16, v167\n"
    "v_and_b32 v219, s68, v167\n"
    "v_pk_mul_f32 v[212:213], v[212:213], s[76:77]\n"
    "v_pk_mul_f32 v[214:215], v[214:215], s[76:77]\n"
    "v_pk_mul_f32 v[216:217], v[216:217], s[76:77]\n"
    "v_pk_mul_f32 v[218:219], v[218:219], s[76:77]\n"
    "v_cvt_pk_bf16_f32 v212, v212, v213\n"
    "v_cvt_pk_bf16_f32 v214, v214, v215\n"
    "v_cvt_pk_bf16_f32 v216, v216, v217\n"
    "v_cvt_pk_bf16_f32 v218, v218, v219\n"
    "s_waitcnt lgkmcnt(2)\n"
    "v_lshlrev_b32 v220, 16, v168\n"
    "v_and_b32 v221, s68, v168\n"
    "v_lshlrev_b32 v222, 16, v169\n"
    "v_and_b32 v223, s68, v169\n"
    "v_lshlrev_b32 v224, 16, v170\n"
    "v_and_b32 v225, s68, v170\n"
    "v_lshlrev_b32 v226, 16, v171\n"
    "v_and_b32 v227, s68, v171\n"
    "s_waitcnt lgkmcnt(1)\n"
    "v_lshlrev_b32 v228, 16, v172\n"
    "v_and_b32 v229, s68, v172\n"
    "v_lshlrev_b32 v230, 16, v173\n"
    "v_and_b32 v231, s68, v173\n"
    "v_lshlrev_b32 v232, 16, v174\n"
    "v_and_b32 v233, s68, v174\n"
    "v_lshlrev_b32 v234, 16, v175\n"
    "v_and_b32 v235, s68, v175\n"
    "s_waitcnt lgkmcnt(0)\n"
    "v_lshlrev_b32 v236, 16, v176\n"
    "v_and_b32 v237, s68, v176\n"
    "v_lshlrev_b32 v238, 16, v177\n"
    "v_and_b32 v239, s68, v177\n"
    "v_lshlrev_b32 v240, 16, v178\n"
    "v_and_b32 v241, s68, v178\n"
    "v_lshlrev_b32 v242, 16, v179\n"
    "v_and_b32 v243, s68, v179\n"
    "v_accvgpr_write_b32 a80, v212\n"
    "v_accvgpr_write_b32 a81, v214\n"
    "v_accvgpr_write_b32 a82, v216\n"
    "v_accvgpr_write_b32 a83, v218\n"
    "v_pk_mul_f32 v[220:221], v[220:221], s[76:77]\n"
    "v_pk_mul_f32 v[222:223], v[222:223], s[76:77]\n"
    "v_pk_mul_f32 v[224:225], v[224:225], s[76:77]\n"
    "v_pk_mul_f32 v[226:227], v[226:227], s[76:77]\n"
    "v_pk_mul_f32 v[228:229], v[228:229], s[76:77]\n"
    "v_pk_mul_f32 v[230:231], v[230:231], s[76:77]\n"
    "v_pk_mul_f32 v[232:233], v[232:233], s[76:77]\n"
    "v_pk_mul_f32 v[234:235], v[234:235], s[76:77]\n"
    "v_pk_mul_f32 v[236:237], v[236:237], s[76:77]\n"
    "v_pk_mul_f32 v[238:239], v[238:239], s[76:77]\n"
    "v_pk_mul_f32 v[240:241], v[240:241], s[76:77]\n"
    "v_pk_mul_f32 v[242:243], v[242:243], s[76:77]\n"
    "v_mfma_f32_16x16x32_bf16 v[52:55], a[172:175], a[80:83], 0\n"
    "v_cvt_pk_bf16_f32 v220, v220, v221\n"
    "v_cvt_pk_bf16_f32 v222, v222, v223\n"
    "v_cvt_pk_bf16_f32 v224, v224, v225\n"
    "v_cvt_pk_bf16_f32 v226, v226, v227\n"
    "v_accvgpr_write_b32 a84, v220\n"
    "v_accvgpr_write_b32 a85, v222\n"
    "v_accvgpr_write_b32 a86, v224\n"
    "v_accvgpr_write_b32 a87, v226\n"
    "v_cvt_pk_bf16_f32 v228, v228, v229\n"
    "v_cvt_pk_bf16_f32 v230, v230, v231\n"
    "v_mfma_f32_16x16x32_bf16 v[52:55], a[176:179], a[84:87], v[52:55]\n"
    "v_cvt_pk_bf16_f32 v232, v232, v233\n"
    "v_cvt_pk_bf16_f32 v234, v234, v235\n"
    "v_accvgpr_write_b32 a88, v228\n"
    "v_accvgpr_write_b32 a89, v230\n"
    "v_accvgpr_write_b32 a90, v232\n"
    "v_accvgpr_write_b32 a91, v234\n"
    "v_cvt_pk_bf16_f32 v236, v236, v237\n"
    "v_cvt_pk_bf16_f32 v238, v238, v239\n"
    "v_mfma_f32_16x16x32_bf16 v[52:55], a[180:183], a[88:91], v[52:55]\n"
    "v_cvt_pk_bf16_f32 v240, v240, v241\n"
    "v_cvt_pk_bf16_f32 v242, v242, v243\n"
    "v_accvgpr_write_b32 a92, v236\n"
    "v_accvgpr_write_b32 a93, v238\n"
    "v_accvgpr_write_b32 a94, v240\n"
    "v_accvgpr_write_b32 a95, v242\n"
    "s_nop 1\n"
    "v_mfma_f32_16x16x32_bf16 v[52:55], a[184:187], a[92:95], v[52:55]\n"
    "s_nop 7\n"
    "v_max3_f32 v136, v52, v53, v54\n"
    "v_max_f32 v136, v136, v55\n"
    "v_mov_b32 v137, v136\n"
    "s_nop 1\n"
    "v_permlane32_swap_b32 v137, v136\n"
    "v_max_f32 v136, v136, v137\n"
    "v_mov_b32 v137, v136\n"
    "s_nop 1\n"
    "v_permlane16_swap_b32 v137, v136\n"
    "v_max_f32 v136, v136, v137\n"
    "v_add_f32 v136, 0x41200000, v136\n"
    "v_sub_f32 v124, 0, v136\n"
    "v_sub_f32 v125, 0, v136\n"
    "v_sub_f32 v126, 0, v136\n"
    "v_sub_f32 v127, 0, v136\n"
    "v_sub_f32 v52, v52, v136\n"
    "v_sub_f32 v53, v53, v136\n"
    "v_mfma_f32_16x16x32_bf16 v[80:83], a[188:191], a[80:83], v[124:127]\n"
    "v_sub_f32 v54, v54, v136\n"
    "v_sub_f32 v55, v55, v136\n"
    "v_mfma_f32_16x16x32_bf16 v[84:87], a[204:207], a[80:83], v[124:127]\n"
    "v_exp_f32 v52, v52\n"
    "v_mfma_f32_16x16x32_bf16 v[100:103], a[220:223], a[80:83], v[124:127]\n"
    "v_exp_f32 v53, v53\n"
    "v_mfma_f32_16x16x32_bf16 v[80:83], a[192:195], a[84:87], v[80:83]\n"
    "v_exp_f32 v54, v54\n"
    "v_mfma_f32_16x16x32_bf16 v[84:87], a[208:211], a[84:87], v[84:87]\n"
    "v_exp_f32 v55, v55\n"
    "v_mfma_f32_16x16x32_bf16 v[100:103], a[224:227], a[84:87], v[100:103]\n"
    "v_cvt_pk_bf16_f32 v40, v52, v53\n"
    "v_cvt_pk_bf16_f32 v41, v54, v55\n"
    "v_mfma_f32_16x16x32_bf16 v[80:83], a[196:199], a[88:91], v[80:83]\n"
    "v_mfma_f32_16x16x32_bf16 v[84:87], a[212:215], a[88:91], v[84:87]\n"
    "v_mfma_f32_16x16x32_bf16 v[100:103], a[228:231], a[88:91], v[100:103]\n"
    "v_mfma_f32_16x16x32_bf16 v[80:83], a[200:203], a[92:95], v[80:83]\n"
    "v_mfma_f32_16x16x32_bf16 v[84:87], a[216:219], a[92:95], v[84:87]\n"
    "v_mfma_f32_16x16x32_bf16 v[100:103], a[232:235], a[92:95], v[100:103]\n"
    "s_waitcnt vmcnt(6)\n"
    "s_barrier\n"
    "ds_read_b128 a[96:99], v128 offset:16384\n"
    "ds_read_b128 a[100:103], v128 offset:16640\n"
    "ds_read_b128 a[104:107], v128 offset:16896\n"
    "ds_read_b128 a[108:111], v128 offset:17152\n"
    "ds_read_b64_tr_b16 a[128:129], v129 offset:32768\n"
    "ds_read_b64_tr_b16 a[130:131], v129 offset:36864\n"
    "ds_read_b64_tr_b16 a[132:133], v129 offset:40960\n"
    "ds_read_b64_tr_b16 a[134:135], v129 offset:45056\n"
    "v_mfma_f32_16x16x32_bf16 v[92:95], a[168:171], a[168:171], 0\n"
    "v_exp_f32 v80, v80\n"
    "s_add_u32 s64, s89, 32768\n"
    "s_and_b32 s64, s64, 262143\n"
    "v_exp_f32 v81, v81\n"
    "s_mov_b32 m0, s72\n"
    "s_nop 0\n"
    "buffer_load_dwordx4 v131, s[40:43], s64 offen nt lds\n"
    "v_exp_f32 v82, v82\n"
    "buffer_load_dwordx4 v131, s[40:43], s64 offen offset:2048 nt lds\n"
    "v_cvt_pk_bf16_f32 v42, v80, v81\n"
    "s_add_u32 s64, s89, 32768\n"
    "s_and_b32 s64, s64, 262143\n"
    "v_exp_f32 v83, v83\n"
    "s_add_u32 s64, s64, 8192\n"
    "v_cvt_pk_bf16_f32 v43, v82, v83\n"
    "s_add_u32 m0, s72, 8192\n"
    "s_nop 0\n"
    "buffer_load_dwordx4 v131, s[40:43], s64 offen nt lds\n"
    "v_exp_f32 v84, v84\n"
    "buffer_load_dwordx4 v131, s[40:43], s64 offen offset:2048 nt lds\n"
    "s_add_u32 s56, s89, 32768\n"
    "v_exp_f32 v85, v85\n"
    "s_and_b32 s56, s56, 262143\n"
    "v_exp_f32 v86, v86\n"
    "s_add_u32 s57, s89, 16384\n"
    "s_and_b32 s57, s57, 262143\n"
    "v_cvt_pk_bf16_f32 v44, v84, v85\n"
    "s_mov_b32 s61, 0\n"
    "v_exp_f32 v87, v87\n"
    "s_mov_b32 s96, 0\n"
    "s_mov_b32 s58, 25247744\n"
    "v_cvt_pk_bf16_f32 v45, v86, v87\n"
    "s_add_u32 s63, s62, 20480\n"
    "s_waitcnt lgkmcnt(4)\n"
    "v_mfma_f32_16x16x32_bf16 v[96:99], a[96:99], a[0:3], v[104:107]\n"
    "v_exp_f32 v100, v100\n"
    "v_mfma_f32_16x16x32_bf16 v[96:99], a[100:103], a[4:7], v[96:99]\n"
    "v_mfma_f32_16x16x32_bf16 v[96:99], a[104:107], a[8:11], v[96:99]\n"
    "v_exp_f32 v101, v101\n"
    "v_mfma_f32_16x16x32_bf16 v[96:99], a[108:111], a[12:15], v[96:99]\n"
    "s_waitcnt lgkmcnt(0)\n"
    "v_mfma_f32_16x16x32_bf16 v[148:151], a[128:131], v[0:3], 0\n"
    "v_exp_f32 v102, v102\n"
    "v_mfma_f32_16x16x32_bf16 v[148:151], a[132:135], v[4:7], v[148:151]\n"
    "ds_read_b128 a[112:115], v128 offset:20480\n"
    "v_cvt_pk_bf16_f32 v46, v100, v101\n"
    "v_mfma_f32_16x16x32_bf16 v[152:155], a[132:135], v[12:15], 0\n"
    "v_exp_f32 v103, v103\n"
    "v_mfma_f32_16x16x32_bf16 v[152:155], a[128:131], v[8:11], v[152:155]\n"
    "ds_read_b128 a[116:119], v128 offset:20736\n"
    "v_cvt_pk_bf16_f32 v47, v102, v103\n"
    "v_mfma_f32_16x16x32_bf16 v[100:103], a[96:99], a[16:19], v[108:111]\n"
    "v_exp_f32 v96, v96\n"
    "v_mfma_f32_16x16x32_bf16 v[100:103], a[100:103], a[20:23], v[100:103]\n"
    "ds_read_b64_tr_b16 a[136:137], v130 offset:32768\n"
    "v_mfma_f32_16x16x32_bf16 v[100:103], a[104:107], a[24:27], v[100:103]\n"
    "v_exp_f32 v97, v97\n"
    "v_mfma_f32_16x16x32_bf16 v[100:103], a[108:111], a[28:31], v[100:103]\n"
    "ds_read_b64_tr_b16 a[138:139], v130 offset:36864\n"
    "v_mfma_f32_16x16x32_bf16 v[156:159], a[128:131], v[16:19], 0\n"
    "v_exp_f32 v98, v98\n"
    "v_mfma_f32_16x16x32_bf16 v[156:159], a[132:135], v[20:23], v[156:159]\n"
    "v_cvt_pk_bf16_f32 v48, v96, v97\n"
    "v_mfma_f32_16x16x32_bf16 v[160:163], a[132:135], v[28:31], 0\n"
    "v_exp_f32 v99, v99\n"
    "v_mfma_f32_16x16x32_bf16 v[160:163], a[128:131], v[24:27], v[160:163]\n"
    "v_cvt_pk_bf16_f32 v49, v98, v99\n"
    "v_mfma_f32_16x16x32_bf16 v[96:99], a[96:99], a[32:35], v[112:115]\n"
    "v_exp_f32 v100, v100\n"
    "v_mfma_f32_16x16x32_bf16 v[96:99], a[100:103], a[36:39], v[96:99]\n"
    "ds_read_b64_tr_b16 a[140:141], v130 offset:40960\n"
    "v_mfma_f32_16x16x32_bf16 v[96:99], a[104:107], a[40:43], v[96:99]\n"
    "v_exp_f32 v101, v101\n"
    "v_mfma_f32_16x16x32_bf16 v[96:99], a[108:111], a[44:47], v[96:99]\n"
    "ds_read_b64_tr_b16 a[142:143], v130 offset:45056\n"
    "v_mfma_f32_16x16x32_bf16 v[164:167], a[128:131], v[32:35], 0\n"
    "v_exp_f32 v102, v102\n"
    "v_mfma_f32_16x16x32_bf16 v[164:167], a[132:135], v[36:39], v[164:167]\n"
    "v_cvt_pk_bf16_f32 v56, v100, v101\n"
    "v_mfma_f32_16x16x32_bf16 v[168:171], a[132:135], v[44:47], 0\n"
    "v_exp_f32 v103, v103\n"
    "v_mfma_f32_16x16x32_bf16 v[168:171], a[128:131], v[40:43], v[168:171]\n"
    "v_cvt_pk_bf16_f32 v57, v102, v103\n"
    "v_mfma_f32_16x16x32_bf16 v[100:103], a[96:99], a[48:51], v[116:119]\n"
    "v_exp_f32 v96, v96\n"
    "v_mfma_f32_16x16x32_bf16 v[100:103], a[100:103], a[52:55], v[100:103]\n"
    "v_mfma_f32_16x16x32_bf16 v[100:103], a[104:107], a[56:59], v[100:103]\n"
    "v_exp_f32 v97, v97\n"
    "v_mfma_f32_16x16x32_bf16 v[100:103], a[108:111], a[60:63], v[100:103]\n"
    "s_waitcnt lgkmcnt(0)\n"
    "v_mfma_f32_16x16x32_bf16 v[172:175], a[136:139], v[0:3], 0\n"
    "v_exp_f32 v98, v98\n"
    "v_mfma_f32_16x16x32_bf16 v[172:175], a[140:143], v[4:7], v[172:175]\n"
    "ds_read_b128 a[120:123], v128 offset:20992\n"
    "v_cvt_pk_bf16_f32 v64, v96, v97\n"
    "v_mfma_f32_16x16x32_bf16 v[176:179], a[140:143], v[12:15], 0\n"
    "v_exp_f32 v99, v99\n"
    "v_mfma_f32_16x16x32_bf16 v[176:179], a[136:139], v[8:11], v[176:179]\n"
    "ds_read_b128 a[124:127], v128 offset:21248\n"
    "v_cvt_pk_bf16_f32 v65, v98, v99\n"
    "v_smfmac_f32_16x16x64_bf16 a[144:147], a[168:171], v[0:7], v133\n"
    "v_mfma_f32_16x16x32_bf16 v[96:99], a[96:99], a[64:67], v[120:123]\n"
    "v_exp_f32 v100, v100\n"
    "v_mfma_f32_16x16x32_bf16 v[96:99], a[100:103], a[68:71], v[96:99]\n"
    "ds_read_b64_tr_b16 a[128:129], v129 offset:33024\n"
    "v_mfma_f32_16x16x32_bf16 v[96:99], a[104:107], a[72:75], v[96:99]\n"
    "v_exp_f32 v101, v101\n"
    "v_mfma_f32_16x16x32_bf16 v[96:99], a[108:111], a[76:79], v[96:99]\n"
    "ds_read_b64_tr_b16 a[130:131], v129 offset:37120\n"
    "v_mfma_f32_16x16x32_bf16 v[180:183], a[136:139], v[16:19], 0\n"
    "v_exp_f32 v102, v102\n"
    "v_mfma_f32_16x16x32_bf16 v[180:183], a[140:143], v[20:23], v[180:183]\n"
    "v_cvt_pk_bf16_f32 v72, v100, v101\n"
    "v_mfma_f32_16x16x32_bf16 v[184:187], a[140:143], v[28:31], 0\n"
    "v_exp_f32 v103, v103\n"
    "v_mfma_f32_16x16x32_bf16 v[184:187], a[136:139], v[24:27], v[184:187]\n"
    "v_cvt_pk_bf16_f32 v73, v102, v103\n"
    "v_mfma_f32_16x16x32_bf16 v[100:103], a[96:99], a[80:83], v[124:127]\n"
    "v_exp_f32 v96, v96\n"
    "v_mfma_f32_16x16x32_bf16 v[100:103], a[100:103], a[84:87], v[100:103]\n"
    "ds_read_b64_tr_b16 a[132:133], v129 offset:41216\n"
    "v_mfma_f32_16x16x32_bf16 v[100:103], a[104:107], a[88:91], v[100:103]\n"
    "v_exp_f32 v97, v97\n"
    "v_mfma_f32_16x16x32_bf16 v[100:103], a[108:111], a[92:95], v[100:103]\n"
    "ds_read_b64_tr_b16 a[134:135], v129 offset:45312\n"
    "v_mfma_f32_16x16x32_bf16 v[188:191], a[136:139], v[32:35], 0\n"
    "v_exp_f32 v98, v98\n"
    "v_mfma_f32_16x16x32_bf16 v[188:191], a[140:143], v[36:39], v[188:191]\n"
    "v_cvt_pk_bf16_f32 v80, v96, v97\n"
    "v_mfma_f32_16x16x32_bf16 v[192:195], a[140:143], v[44:47], 0\n"
    "v_exp_f32 v99, v99\n"
    "v_mfma_f32_16x16x32_bf16 v[192:195], a[136:139], v[40:43], v[192:195]\n"
    "s_waitcnt lgkmcnt(4)\n"
    "v_cvt_pk_bf16_f32 v81, v98, v99\n"
    "v_mfma_f32_16x16x32_bf16 v[96:99], a[112:115], a[0:3], v[104:107]\n"
    "s_mov_b32 m0, s63\n"
    "v_exp_f32 v100, v100\n"
    "v_mfma_f32_16x16x32_bf16 v[96:99], a[116:119], a[4:7], v[96:99]\n"
    "s_add_u32 s56, s56, 16384\n"
    "buffer_load_dwordx4 v131, s[36:39], s58 offen offset:2048 lds\n"
    "v_mfma_f32_16x16x32_bf16 v[96:99], a[120:123], a[8:11], v[96:99]\n"
    "s_and_b32 s56, s56, 262143\n"
    "v_exp_f32 v101, v101\n"
    "v_mfma_f32_16x16x32_bf16 v[96:99], a[124:127], a[12:15], v[96:99]\n"
    "s_waitcnt lgkmcnt(0)\n"
    "v_mfma_f32_16x16x32_bf16 v[196:199], a[128:131], v[0:3], 0\n"
    "s_or_b32 s56, s56, s61\n"
    "v_exp_f32 v102, v102\n"
    "v_mfma_f32_16x16x32_bf16 v[196:199], a[132:135], v[4:7], v[196:199]\n"
    "s_add_u32 s94, s56, 8192\n"
    "v_cvt_pk_bf16_f32 v88, v100, v101\n"
    "v_mfma_f32_16x16x32_bf16 v[200:203], a[132:135], v[12:15], 0\n"
    "s_add_u32 s57, s57, 16384\n"
    "v_exp_f32 v103, v103\n"
    "v_mfma_f32_16x16x32_bf16 v[200:203], a[128:131], v[8:11], v[200:203]\n"
    "s_and_b32 s57, s57, 262143\n"
    "v_cvt_pk_bf16_f32 v89, v102, v103\n"
    "v_mfma_f32_16x16x32_bf16 v[100:103], a[112:115], a[16:19], v[108:111]\n"
    "s_or_b32 s57, s57, s96\n"
    "v_exp_f32 v96, v96\n"
    "v_mfma_f32_16x16x32_bf16 v[100:103], a[116:119], a[20:23], v[100:103]\n"
    "s_add_u32 s70, s57, 8192\n"
    "ds_read_b64_tr_b16 a[136:137], v130 offset:33024\n"
    "v_mfma_f32_16x16x32_bf16 v[100:103], a[120:123], a[24:27], v[100:103]\n"
    "v_exp_f32 v97, v97\n"
    "v_mfma_f32_16x16x32_bf16 v[100:103], a[124:127], a[28:31], v[100:103]\n"
    "ds_read_b64_tr_b16 a[138:139], v130 offset:37120\n"
    "v_mfma_f32_16x16x32_bf16 v[204:207], a[128:131], v[16:19], 0\n"
    "v_exp_f32 v98, v98\n"
    "v_mfma_f32_16x16x32_bf16 v[204:207], a[132:135], v[20:23], v[204:207]\n"
    "v_cvt_pk_bf16_f32 v50, v96, v97\n"
    "v_mfma_f32_16x16x32_bf16 v[208:211], a[132:135], v[28:31], 0\n"
    "v_exp_f32 v99, v99\n"
    "v_mfma_f32_16x16x32_bf16 v[208:211], a[128:131], v[24:27], v[208:211]\n"
    "v_cvt_pk_bf16_f32 v51, v98, v99\n"
    "v_smfmac_f32_16x16x64_bf16 a[148:151], a[168:171], v[8:15], v133\n"
    "v_mfma_f32_16x16x32_bf16 v[96:99], a[112:115], a[32:35], v[112:115]\n"
    "v_exp_f32 v100, v100\n"
    "v_mfma_f32_16x16x32_bf16 v[96:99], a[116:119], a[36:39], v[96:99]\n"
    "ds_read_b64_tr_b16 a[140:141], v130 offset:41216\n"
    "v_mfma_f32_16x16x32_bf16 v[96:99], a[120:123], a[40:43], v[96:99]\n"
    "v_exp_f32 v101, v101\n"
    "v_mfma_f32_16x16x32_bf16 v[96:99], a[124:127], a[44:47], v[96:99]\n"
    "ds_read_b64_tr_b16 a[142:143], v130 offset:45312\n"
    "v_mfma_f32_16x16x32_bf16 v[212:215], a[128:131], v[32:35], 0\n"
    "v_exp_f32 v102, v102\n"
    "v_mfma_f32_16x16x32_bf16 v[212:215], a[132:135], v[36:39], v[212:215]\n"
    "v_cvt_pk_bf16_f32 v58, v100, v101\n"
    "v_mfma_f32_16x16x32_bf16 v[216:219], a[132:135], v[44:47], 0\n"
    "v_exp_f32 v103, v103\n"
    "v_mfma_f32_16x16x32_bf16 v[216:219], a[128:131], v[40:43], v[216:219]\n"
    "v_cvt_pk_bf16_f32 v59, v102, v103\n"
    "v_mfma_f32_16x16x32_bf16 v[100:103], a[112:115], a[48:51], v[116:119]\n"
    "v_exp_f32 v96, v96\n"
    "v_mfma_f32_16x16x32_bf16 v[100:103], a[116:119], a[52:55], v[100:103]\n"
    "v_mfma_f32_16x16x32_bf16 v[100:103], a[120:123], a[56:59], v[100:103]\n"
    "s_waitcnt vmcnt(9) lgkmcnt(0)\n"
    "v_exp_f32 v97, v97\n"
    "v_mfma_f32_16x16x32_bf16 v[100:103], a[124:127], a[60:63], v[100:103]\n"
    "s_barrier\n"
    "v_mfma_f32_16x16x32_bf16 v[220:223], a[136:139], v[0:3], 0\n"
    "v_exp_f32 v98, v98\n"
    "v_mfma_f32_16x16x32_bf16 v[220:223], a[140:143], v[4:7], v[220:223]\n"
    "ds_read_b128 a[96:99], v128 offset:24576\n"
    "v_cvt_pk_bf16_f32 v66, v96, v97\n"
    "v_mfma_f32_16x16x32_bf16 v[224:227], a[140:143], v[12:15], 0\n"
    "v_exp_f32 v99, v99\n"
    "v_mfma_f32_16x16x32_bf16 v[224:227], a[136:139], v[8:11], v[224:227]\n"
    "ds_read_b128 a[100:103], v128 offset:24832\n"
    "v_cvt_pk_bf16_f32 v67, v98, v99\n"
    "v_mfma_f32_16x16x32_bf16 v[96:99], a[112:115], a[64:67], v[120:123]\n"
    "v_exp_f32 v100, v100\n"
    "v_mfma_f32_16x16x32_bf16 v[96:99], a[116:119], a[68:71], v[96:99]\n"
    "ds_read_b64_tr_b16 a[128:129], v129 offset:33280\n"
    "ds_read_b64_tr_b16 a[130:131], v129 offset:37376\n"
    "v_mfma_f32_16x16x32_bf16 v[96:99], a[120:123], a[72:75], v[96:99]\n"
    "s_mov_b32 m0, s82\n"
    "v_exp_f32 v101, v101\n"
    "v_mfma_f32_16x16x32_bf16 v[96:99], a[124:127], a[76:79], v[96:99]\n"
    "buffer_load_dwordx4 v131, s[40:43], s56 offen lds\n"
    "v_mfma_f32_16x16x32_bf16 v[228:231], a[136:139], v[16:19], 0\n"
    "v_exp_f32 v102, v102\n"
    "v_mfma_f32_16x16x32_bf16 v[228:231], a[140:143], v[20:23], v[228:231]\n"
    "ds_read_b128 a[104:107], v128 offset:25088\n"
    "v_cvt_pk_bf16_f32 v74, v100, v101\n"
    "v_mfma_f32_16x16x32_bf16 v[232:235], a[140:143], v[28:31], 0\n"
    "v_exp_f32 v103, v103\n"
    "v_mfma_f32_16x16x32_bf16 v[232:235], a[136:139], v[24:27], v[232:235]\n"
    "ds_read_b128 a[108:111], v128 offset:25344\n"
    "v_cvt_pk_bf16_f32 v75, v102, v103\n"
    "v_mfma_f32_16x16x32_bf16 v[100:103], a[112:115], a[80:83], v[124:127]\n"
    "v_exp_f32 v96, v96\n"
    "v_mfma_f32_16x16x32_bf16 v[100:103], a[116:119], a[84:87], v[100:103]\n"
    "ds_read_b64_tr_b16 a[132:133], v129 offset:41472\n"
    "ds_read_b64_tr_b16 a[134:135], v129 offset:45568\n"
    "v_mfma_f32_16x16x32_bf16 v[100:103], a[120:123], a[88:91], v[100:103]\n"
    "v_exp_f32 v97, v97\n"
    "v_mfma_f32_16x16x32_bf16 v[100:103], a[124:127], a[92:95], v[100:103]\n"
    "buffer_load_dwordx4 v131, s[40:43], s56 offen offset:2048 lds\n"
    "v_mfma_f32_16x16x32_bf16 v[236:239], a[136:139], v[32:35], 0\n"
    "v_exp_f32 v98, v98\n"
    "v_mfma_f32_16x16x32_bf16 v[236:239], a[140:143], v[36:39], v[236:239]\n"
    "v_cvt_pk_bf16_f32 v82, v96, v97\n"
    "v_mfma_f32_16x16x32_bf16 v[240:243], a[140:143], v[44:47], 0\n"
    "v_exp_f32 v99, v99\n"
    "v_mfma_f32_16x16x32_bf16 v[240:243], a[136:139], v[40:43], v[240:243]\n"
    "s_waitcnt lgkmcnt(2)\n"
    "v_cvt_pk_bf16_f32 v83, v98, v99\n"
    "v_smfmac_f32_16x16x64_bf16 a[152:155], a[168:171], v[16:23], v133\n"
    "v_mfma_f32_16x16x32_bf16 v[96:99], a[96:99], a[0:3], v[104:107]\n"
    "v_exp_f32 v100, v100\n"
    "v_mfma_f32_16x16x32_bf16 v[96:99], a[100:103], a[4:7], v[96:99]\n"
    "v_mbcnt_lo_u32_b32 v134, -1, 0\n"
    "v_mbcnt_hi_u32_b32 v134, -1, v134\n"
    "v_mfma_f32_16x16x32_bf16 v[96:99], a[104:107], a[8:11], v[96:99]\n"
    "v_exp_f32 v101, v101\n"
    "v_mfma_f32_16x16x32_bf16 v[96:99], a[108:111], a[12:15], v[96:99]\n"
    "s_waitcnt lgkmcnt(0)\n"
    "v_lshlrev_b32 v134, 4, v134\n"
    "v_mfma_f32_16x16x32_bf16 v[244:247], a[128:131], v[0:3], 0\n"
    "s_add_u32 s65, s62, 20480\n"
    "v_exp_f32 v102, v102\n"
    "v_mfma_f32_16x16x32_bf16 v[244:247], a[132:135], v[4:7], v[244:247]\n"
    "ds_read_b128 a[112:115], v128 offset:28672\n"
    "v_cvt_pk_bf16_f32 v90, v100, v101\n"
    "v_mfma_f32_16x16x32_bf16 v[248:251], a[132:135], v[12:15], 0\n"
    "v_exp_f32 v103, v103\n"
    "v_mfma_f32_16x16x32_bf16 v[248:251], a[128:131], v[8:11], v[248:251]\n"
    "ds_read_b128 a[116:119], v128 offset:28928\n"
    "v_cvt_pk_bf16_f32 v91, v102, v103\n"
    "v_mfma_f32_16x16x32_bf16 v[100:103], a[96:99], a[16:19], v[108:111]\n"
    "v_exp_f32 v96, v96\n"
    "v_mfma_f32_16x16x32_bf16 v[100:103], a[100:103], a[20:23], v[100:103]\n"
    "ds_read_b64_tr_b16 a[136:137], v130 offset:33280\n"
    "v_add_u32 v134, s65, v134\n"
    "v_mfma_f32_16x16x32_bf16 v[100:103], a[104:107], a[24:27], v[100:103]\n"
    "v_exp_f32 v97, v97\n"
    "v_mfma_f32_16x16x32_bf16 v[100:103], a[108:111], a[28:31], v[100:103]\n"
    "ds_read_b64_tr_b16 a[138:139], v130 offset:37376\n"
    "v_mfma_f32_16x16x32_bf16 v[252:255], a[128:131], v[16:19], 0\n"
    "v_exp_f32 v98, v98\n"
    "v_mfma_f32_16x16x32_bf16 v[252:255], a[132:135], v[20:23], v[252:255]\n"
    "v_cvt_pk_bf16_f32 v52, v96, v97\n"
    "v_mfma_f32_16x16x32_bf16 a[172:175], a[132:135], v[28:31], 0\n"
    "v_exp_f32 v99, v99\n"
    "v_mfma_f32_16x16x32_bf16 a[172:175], a[128:131], v[24:27], a[172:175]\n"
    "v_cvt_pk_bf16_f32 v53, v98, v99\n"
    "v_mfma_f32_16x16x32_bf16 v[96:99], a[96:99], a[32:35], v[112:115]\n"
    "v_exp_f32 v100, v100\n"
    "v_mfma_f32_16x16x32_bf16 v[96:99], a[100:103], a[36:39], v[96:99]\n"
    "ds_read_b64_tr_b16 a[140:141], v130 offset:41472\n"
    "v_mfma_f32_16x16x32_bf16 v[96:99], a[104:107], a[40:43], v[96:99]\n"
    "v_exp_f32 v101, v101\n"
    "v_mfma_f32_16x16x32_bf16 v[96:99], a[108:111], a[44:47], v[96:99]\n"
    "ds_read_b64_tr_b16 a[142:143], v130 offset:45568\n"
    "v_mfma_f32_16x16x32_bf16 a[176:179], a[128:131], v[32:35], 0\n"
    "v_exp_f32 v102, v102\n"
    "v_mfma_f32_16x16x32_bf16 a[176:179], a[132:135], v[36:39], a[176:179]\n"
    "v_cvt_pk_bf16_f32 v60, v100, v101\n"
    "v_mfma_f32_16x16x32_bf16 a[180:183], a[132:135], v[44:47], 0\n"
    "v_exp_f32 v103, v103\n"
    "v_mfma_f32_16x16x32_bf16 a[180:183], a[128:131], v[40:43], a[180:183]\n"
    "v_cvt_pk_bf16_f32 v61, v102, v103\n"
    "v_mfma_f32_16x16x32_bf16 v[100:103], a[96:99], a[48:51], v[116:119]\n"
    "s_mov_b32 m0, s63\n"
    "v_exp_f32 v96, v96\n"
    "v_mfma_f32_16x16x32_bf16 v[100:103], a[100:103], a[52:55], v[100:103]\n"
    "buffer_load_dwordx4 v131, s[36:39], s58 offen offset:3072 lds\n"
    "v_mfma_f32_16x16x32_bf16 v[100:103], a[104:107], a[56:59], v[100:103]\n"
    "v_exp_f32 v97, v97\n"
    "v_mfma_f32_16x16x32_bf16 v[100:103], a[108:111], a[60:63], v[100:103]\n"
    "s_waitcnt lgkmcnt(0)\n"
    "v_mfma_f32_16x16x32_bf16 a[184:187], a[136:139], v[0:3], 0\n"
    "v_exp_f32 v98, v98\n"
    "v_mfma_f32_16x16x32_bf16 a[184:187], a[140:143], v[4:7], a[184:187]\n"
    "ds_read_b128 a[120:123], v128 offset:29184\n"
    "v_cvt_pk_bf16_f32 v68, v96, v97\n"
    "v_mfma_f32_16x16x32_bf16 a[188:191], a[140:143], v[12:15], 0\n"
    "v_exp_f32 v99, v99\n"
    "v_mfma_f32_16x16x32_bf16 a[188:191], a[136:139], v[8:11], a[188:191]\n"
    "ds_read_b128 a[124:127], v128 offset:29440\n"
    "v_cvt_pk_bf16_f32 v69, v98, v99\n"
    "v_smfmac_f32_16x16x64_bf16 a[156:159], a[168:171], v[24:31], v133\n"
    "v_mfma_f32_16x16x32_bf16 v[96:99], a[96:99], a[64:67], v[120:123]\n"
    "v_exp_f32 v100, v100\n"
    "v_mfma_f32_16x16x32_bf16 v[96:99], a[100:103], a[68:71], v[96:99]\n"
    "ds_read_b64_tr_b16 a[128:129], v129 offset:33536\n"
    "v_mfma_f32_16x16x32_bf16 v[96:99], a[104:107], a[72:75], v[96:99]\n"
    "v_exp_f32 v101, v101\n"
    "v_mfma_f32_16x16x32_bf16 v[96:99], a[108:111], a[76:79], v[96:99]\n"
    "ds_read_b64_tr_b16 a[130:131], v129 offset:37632\n"
    "v_mfma_f32_16x16x32_bf16 a[192:195], a[136:139], v[16:19], 0\n"
    "v_exp_f32 v102, v102\n"
    "v_mfma_f32_16x16x32_bf16 a[192:195], a[140:143], v[20:23], a[192:195]\n"
    "v_cvt_pk_bf16_f32 v76, v100, v101\n"
    "v_mfma_f32_16x16x32_bf16 a[196:199], a[140:143], v[28:31], 0\n"
    "v_exp_f32 v103, v103\n"
    "v_mfma_f32_16x16x32_bf16 a[196:199], a[136:139], v[24:27], a[196:199]\n"
    "v_cvt_pk_bf16_f32 v77, v102, v103\n"
    "v_mfma_f32_16x16x32_bf16 v[100:103], a[96:99], a[80:83], v[124:127]\n"
    "v_exp_f32 v96, v96\n"
    "v_mfma_f32_16x16x32_bf16 v[100:103], a[100:103], a[84:87], v[100:103]\n"
    "ds_read_b64_tr_b16 a[132:133], v129 offset:41728\n"
    "v_mfma_f32_16x16x32_bf16 v[100:103], a[104:107], a[88:91], v[100:103]\n"
    "v_exp_f32 v97, v97\n"
    "v_mfma_f32_16x16x32_bf16 v[100:103], a[108:111], a[92:95], v[100:103]\n"
    "ds_read_b64_tr_b16 a[134:135], v129 offset:45824\n"
    "v_mfma_f32_16x16x32_bf16 a[200:203], a[136:139], v[32:35], 0\n"
    "v_exp_f32 v98, v98\n"
    "v_mfma_f32_16x16x32_bf16 a[200:203], a[140:143], v[36:39], a[200:203]\n"
    "v_cvt_pk_bf16_f32 v84, v96, v97\n"
    "v_mfma_f32_16x16x32_bf16 a[204:207], a[140:143], v[44:47], 0\n"
    "v_exp_f32 v99, v99\n"
    "v_mfma_f32_16x16x32_bf16 a[204:207], a[136:139], v[40:43], a[204:207]\n"
    "s_waitcnt lgkmcnt(4)\n"
    "v_cvt_pk_bf16_f32 v85, v98, v99\n"
    "v_mfma_f32_16x16x32_bf16 v[96:99], a[112:115], a[0:3], v[104:107]\n"
    "v_exp_f32 v100, v100\n"
    "v_mfma_f32_16x16x32_bf16 v[96:99], a[116:119], a[4:7], v[96:99]\n"
    "v_mfma_f32_16x16x32_bf16 v[96:99], a[120:123], a[8:11], v[96:99]\n"
    "v_exp_f32 v101, v101\n"
    "v_mfma_f32_16x16x32_bf16 v[96:99], a[124:127], a[12:15], v[96:99]\n"
    "s_waitcnt lgkmcnt(0)\n"
    "v_mfma_f32_16x16x32_bf16 a[208:211], a[128:131], v[0:3], 0\n"
    "v_exp_f32 v102, v102\n"
    "v_mfma_f32_16x16x32_bf16 a[208:211], a[132:135], v[4:7], a[208:211]\n"
    "v_cvt_pk_bf16_f32 v92, v100, v101\n"
    "v_mfma_f32_16x16x32_bf16 a[212:215], a[132:135], v[12:15], 0\n"
    "v_exp_f32 v103, v103\n"
    "v_mfma_f32_16x16x32_bf16 a[212:215], a[128:131], v[8:11], a[212:215]\n"
    "v_cvt_pk_bf16_f32 v93, v102, v103\n"
    "v_mfma_f32_16x16x32_bf16 v[100:103], a[112:115], a[16:19], v[108:111]\n"
    "v_exp_f32 v96, v96\n"
    "v_mfma_f32_16x16x32_bf16 v[100:103], a[116:119], a[20:23], v[100:103]\n"
    "ds_read_b64_tr_b16 a[136:137], v130 offset:33536\n"
    "v_mfma_f32_16x16x32_bf16 v[100:103], a[120:123], a[24:27], v[100:103]\n"
    "v_exp_f32 v97, v97\n"
    "v_mfma_f32_16x16x32_bf16 v[100:103], a[124:127], a[28:31], v[100:103]\n"
    "ds_read_b64_tr_b16 a[138:139], v130 offset:37632\n"
    "v_mfma_f32_16x16x32_bf16 a[216:219], a[128:131], v[16:19], 0\n"
    "v_exp_f32 v98, v98\n"
    "v_mfma_f32_16x16x32_bf16 a[216:219], a[132:135], v[20:23], a[216:219]\n"
    "v_cvt_pk_bf16_f32 v54, v96, v97\n"
    "v_mfma_f32_16x16x32_bf16 a[220:223], a[132:135], v[28:31], 0\n"
    "v_exp_f32 v99, v99\n"
    "v_mfma_f32_16x16x32_bf16 a[220:223], a[128:131], v[24:27], a[220:223]\n"
    "v_cvt_pk_bf16_f32 v55, v98, v99\n"
    "v_smfmac_f32_16x16x64_bf16 a[160:163], a[168:171], v[32:39], v133\n"
    "v_mfma_f32_16x16x32_bf16 v[96:99], a[112:115], a[32:35], v[112:115]\n"
    "v_exp_f32 v100, v100\n"
    "v_mfma_f32_16x16x32_bf16 v[96:99], a[116:119], a[36:39], v[96:99]\n"
    "ds_read_b64_tr_b16 a[140:141], v130 offset:41728\n"
    "v_mfma_f32_16x16x32_bf16 v[96:99], a[120:123], a[40:43], v[96:99]\n"
    "v_exp_f32 v101, v101\n"
    "v_mfma_f32_16x16x32_bf16 v[96:99], a[124:127], a[44:47], v[96:99]\n"
    "ds_read_b64_tr_b16 a[142:143], v130 offset:45824\n"
    "v_mfma_f32_16x16x32_bf16 a[224:227], a[128:131], v[32:35], 0\n"
    "v_exp_f32 v102, v102\n"
    "v_mfma_f32_16x16x32_bf16 a[224:227], a[132:135], v[36:39], a[224:227]\n"
    "v_cvt_pk_bf16_f32 v62, v100, v101\n"
    "v_mfma_f32_16x16x32_bf16 a[228:231], a[132:135], v[44:47], 0\n"
    "v_exp_f32 v103, v103\n"
    "v_mfma_f32_16x16x32_bf16 a[228:231], a[128:131], v[40:43], a[228:231]\n"
    "v_cvt_pk_bf16_f32 v63, v102, v103\n"
    "v_mfma_f32_16x16x32_bf16 v[100:103], a[112:115], a[48:51], v[116:119]\n"
    "v_exp_f32 v96, v96\n"
    "v_mfma_f32_16x16x32_bf16 v[100:103], a[116:119], a[52:55], v[100:103]\n"
    "v_mfma_f32_16x16x32_bf16 v[100:103], a[120:123], a[56:59], v[100:103]\n"
    "s_waitcnt vmcnt(6) lgkmcnt(0)\n"
    "v_exp_f32 v97, v97\n"
    "v_mfma_f32_16x16x32_bf16 v[100:103], a[124:127], a[60:63], v[100:103]\n"
    "s_barrier\n"
    "v_mfma_f32_16x16x32_bf16 a[232:235], a[136:139], v[0:3], 0\n"
    "v_exp_f32 v98, v98\n"
    "v_mfma_f32_16x16x32_bf16 a[232:235], a[140:143], v[4:7], a[232:235]\n"
    "ds_read_b128 a[96:99], v128 offset:0\n"
    "v_cvt_pk_bf16_f32 v70, v96, v97\n"
    "v_mfma_f32_16x16x32_bf16 a[236:239], a[140:143], v[12:15], 0\n"
    "v_exp_f32 v99, v99\n"
    "v_mfma_f32_16x16x32_bf16 a[236:239], a[136:139], v[8:11], a[236:239]\n"
    "ds_read_b128 a[100:103], v128 offset:256\n"
    "v_cvt_pk_bf16_f32 v71, v98, v99\n"
    "v_mfma_f32_16x16x32_bf16 v[96:99], a[112:115], a[64:67], v[120:123]\n"
    "v_exp_f32 v100, v100\n"
    "v_mfma_f32_16x16x32_bf16 v[96:99], a[116:119], a[68:71], v[96:99]\n"
    "ds_read_b64_tr_b16 a[128:129], v129 offset:49152\n"
    "ds_read_b64_tr_b16 a[130:131], v129 offset:53248\n"
    "v_mfma_f32_16x16x32_bf16 v[96:99], a[120:123], a[72:75], v[96:99]\n"
    "s_mov_b32 m0, s84\n"
    "v_exp_f32 v101, v101\n"
    "v_mfma_f32_16x16x32_bf16 v[96:99], a[124:127], a[76:79], v[96:99]\n"
    "buffer_load_dwordx4 v131, s[44:47], s57 offen lds\n"
    "v_mfma_f32_16x16x32_bf16 a[240:243], a[136:139], v[16:19], 0\n"
    "v_exp_f32 v102, v102\n"
    "v_mfma_f32_16x16x32_bf16 a[240:243], a[140:143], v[20:23], a[240:243]\n"
    "ds_read_b128 a[104:107], v128 offset:512\n"
    "v_cvt_pk_bf16_f32 v78, v100, v101\n"
    "v_mfma_f32_16x16x32_bf16 a[244:247], a[140:143], v[28:31], 0\n"
    "v_exp_f32 v103, v103\n"
    "v_mfma_f32_16x16x32_bf16 a[244:247], a[136:139], v[24:27], a[244:247]\n"
    "ds_read_b128 a[108:111], v128 offset:768\n"
    "v_cvt_pk_bf16_f32 v79, v102, v103\n"
    "v_mfma_f32_16x16x32_bf16 v[100:103], a[112:115], a[80:83], v[124:127]\n"
    "v_exp_f32 v96, v96\n"
    "v_mfma_f32_16x16x32_bf16 v[100:103], a[116:119], a[84:87], v[100:103]\n"
    "ds_read_b64_tr_b16 a[132:133], v129 offset:57344\n"
    "ds_read_b64_tr_b16 a[134:135], v129 offset:61440\n"
    "v_mfma_f32_16x16x32_bf16 v[100:103], a[120:123], a[88:91], v[100:103]\n"
    "v_exp_f32 v97, v97\n"
    "v_mfma_f32_16x16x32_bf16 v[100:103], a[124:127], a[92:95], v[100:103]\n"
    "buffer_load_dwordx4 v131, s[44:47], s57 offen offset:2048 lds\n"
    "v_mfma_f32_16x16x32_bf16 a[248:251], a[136:139], v[32:35], 0\n"
    "v_exp_f32 v98, v98\n"
    "v_mfma_f32_16x16x32_bf16 a[248:251], a[140:143], v[36:39], a[248:251]\n"
    "v_cvt_pk_bf16_f32 v86, v96, v97\n"
    "v_mfma_f32_16x16x32_bf16 a[252:255], a[140:143], v[44:47], 0\n"
    "v_exp_f32 v99, v99\n"
    "v_mfma_f32_16x16x32_bf16 a[252:255], a[136:139], v[40:43], a[252:255]\n"
    "s_waitcnt lgkmcnt(2)\n"
    "v_cvt_pk_bf16_f32 v87, v98, v99\n"
    "v_smfmac_f32_16x16x64_bf16 a[164:167], a[168:171], v[40:47], v133\n"
    "s_mov_b32 s60, 0\n"
    ".p2align 6\n"
    "Lloop1_%=:\n"
    "v_mfma_f32_16x16x32_bf16 v[96:99], a[96:99], a[0:3], v[104:107]\n"
    "s_mov_b32 m0, s85\n"
    "v_exp_f32 v100, v100\n"
    "v_mfma_f32_16x16x32_bf16 v[96:99], a[100:103], a[4:7], v[96:99]\n"
    "buffer_load_dwordx4 v131, s[44:47], s70 offen lds\n"
    "v_mfma_f32_16x16x32_bf16 v[96:99], a[104:107], a[8:11], v[96:99]\n"
    "v_exp_f32 v101, v101\n"
    "v_mfma_f32_16x16x32_bf16 v[96:99], a[108:111], a[12:15], v[96:99]\n"
    "s_waitcnt lgkmcnt(0)\n"
    "ds_read_b128 v[136:139], v134 offset:4096\n"
    "v_mfma_f32_16x16x32_bf16 v[148:151], a[128:131], v[48:51], v[148:151]\n"
    "v_exp_f32 v102, v102\n"
    "v_mfma_f32_16x16x32_bf16 v[148:151], a[132:135], v[52:55], v[148:151]\n"
    "ds_read_b128 a[112:115], v128 offset:4096\n"
    "v_cvt_pk_bf16_f32 v94, v100, v101\n"
    "v_mfma_f32_16x16x32_bf16 v[152:155], a[132:135], v[60:63], v[152:155]\n"
    "v_exp_f32 v103, v103\n"
    "v_mfma_f32_16x16x32_bf16 v[152:155], a[128:131], v[56:59], v[152:155]\n"
    "ds_read_b128 a[116:119], v128 offset:4352\n"
    "v_cvt_pk_bf16_f32 v95, v102, v103\n"
    "v_mfma_f32_16x16x32_bf16 v[100:103], a[96:99], a[16:19], v[108:111]\n"
    "v_exp_f32 v96, v96\n"
    "v_mfma_f32_16x16x32_bf16 v[100:103], a[100:103], a[20:23], v[100:103]\n"
    "ds_read_b64_tr_b16 a[136:137], v130 offset:49152\n"
    "ds_read_b64_tr_b16 a[138:139], v130 offset:53248\n"
    "v_mfma_f32_16x16x32_bf16 v[100:103], a[104:107], a[24:27], v[100:103]\n"
    "v_exp_f32 v97, v97\n"
    "v_mfma_f32_16x16x32_bf16 v[100:103], a[108:111], a[28:31], v[100:103]\n"
    "buffer_load_dwordx4 v131, s[44:47], s70 offen offset:2048 lds\n"
    "v_mfma_f32_16x16x32_bf16 v[156:159], a[128:131], v[64:67], v[156:159]\n"
    "s_waitcnt lgkmcnt(4)\n"
    "v_exp_f32 v98, v98\n"
    "v_mfma_f32_16x16x32_bf16 v[156:159], a[132:135], v[68:71], v[156:159]\n"
    "v_cvt_pk_bf16_f32 v0, v96, v97\n"
    "v_lshlrev_b32 v140, 16, v136\n"
    "v_mfma_f32_16x16x32_bf16 v[160:163], a[132:135], v[76:79], v[160:163]\n"
    "v_exp_f32 v99, v99\n"
    "v_mfma_f32_16x16x32_bf16 v[160:163], a[128:131], v[72:75], v[160:163]\n"
    "v_cvt_pk_bf16_f32 v1, v98, v99\n"
    "v_and_b32 v141, s68, v136\n"
    "v_mfma_f32_16x16x32_bf16 v[96:99], a[96:99], a[32:35], v[112:115]\n"
    "v_exp_f32 v100, v100\n"
    "v_mfma_f32_16x16x32_bf16 v[96:99], a[100:103], a[36:39], v[96:99]\n"
    "ds_read_b64_tr_b16 a[140:141], v130 offset:57344\n"
    "ds_read_b64_tr_b16 a[142:143], v130 offset:61440\n"
    "v_mfma_f32_16x16x32_bf16 v[96:99], a[104:107], a[40:43], v[96:99]\n"
    "s_mov_b32 m0, s83\n"
    "v_exp_f32 v101, v101\n"
    "v_mfma_f32_16x16x32_bf16 v[96:99], a[108:111], a[44:47], v[96:99]\n"
    "buffer_load_dwordx4 v131, s[40:43], s94 offen lds\n"
    "v_mfma_f32_16x16x32_bf16 v[164:167], a[128:131], v[80:83], v[164:167]\n"
    "v_exp_f32 v102, v102\n"
    "v_mfma_f32_16x16x32_bf16 v[164:167], a[132:135], v[84:87], v[164:167]\n"
    "v_cvt_pk_bf16_f32 v8, v100, v101\n"
    "v_mul_f32 v140, s76, v140\n"
    "v_mfma_f32_16x16x32_bf16 v[168:171], a[132:135], v[92:95], v[168:171]\n"
    "v_exp_f32 v103, v103\n"
    "v_mfma_f32_16x16x32_bf16 v[168:171], a[128:131], v[88:91], v[168:171]\n"
    "v_cvt_pk_bf16_f32 v9, v102, v103\n"
    "v_mul_f32 v141, s76, v141\n"
    "v_mfma_f32_16x16x32_bf16 v[100:103], a[96:99], a[48:51], v[116:119]\n"
    "v_exp_f32 v96, v96\n"
    "v_mfma_f32_16x16x32_bf16 v[100:103], a[100:103], a[52:55], v[100:103]\n"
    "buffer_load_dwordx4 v131, s[40:43], s94 offen offset:2048 lds\n"
    "v_mfma_f32_16x16x32_bf16 v[100:103], a[104:107], a[56:59], v[100:103]\n"
    "v_exp_f32 v97, v97\n"
    "v_mfma_f32_16x16x32_bf16 v[100:103], a[108:111], a[60:63], v[100:103]\n"
    "s_waitcnt lgkmcnt(0)\n"
    "v_cvt_pk_bf16_f32 v136, v140, v141\n"
    "v_lshlrev_b32 v142, 16, v137\n"
    "v_mfma_f32_16x16x32_bf16 v[172:175], a[136:139], v[48:51], v[172:175]\n"
    "v_exp_f32 v98, v98\n"
    "v_mfma_f32_16x16x32_bf16 v[172:175], a[140:143], v[52:55], v[172:175]\n"
    "ds_read_b128 a[120:123], v128 offset:4608\n"
    "v_cvt_pk_bf16_f32 v16, v96, v97\n"
    "v_mfma_f32_16x16x32_bf16 v[176:179], a[140:143], v[60:63], v[176:179]\n"
    "v_exp_f32 v99, v99\n"
    "v_mfma_f32_16x16x32_bf16 v[176:179], a[136:139], v[56:59], v[176:179]\n"
    "ds_read_b128 a[124:127], v128 offset:4864\n"
    "v_cvt_pk_bf16_f32 v17, v98, v99\n"
    "v_smfmac_f32_16x16x64_bf16 a[144:147], a[168:171], v[48:55], v133\n"
    "v_and_b32 v143, s68, v137\n"
    "v_mul_f32 v142, s76, v142\n"
    "v_mfma_f32_16x16x32_bf16 v[96:99], a[96:99], a[64:67], v[120:123]\n"
    "v_exp_f32 v100, v100\n"
    "v_mfma_f32_16x16x32_bf16 v[96:99], a[100:103], a[68:71], v[96:99]\n"
    "ds_read_b64_tr_b16 a[128:129], v129 offset:49408\n"
    "v_mul_f32 v143, s76, v143\n"
    "v_mfma_f32_16x16x32_bf16 v[96:99], a[104:107], a[72:75], v[96:99]\n"
    "v_exp_f32 v101, v101\n"
    "v_mfma_f32_16x16x32_bf16 v[96:99], a[108:111], a[76:79], v[96:99]\n"
    "ds_read_b64_tr_b16 a[130:131], v129 offset:53504\n"
    "v_cvt_pk_bf16_f32 v137, v142, v143\n"
    "v_mfma_f32_16x16x32_bf16 v[180:183], a[136:139], v[64:67], v[180:183]\n"
    "v_exp_f32 v102, v102\n"
    "v_mfma_f32_16x16x32_bf16 v[180:183], a[140:143], v[68:71], v[180:183]\n"
    "v_cvt_pk_bf16_f32 v24, v100, v101\n"
    "v_lshlrev_b32 v140, 16, v138\n"
    "v_mfma_f32_16x16x32_bf16 v[184:187], a[140:143], v[76:79], v[184:187]\n"
    "v_exp_f32 v103, v103\n"
    "v_mfma_f32_16x16x32_bf16 v[184:187], a[136:139], v[72:75], v[184:187]\n"
    "v_cvt_pk_bf16_f32 v25, v102, v103\n"
    "v_and_b32 v141, s68, v138\n"
    "v_mfma_f32_16x16x32_bf16 v[100:103], a[96:99], a[80:83], v[124:127]\n"
    "v_exp_f32 v96, v96\n"
    "v_mfma_f32_16x16x32_bf16 v[100:103], a[100:103], a[84:87], v[100:103]\n"
    "ds_read_b64_tr_b16 a[132:133], v129 offset:57600\n"
    "v_mul_f32 v140, s76, v140\n"
    "v_mfma_f32_16x16x32_bf16 v[100:103], a[104:107], a[88:91], v[100:103]\n"
    "v_exp_f32 v97, v97\n"
    "v_mfma_f32_16x16x32_bf16 v[100:103], a[108:111], a[92:95], v[100:103]\n"
    "ds_read_b64_tr_b16 a[134:135], v129 offset:61696\n"
    "v_mul_f32 v141, s76, v141\n"
    "v_mfma_f32_16x16x32_bf16 v[188:191], a[136:139], v[80:83], v[188:191]\n"
    "v_exp_f32 v98, v98\n"
    "v_mfma_f32_16x16x32_bf16 v[188:191], a[140:143], v[84:87], v[188:191]\n"
    "v_cvt_pk_bf16_f32 v32, v96, v97\n"
    "v_cvt_pk_bf16_f32 v138, v140, v141\n"
    "v_mfma_f32_16x16x32_bf16 v[192:195], a[140:143], v[92:95], v[192:195]\n"
    "v_exp_f32 v99, v99\n"
    "v_mfma_f32_16x16x32_bf16 v[192:195], a[136:139], v[88:91], v[192:195]\n"
    "s_waitcnt lgkmcnt(4)\n"
    "v_cvt_pk_bf16_f32 v33, v98, v99\n"
    "v_lshlrev_b32 v142, 16, v139\n"
    "v_mfma_f32_16x16x32_bf16 v[96:99], a[112:115], a[0:3], v[104:107]\n"
    "s_mov_b32 m0, s63\n"
    "v_exp_f32 v100, v100\n"
    "v_mfma_f32_16x16x32_bf16 v[96:99], a[116:119], a[4:7], v[96:99]\n"
    "s_add_u32 s56, s56, 16384\n"
    "buffer_load_dwordx4 v131, s[36:39], s58 offen lds\n"
    "v_mfma_f32_16x16x32_bf16 v[96:99], a[120:123], a[8:11], v[96:99]\n"
    "s_and_b32 s56, s56, 262143\n"
    "v_exp_f32 v101, v101\n"
    "v_mfma_f32_16x16x32_bf16 v[96:99], a[124:127], a[12:15], v[96:99]\n"
    "s_waitcnt lgkmcnt(0)\n"
    "v_and_b32 v143, s68, v139\n"
    "v_mul_f32 v142, s76, v142\n"
    "v_mfma_f32_16x16x32_bf16 v[196:199], a[128:131], v[48:51], v[196:199]\n"
    "s_or_b32 s56, s56, s61\n"
    "v_exp_f32 v102, v102\n"
    "v_mfma_f32_16x16x32_bf16 v[196:199], a[132:135], v[52:55], v[196:199]\n"
    "s_add_u32 s94, s56, 8192\n"
    "v_cvt_pk_bf16_f32 v40, v100, v101\n"
    "v_mul_f32 v143, s76, v143\n"
    "v_mfma_f32_16x16x32_bf16 v[200:203], a[132:135], v[60:63], v[200:203]\n"
    "s_add_u32 s57, s57, 16384\n"
    "v_exp_f32 v103, v103\n"
    "v_mfma_f32_16x16x32_bf16 v[200:203], a[128:131], v[56:59], v[200:203]\n"
    "s_and_b32 s57, s57, 262143\n"
    "v_cvt_pk_bf16_f32 v41, v102, v103\n"
    "v_cvt_pk_bf16_f32 v139, v142, v143\n"
    "v_mfma_f32_16x16x32_bf16 v[100:103], a[112:115], a[16:19], v[108:111]\n"
    "s_or_b32 s57, s57, s96\n"
    "v_exp_f32 v96, v96\n"
    "v_mfma_f32_16x16x32_bf16 v[100:103], a[116:119], a[20:23], v[100:103]\n"
    "s_add_u32 s70, s57, 8192\n"
    "ds_read_b64_tr_b16 a[136:137], v130 offset:49408\n"
    "v_mfma_f32_16x16x32_bf16 v[100:103], a[120:123], a[24:27], v[100:103]\n"
    "v_exp_f32 v97, v97\n"
    "v_mfma_f32_16x16x32_bf16 v[100:103], a[124:127], a[28:31], v[100:103]\n"
    "ds_read_b64_tr_b16 a[138:139], v130 offset:53504\n"
    "v_mfma_f32_16x16x32_bf16 v[204:207], a[128:131], v[64:67], v[204:207]\n"
    "v_exp_f32 v98, v98\n"
    "v_mfma_f32_16x16x32_bf16 v[204:207], a[132:135], v[68:71], v[204:207]\n"
    "v_cvt_pk_bf16_f32 v2, v96, v97\n"
    "v_mfma_f32_16x16x32_bf16 v[208:211], a[132:135], v[76:79], v[208:211]\n"
    "v_exp_f32 v99, v99\n"
    "v_mfma_f32_16x16x32_bf16 v[208:211], a[128:131], v[72:75], v[208:211]\n"
    "v_cvt_pk_bf16_f32 v3, v98, v99\n"
    "v_smfmac_f32_16x16x64_bf16 a[148:151], a[168:171], v[56:63], v133\n"
    "v_mfma_f32_16x16x32_bf16 v[96:99], a[112:115], a[32:35], v[112:115]\n"
    "v_exp_f32 v100, v100\n"
    "v_mfma_f32_16x16x32_bf16 v[96:99], a[116:119], a[36:39], v[96:99]\n"
    "ds_read_b64_tr_b16 a[140:141], v130 offset:57600\n"
    "v_mfma_f32_16x16x32_bf16 v[96:99], a[120:123], a[40:43], v[96:99]\n"
    "v_exp_f32 v101, v101\n"
    "v_mfma_f32_16x16x32_bf16 v[96:99], a[124:127], a[44:47], v[96:99]\n"
    "ds_read_b64_tr_b16 a[142:143], v130 offset:61696\n"
    "v_mfma_f32_16x16x32_bf16 v[212:215], a[128:131], v[80:83], v[212:215]\n"
    "v_exp_f32 v102, v102\n"
    "v_mfma_f32_16x16x32_bf16 v[212:215], a[132:135], v[84:87], v[212:215]\n"
    "v_cvt_pk_bf16_f32 v10, v100, v101\n"
    "v_mfma_f32_16x16x32_bf16 v[216:219], a[132:135], v[92:95], v[216:219]\n"
    "v_exp_f32 v103, v103\n"
    "v_mfma_f32_16x16x32_bf16 v[216:219], a[128:131], v[88:91], v[216:219]\n"
    "v_cvt_pk_bf16_f32 v11, v102, v103\n"
    "v_mfma_f32_16x16x32_bf16 v[100:103], a[112:115], a[48:51], v[116:119]\n"
    "v_exp_f32 v96, v96\n"
    "v_mfma_f32_16x16x32_bf16 v[100:103], a[116:119], a[52:55], v[100:103]\n"
    "ds_write_b64 v134, v[136:137] offset:4096\n"
    "v_mfma_f32_16x16x32_bf16 v[100:103], a[120:123], a[56:59], v[100:103]\n"
    "s_waitcnt vmcnt(11) lgkmcnt(1)\n"
    "v_exp_f32 v97, v97\n"
    "v_mfma_f32_16x16x32_bf16 v[100:103], a[124:127], a[60:63], v[100:103]\n"
    "s_barrier\n"
    "v_mfma_f32_16x16x32_bf16 v[220:223], a[136:139], v[48:51], v[220:223]\n"
    "v_exp_f32 v98, v98\n"
    "v_mfma_f32_16x16x32_bf16 v[220:223], a[140:143], v[52:55], v[220:223]\n"
    "ds_read_b128 a[96:99], v128 offset:8192\n"
    "v_cvt_pk_bf16_f32 v18, v96, v97\n"
    "v_mfma_f32_16x16x32_bf16 v[224:227], a[140:143], v[60:63], v[224:227]\n"
    "v_exp_f32 v99, v99\n"
    "v_mfma_f32_16x16x32_bf16 v[224:227], a[136:139], v[56:59], v[224:227]\n"
    "ds_read_b128 a[100:103], v128 offset:8448\n"
    "v_cvt_pk_bf16_f32 v19, v98, v99\n"
    "v_mfma_f32_16x16x32_bf16 v[96:99], a[112:115], a[64:67], v[120:123]\n"
    "v_exp_f32 v100, v100\n"
    "v_mfma_f32_16x16x32_bf16 v[96:99], a[116:119], a[68:71], v[96:99]\n"
    "ds_read_b64_tr_b16 a[128:129], v129 offset:49664\n"
    "ds_read_b64_tr_b16 a[130:131], v129 offset:53760\n"
    "v_mfma_f32_16x16x32_bf16 v[96:99], a[120:123], a[72:75], v[96:99]\n"
    "s_mov_b32 m0, s80\n"
    "v_exp_f32 v101, v101\n"
    "v_mfma_f32_16x16x32_bf16 v[96:99], a[124:127], a[76:79], v[96:99]\n"
    "buffer_load_dwordx4 v131, s[40:43], s56 offen lds\n"
    "v_mfma_f32_16x16x32_bf16 v[228:231], a[136:139], v[64:67], v[228:231]\n"
    "v_exp_f32 v102, v102\n"
    "v_mfma_f32_16x16x32_bf16 v[228:231], a[140:143], v[68:71], v[228:231]\n"
    "ds_read_b128 a[104:107], v128 offset:8704\n"
    "v_cvt_pk_bf16_f32 v26, v100, v101\n"
    "v_mfma_f32_16x16x32_bf16 v[232:235], a[140:143], v[76:79], v[232:235]\n"
    "v_exp_f32 v103, v103\n"
    "v_mfma_f32_16x16x32_bf16 v[232:235], a[136:139], v[72:75], v[232:235]\n"
    "ds_read_b128 a[108:111], v128 offset:8960\n"
    "v_cvt_pk_bf16_f32 v27, v102, v103\n"
    "v_mfma_f32_16x16x32_bf16 v[100:103], a[112:115], a[80:83], v[124:127]\n"
    "v_exp_f32 v96, v96\n"
    "v_mfma_f32_16x16x32_bf16 v[100:103], a[116:119], a[84:87], v[100:103]\n"
    "ds_read_b64_tr_b16 a[132:133], v129 offset:57856\n"
    "ds_read_b64_tr_b16 a[134:135], v129 offset:61952\n"
    "v_mfma_f32_16x16x32_bf16 v[100:103], a[120:123], a[88:91], v[100:103]\n"
    "v_exp_f32 v97, v97\n"
    "v_mfma_f32_16x16x32_bf16 v[100:103], a[124:127], a[92:95], v[100:103]\n"
    "buffer_load_dwordx4 v131, s[40:43], s56 offen offset:2048 lds\n"
    "v_mfma_f32_16x16x32_bf16 v[236:239], a[136:139], v[80:83], v[236:239]\n"
    "v_exp_f32 v98, v98\n"
    "v_mfma_f32_16x16x32_bf16 v[236:239], a[140:143], v[84:87], v[236:239]\n"
    "v_cvt_pk_bf16_f32 v34, v96, v97\n"
    "v_mfma_f32_16x16x32_bf16 v[240:243], a[140:143], v[92:95], v[240:243]\n"
    "v_exp_f32 v99, v99\n"
    "v_mfma_f32_16x16x32_bf16 v[240:243], a[136:139], v[88:91], v[240:243]\n"
    "s_waitcnt lgkmcnt(2)\n"
    "v_cvt_pk_bf16_f32 v35, v98, v99\n"
    "v_smfmac_f32_16x16x64_bf16 a[152:155], a[168:171], v[64:71], v133\n"
    "ds_write_b64 v134, v[138:139] offset:4104\n"
    "v_mfma_f32_16x16x32_bf16 v[96:99], a[96:99], a[0:3], v[104:107]\n"
    "v_exp_f32 v100, v100\n"
    "v_mfma_f32_16x16x32_bf16 v[96:99], a[100:103], a[4:7], v[96:99]\n"
    "ds_read_b128 v[136:139], v134 offset:5120\n"
    "v_mfma_f32_16x16x32_bf16 v[96:99], a[104:107], a[8:11], v[96:99]\n"
    "v_exp_f32 v101, v101\n"
    "v_mfma_f32_16x16x32_bf16 v[96:99], a[108:111], a[12:15], v[96:99]\n"
    "s_waitcnt lgkmcnt(2)\n"
    "v_mfma_f32_16x16x32_bf16 v[244:247], a[128:131], v[48:51], v[244:247]\n"
    "v_exp_f32 v102, v102\n"
    "v_mfma_f32_16x16x32_bf16 v[244:247], a[132:135], v[52:55], v[244:247]\n"
    "ds_read_b128 a[112:115], v128 offset:12288\n"
    "v_cvt_pk_bf16_f32 v42, v100, v101\n"
    "v_mfma_f32_16x16x32_bf16 v[248:251], a[132:135], v[60:63], v[248:251]\n"
    "v_exp_f32 v103, v103\n"
    "v_mfma_f32_16x16x32_bf16 v[248:251], a[128:131], v[56:59], v[248:251]\n"
    "ds_read_b128 a[116:119], v128 offset:12544\n"
    "v_cvt_pk_bf16_f32 v43, v102, v103\n"
    "v_mfma_f32_16x16x32_bf16 v[100:103], a[96:99], a[16:19], v[108:111]\n"
    "v_exp_f32 v96, v96\n"
    "v_mfma_f32_16x16x32_bf16 v[100:103], a[100:103], a[20:23], v[100:103]\n"
    "ds_read_b64_tr_b16 a[136:137], v130 offset:49664\n"
    "v_mfma_f32_16x16x32_bf16 v[100:103], a[104:107], a[24:27], v[100:103]\n"
    "v_exp_f32 v97, v97\n"
    "v_mfma_f32_16x16x32_bf16 v[100:103], a[108:111], a[28:31], v[100:103]\n"
    "ds_read_b64_tr_b16 a[138:139], v130 offset:53760\n"
    "v_mfma_f32_16x16x32_bf16 v[252:255], a[128:131], v[64:67], v[252:255]\n"
    "s_waitcnt lgkmcnt(4)\n"
    "v_exp_f32 v98, v98\n"
    "v_mfma_f32_16x16x32_bf16 v[252:255], a[132:135], v[68:71], v[252:255]\n"
    "v_cvt_pk_bf16_f32 v4, v96, v97\n"
    "v_lshlrev_b32 v140, 16, v136\n"
    "v_mfma_f32_16x16x32_bf16 a[172:175], a[132:135], v[76:79], a[172:175]\n"
    "v_exp_f32 v99, v99\n"
    "v_mfma_f32_16x16x32_bf16 a[172:175], a[128:131], v[72:75], a[172:175]\n"
    "v_cvt_pk_bf16_f32 v5, v98, v99\n"
    "v_and_b32 v141, s68, v136\n"
    "v_mfma_f32_16x16x32_bf16 v[96:99], a[96:99], a[32:35], v[112:115]\n"
    "v_exp_f32 v100, v100\n"
    "v_mfma_f32_16x16x32_bf16 v[96:99], a[100:103], a[36:39], v[96:99]\n"
    "ds_read_b64_tr_b16 a[140:141], v130 offset:57856\n"
    "v_mul_f32 v140, s76, v140\n"
    "v_mfma_f32_16x16x32_bf16 v[96:99], a[104:107], a[40:43], v[96:99]\n"
    "v_exp_f32 v101, v101\n"
    "v_mfma_f32_16x16x32_bf16 v[96:99], a[108:111], a[44:47], v[96:99]\n"
    "ds_read_b64_tr_b16 a[142:143], v130 offset:61952\n"
    "v_mul_f32 v141, s76, v141\n"
    "v_mfma_f32_16x16x32_bf16 a[176:179], a[128:131], v[80:83], a[176:179]\n"
    "v_exp_f32 v102, v102\n"
    "v_mfma_f32_16x16x32_bf16 a[176:179], a[132:135], v[84:87], a[176:179]\n"
    "v_cvt_pk_bf16_f32 v12, v100, v101\n"
    "v_cvt_pk_bf16_f32 v136, v140, v141\n"
    "v_mfma_f32_16x16x32_bf16 a[180:183], a[132:135], v[92:95], a[180:183]\n"
    "v_exp_f32 v103, v103\n"
    "v_mfma_f32_16x16x32_bf16 a[180:183], a[128:131], v[88:91], a[180:183]\n"
    "v_cvt_pk_bf16_f32 v13, v102, v103\n"
    "v_lshlrev_b32 v142, 16, v137\n"
    "v_mfma_f32_16x16x32_bf16 v[100:103], a[96:99], a[48:51], v[116:119]\n"
    "s_mov_b32 m0, s63\n"
    "v_exp_f32 v96, v96\n"
    "v_mfma_f32_16x16x32_bf16 v[100:103], a[100:103], a[52:55], v[100:103]\n"
    "buffer_load_dwordx4 v131, s[36:39], s58 offen offset:1024 lds\n"
    "v_mfma_f32_16x16x32_bf16 v[100:103], a[104:107], a[56:59], v[100:103]\n"
    "v_exp_f32 v97, v97\n"
    "v_mfma_f32_16x16x32_bf16 v[100:103], a[108:111], a[60:63], v[100:103]\n"
    "s_waitcnt lgkmcnt(0)\n"
    "v_and_b32 v143, s68, v137\n"
    "v_mul_f32 v142, s76, v142\n"
    "v_mfma_f32_16x16x32_bf16 a[184:187], a[136:139], v[48:51], a[184:187]\n"
    "v_exp_f32 v98, v98\n"
    "v_mfma_f32_16x16x32_bf16 a[184:187], a[140:143], v[52:55], a[184:187]\n"
    "ds_read_b128 a[120:123], v128 offset:12800\n"
    "v_cvt_pk_bf16_f32 v20, v96, v97\n"
    "v_mfma_f32_16x16x32_bf16 a[188:191], a[140:143], v[60:63], a[188:191]\n"
    "v_exp_f32 v99, v99\n"
    "v_mfma_f32_16x16x32_bf16 a[188:191], a[136:139], v[56:59], a[188:191]\n"
    "ds_read_b128 a[124:127], v128 offset:13056\n"
    "v_cvt_pk_bf16_f32 v21, v98, v99\n"
    "v_smfmac_f32_16x16x64_bf16 a[156:159], a[168:171], v[72:79], v133\n"
    "v_mul_f32 v143, s76, v143\n"
    "v_cvt_pk_bf16_f32 v137, v142, v143\n"
    "v_mfma_f32_16x16x32_bf16 v[96:99], a[96:99], a[64:67], v[120:123]\n"
    "s_sub_u32 s58, s58, 16384\n"
    "v_exp_f32 v100, v100\n"
    "v_mfma_f32_16x16x32_bf16 v[96:99], a[100:103], a[68:71], v[96:99]\n"
    "s_sub_u32 s63, s63, 4096\n"
    "ds_read_b64_tr_b16 a[128:129], v129 offset:49920\n"
    "v_lshlrev_b32 v140, 16, v138\n"
    "v_mfma_f32_16x16x32_bf16 v[96:99], a[104:107], a[72:75], v[96:99]\n"
    "s_cmp_eq_u32 s60, 5\n"
    "v_exp_f32 v101, v101\n"
    "v_mfma_f32_16x16x32_bf16 v[96:99], a[108:111], a[76:79], v[96:99]\n"
    "s_cselect_b32 s38, 0, s38\n"
    "ds_read_b64_tr_b16 a[130:131], v129 offset:54016\n"
    "v_and_b32 v141, s68, v138\n"
    "v_mfma_f32_16x16x32_bf16 a[192:195], a[136:139], v[64:67], a[192:195]\n"
    "s_cselect_b32 s63, s88, s63\n"
    "v_exp_f32 v102, v102\n"
    "v_mfma_f32_16x16x32_bf16 a[192:195], a[140:143], v[68:71], a[192:195]\n"
    "v_cvt_pk_bf16_f32 v28, v100, v101\n"
    "v_mul_f32 v140, s76, v140\n"
    "v_mfma_f32_16x16x32_bf16 a[196:199], a[140:143], v[76:79], a[196:199]\n"
    "v_exp_f32 v103, v103\n"
    "v_mfma_f32_16x16x32_bf16 a[196:199], a[136:139], v[72:75], a[196:199]\n"
    "v_cvt_pk_bf16_f32 v29, v102, v103\n"
    "v_mul_f32 v141, s76, v141\n"
    "v_mfma_f32_16x16x32_bf16 v[100:103], a[96:99], a[80:83], v[124:127]\n"
    "v_exp_f32 v96, v96\n"
    "v_mfma_f32_16x16x32_bf16 v[100:103], a[100:103], a[84:87], v[100:103]\n"
    "ds_read_b64_tr_b16 a[132:133], v129 offset:58112\n"
    "v_cvt_pk_bf16_f32 v138, v140, v141\n"
    "v_mfma_f32_16x16x32_bf16 v[100:103], a[104:107], a[88:91], v[100:103]\n"
    "v_exp_f32 v97, v97\n"
    "v_mfma_f32_16x16x32_bf16 v[100:103], a[108:111], a[92:95], v[100:103]\n"
    "ds_read_b64_tr_b16 a[134:135], v129 offset:62208\n"
    "v_lshlrev_b32 v142, 16, v139\n"
    "v_mfma_f32_16x16x32_bf16 a[200:203], a[136:139], v[80:83], a[200:203]\n"
    "v_exp_f32 v98, v98\n"
    "v_mfma_f32_16x16x32_bf16 a[200:203], a[140:143], v[84:87], a[200:203]\n"
    "v_cvt_pk_bf16_f32 v36, v96, v97\n"
    "v_and_b32 v143, s68, v139\n"
    "v_mfma_f32_16x16x32_bf16 a[204:207], a[140:143], v[92:95], a[204:207]\n"
    "v_exp_f32 v99, v99\n"
    "v_mfma_f32_16x16x32_bf16 a[204:207], a[136:139], v[88:91], a[204:207]\n"
    "s_waitcnt lgkmcnt(4)\n"
    "v_cvt_pk_bf16_f32 v37, v98, v99\n"
    "v_mul_f32 v142, s76, v142\n"
    "v_mfma_f32_16x16x32_bf16 v[96:99], a[112:115], a[0:3], v[104:107]\n"
    "v_exp_f32 v100, v100\n"
    "v_mfma_f32_16x16x32_bf16 v[96:99], a[116:119], a[4:7], v[96:99]\n"
    "v_mul_f32 v143, s76, v143\n"
    "v_cvt_pk_bf16_f32 v139, v142, v143\n"
    "v_mfma_f32_16x16x32_bf16 v[96:99], a[120:123], a[8:11], v[96:99]\n"
    "v_exp_f32 v101, v101\n"
    "v_mfma_f32_16x16x32_bf16 v[96:99], a[124:127], a[12:15], v[96:99]\n"
    "s_waitcnt lgkmcnt(0)\n"
    "v_mfma_f32_16x16x32_bf16 a[208:211], a[128:131], v[48:51], a[208:211]\n"
    "v_exp_f32 v102, v102\n"
    "v_mfma_f32_16x16x32_bf16 a[208:211], a[132:135], v[52:55], a[208:211]\n"
    "v_cvt_pk_bf16_f32 v44, v100, v101\n"
    "v_mfma_f32_16x16x32_bf16 a[212:215], a[132:135], v[60:63], a[212:215]\n"
    "v_exp_f32 v103, v103\n"
    "v_mfma_f32_16x16x32_bf16 a[212:215], a[128:131], v[56:59], a[212:215]\n"
    "v_cvt_pk_bf16_f32 v45, v102, v103\n"
    "v_mfma_f32_16x16x32_bf16 v[100:103], a[112:115], a[16:19], v[108:111]\n"
    "v_exp_f32 v96, v96\n"
    "v_mfma_f32_16x16x32_bf16 v[100:103], a[116:119], a[20:23], v[100:103]\n"
    "ds_read_b64_tr_b16 a[136:137], v130 offset:49920\n"
    "v_mfma_f32_16x16x32_bf16 v[100:103], a[120:123], a[24:27], v[100:103]\n"
    "v_exp_f32 v97, v97\n"
    "v_mfma_f32_16x16x32_bf16 v[100:103], a[124:127], a[28:31], v[100:103]\n"
    "ds_read_b64_tr_b16 a[138:139], v130 offset:54016\n"
    "v_mfma_f32_16x16x32_bf16 a[216:219], a[128:131], v[64:67], a[216:219]\n"
    "v_exp_f32 v98, v98\n"
    "v_mfma_f32_16x16x32_bf16 a[216:219], a[132:135], v[68:71], a[216:219]\n"
    "v_cvt_pk_bf16_f32 v6, v96, v97\n"
    "v_mfma_f32_16x16x32_bf16 a[220:223], a[132:135], v[76:79], a[220:223]\n"
    "v_exp_f32 v99, v99\n"
    "v_mfma_f32_16x16x32_bf16 a[220:223], a[128:131], v[72:75], a[220:223]\n"
    "v_cvt_pk_bf16_f32 v7, v98, v99\n"
    "v_smfmac_f32_16x16x64_bf16 a[160:163], a[168:171], v[80:87], v133\n"
    "ds_write_b64 v134, v[136:137] offset:5120\n"
    "v_mfma_f32_16x16x32_bf16 v[96:99], a[112:115], a[32:35], v[112:115]\n"
    "v_exp_f32 v100, v100\n"
    "v_mfma_f32_16x16x32_bf16 v[96:99], a[116:119], a[36:39], v[96:99]\n"
    "ds_read_b64_tr_b16 a[140:141], v130 offset:58112\n"
    "v_mfma_f32_16x16x32_bf16 v[96:99], a[120:123], a[40:43], v[96:99]\n"
    "v_exp_f32 v101, v101\n"
    "v_mfma_f32_16x16x32_bf16 v[96:99], a[124:127], a[44:47], v[96:99]\n"
    "ds_read_b64_tr_b16 a[142:143], v130 offset:62208\n"
    "v_mfma_f32_16x16x32_bf16 a[224:227], a[128:131], v[80:83], a[224:227]\n"
    "v_exp_f32 v102, v102\n"
    "v_mfma_f32_16x16x32_bf16 a[224:227], a[132:135], v[84:87], a[224:227]\n"
    "v_cvt_pk_bf16_f32 v14, v100, v101\n"
    "v_mfma_f32_16x16x32_bf16 a[228:231], a[132:135], v[92:95], a[228:231]\n"
    "v_exp_f32 v103, v103\n"
    "v_mfma_f32_16x16x32_bf16 a[228:231], a[128:131], v[88:91], a[228:231]\n"
    "v_cvt_pk_bf16_f32 v15, v102, v103\n"
    "v_mfma_f32_16x16x32_bf16 v[100:103], a[112:115], a[48:51], v[116:119]\n"
    "v_exp_f32 v96, v96\n"
    "v_mfma_f32_16x16x32_bf16 v[100:103], a[116:119], a[52:55], v[100:103]\n"
    "ds_write_b64 v134, v[138:139] offset:5128\n"
    "v_mfma_f32_16x16x32_bf16 v[100:103], a[120:123], a[56:59], v[100:103]\n"
    "s_waitcnt vmcnt(6) lgkmcnt(1)\n"
    "v_exp_f32 v97, v97\n"
    "v_mfma_f32_16x16x32_bf16 v[100:103], a[124:127], a[60:63], v[100:103]\n"
    "s_barrier\n"
    "v_mfma_f32_16x16x32_bf16 a[232:235], a[136:139], v[48:51], a[232:235]\n"
    "v_exp_f32 v98, v98\n"
    "v_mfma_f32_16x16x32_bf16 a[232:235], a[140:143], v[52:55], a[232:235]\n"
    "ds_read_b128 a[96:99], v128 offset:16384\n"
    "v_cvt_pk_bf16_f32 v22, v96, v97\n"
    "v_mfma_f32_16x16x32_bf16 a[236:239], a[140:143], v[60:63], a[236:239]\n"
    "v_exp_f32 v99, v99\n"
    "v_mfma_f32_16x16x32_bf16 a[236:239], a[136:139], v[56:59], a[236:239]\n"
    "ds_read_b128 a[100:103], v128 offset:16640\n"
    "v_cvt_pk_bf16_f32 v23, v98, v99\n"
    "v_mfma_f32_16x16x32_bf16 v[96:99], a[112:115], a[64:67], v[120:123]\n"
    "v_exp_f32 v100, v100\n"
    "v_mfma_f32_16x16x32_bf16 v[96:99], a[116:119], a[68:71], v[96:99]\n"
    "ds_read_b64_tr_b16 a[128:129], v129 offset:32768\n"
    "ds_read_b64_tr_b16 a[130:131], v129 offset:36864\n"
    "v_mfma_f32_16x16x32_bf16 v[96:99], a[120:123], a[72:75], v[96:99]\n"
    "s_mov_b32 m0, s86\n"
    "v_exp_f32 v101, v101\n"
    "v_mfma_f32_16x16x32_bf16 v[96:99], a[124:127], a[76:79], v[96:99]\n"
    "buffer_load_dwordx4 v131, s[44:47], s57 offen lds\n"
    "v_mfma_f32_16x16x32_bf16 a[240:243], a[136:139], v[64:67], a[240:243]\n"
    "v_exp_f32 v102, v102\n"
    "v_mfma_f32_16x16x32_bf16 a[240:243], a[140:143], v[68:71], a[240:243]\n"
    "ds_read_b128 a[104:107], v128 offset:16896\n"
    "v_cvt_pk_bf16_f32 v30, v100, v101\n"
    "v_mfma_f32_16x16x32_bf16 a[244:247], a[140:143], v[76:79], a[244:247]\n"
    "v_exp_f32 v103, v103\n"
    "v_mfma_f32_16x16x32_bf16 a[244:247], a[136:139], v[72:75], a[244:247]\n"
    "ds_read_b128 a[108:111], v128 offset:17152\n"
    "v_cvt_pk_bf16_f32 v31, v102, v103\n"
    "v_mfma_f32_16x16x32_bf16 v[100:103], a[112:115], a[80:83], v[124:127]\n"
    "v_exp_f32 v96, v96\n"
    "v_mfma_f32_16x16x32_bf16 v[100:103], a[116:119], a[84:87], v[100:103]\n"
    "ds_read_b64_tr_b16 a[132:133], v129 offset:40960\n"
    "ds_read_b64_tr_b16 a[134:135], v129 offset:45056\n"
    "v_mfma_f32_16x16x32_bf16 v[100:103], a[120:123], a[88:91], v[100:103]\n"
    "v_exp_f32 v97, v97\n"
    "v_mfma_f32_16x16x32_bf16 v[100:103], a[124:127], a[92:95], v[100:103]\n"
    "buffer_load_dwordx4 v131, s[44:47], s57 offen offset:2048 lds\n"
    "v_mfma_f32_16x16x32_bf16 a[248:251], a[136:139], v[80:83], a[248:251]\n"
    "v_exp_f32 v98, v98\n"
    "v_mfma_f32_16x16x32_bf16 a[248:251], a[140:143], v[84:87], a[248:251]\n"
    "v_cvt_pk_bf16_f32 v38, v96, v97\n"
    "v_mfma_f32_16x16x32_bf16 a[252:255], a[140:143], v[92:95], a[252:255]\n"
    "v_exp_f32 v99, v99\n"
    "v_mfma_f32_16x16x32_bf16 a[252:255], a[136:139], v[88:91], a[252:255]\n"
    "s_waitcnt lgkmcnt(2)\n"
    "v_cvt_pk_bf16_f32 v39, v98, v99\n"
    "v_smfmac_f32_16x16x64_bf16 a[164:167], a[168:171], v[88:95], v133\n"
    "v_mfma_f32_16x16x32_bf16 v[96:99], a[96:99], a[0:3], v[104:107]\n"
    "s_mov_b32 m0, s87\n"
    "v_exp_f32 v100, v100\n"
    "v_mfma_f32_16x16x32_bf16 v[96:99], a[100:103], a[4:7], v[96:99]\n"
    "buffer_load_dwordx4 v131, s[44:47], s70 offen lds\n"
    "v_mfma_f32_16x16x32_bf16 v[96:99], a[104:107], a[8:11], v[96:99]\n"
    "v_exp_f32 v101, v101\n"
    "v_mfma_f32_16x16x32_bf16 v[96:99], a[108:111], a[12:15], v[96:99]\n"
    "s_waitcnt lgkmcnt(0)\n"
    "ds_read_b128 v[136:139], v134 offset:2048\n"
    "v_mfma_f32_16x16x32_bf16 v[148:151], a[128:131], v[0:3], v[148:151]\n"
    "v_exp_f32 v102, v102\n"
    "v_mfma_f32_16x16x32_bf16 v[148:151], a[132:135], v[4:7], v[148:151]\n"
    "ds_read_b128 a[112:115], v128 offset:20480\n"
    "v_cvt_pk_bf16_f32 v46, v100, v101\n"
    "v_mfma_f32_16x16x32_bf16 v[152:155], a[132:135], v[12:15], v[152:155]\n"
    "v_exp_f32 v103, v103\n"
    "v_mfma_f32_16x16x32_bf16 v[152:155], a[128:131], v[8:11], v[152:155]\n"
    "ds_read_b128 a[116:119], v128 offset:20736\n"
    "v_cvt_pk_bf16_f32 v47, v102, v103\n"
    "v_mfma_f32_16x16x32_bf16 v[100:103], a[96:99], a[16:19], v[108:111]\n"
    "v_exp_f32 v96, v96\n"
    "v_mfma_f32_16x16x32_bf16 v[100:103], a[100:103], a[20:23], v[100:103]\n"
    "ds_read_b64_tr_b16 a[136:137], v130 offset:32768\n"
    "ds_read_b64_tr_b16 a[138:139], v130 offset:36864\n"
    "v_mfma_f32_16x16x32_bf16 v[100:103], a[104:107], a[24:27], v[100:103]\n"
    "v_exp_f32 v97, v97\n"
    "v_mfma_f32_16x16x32_bf16 v[100:103], a[108:111], a[28:31], v[100:103]\n"
    "buffer_load_dwordx4 v131, s[44:47], s70 offen offset:2048 lds\n"
    "v_mfma_f32_16x16x32_bf16 v[156:159], a[128:131], v[16:19], v[156:159]\n"
    "s_waitcnt lgkmcnt(4)\n"
    "v_exp_f32 v98, v98\n"
    "v_mfma_f32_16x16x32_bf16 v[156:159], a[132:135], v[20:23], v[156:159]\n"
    "v_cvt_pk_bf16_f32 v48, v96, v97\n"
    "v_lshlrev_b32 v140, 16, v136\n"
    "v_mfma_f32_16x16x32_bf16 v[160:163], a[132:135], v[28:31], v[160:163]\n"
    "v_exp_f32 v99, v99\n"
    "v_mfma_f32_16x16x32_bf16 v[160:163], a[128:131], v[24:27], v[160:163]\n"
    "v_cvt_pk_bf16_f32 v49, v98, v99\n"
    "v_and_b32 v141, s68, v136\n"
    "v_mfma_f32_16x16x32_bf16 v[96:99], a[96:99], a[32:35], v[112:115]\n"
    "v_exp_f32 v100, v100\n"
    "v_mfma_f32_16x16x32_bf16 v[96:99], a[100:103], a[36:39], v[96:99]\n"
    "ds_read_b64_tr_b16 a[140:141], v130 offset:40960\n"
    "ds_read_b64_tr_b16 a[142:143], v130 offset:45056\n"
    "v_mfma_f32_16x16x32_bf16 v[96:99], a[104:107], a[40:43], v[96:99]\n"
    "s_mov_b32 m0, s81\n"
    "v_exp_f32 v101, v101\n"
    "v_mfma_f32_16x16x32_bf16 v[96:99], a[108:111], a[44:47], v[96:99]\n"
    "buffer_load_dwordx4 v131, s[40:43], s94 offen lds\n"
    "v_mfma_f32_16x16x32_bf16 v[164:167], a[128:131], v[32:35], v[164:167]\n"
    "v_exp_f32 v102, v102\n"
    "v_mfma_f32_16x16x32_bf16 v[164:167], a[132:135], v[36:39], v[164:167]\n"
    "v_cvt_pk_bf16_f32 v56, v100, v101\n"
    "v_mul_f32 v140, s76, v140\n"
    "v_mfma_f32_16x16x32_bf16 v[168:171], a[132:135], v[44:47], v[168:171]\n"
    "v_exp_f32 v103, v103\n"
    "v_mfma_f32_16x16x32_bf16 v[168:171], a[128:131], v[40:43], v[168:171]\n"
    "v_cvt_pk_bf16_f32 v57, v102, v103\n"
    "v_mul_f32 v141, s76, v141\n"
    "v_mfma_f32_16x16x32_bf16 v[100:103], a[96:99], a[48:51], v[116:119]\n"
    "v_exp_f32 v96, v96\n"
    "v_mfma_f32_16x16x32_bf16 v[100:103], a[100:103], a[52:55], v[100:103]\n"
    "buffer_load_dwordx4 v131, s[40:43], s94 offen offset:2048 lds\n"
    "v_mfma_f32_16x16x32_bf16 v[100:103], a[104:107], a[56:59], v[100:103]\n"
    "v_exp_f32 v97, v97\n"
    "v_mfma_f32_16x16x32_bf16 v[100:103], a[108:111], a[60:63], v[100:103]\n"
    "s_waitcnt lgkmcnt(0)\n"
    "v_cvt_pk_bf16_f32 v136, v140, v141\n"
    "v_lshlrev_b32 v142, 16, v137\n"
    "v_mfma_f32_16x16x32_bf16 v[172:175], a[136:139], v[0:3], v[172:175]\n"
    "v_exp_f32 v98, v98\n"
    "v_mfma_f32_16x16x32_bf16 v[172:175], a[140:143], v[4:7], v[172:175]\n"
    "ds_read_b128 a[120:123], v128 offset:20992\n"
    "v_cvt_pk_bf16_f32 v64, v96, v97\n"
    "v_mfma_f32_16x16x32_bf16 v[176:179], a[140:143], v[12:15], v[176:179]\n"
    "v_exp_f32 v99, v99\n"
    "v_mfma_f32_16x16x32_bf16 v[176:179], a[136:139], v[8:11], v[176:179]\n"
    "ds_read_b128 a[124:127], v128 offset:21248\n"
    "v_cvt_pk_bf16_f32 v65, v98, v99\n"
    "v_smfmac_f32_16x16x64_bf16 a[144:147], a[168:171], v[0:7], v133\n"
    "v_and_b32 v143, s68, v137\n"
    "v_mul_f32 v142, s76, v142\n"
    "v_mfma_f32_16x16x32_bf16 v[96:99], a[96:99], a[64:67], v[120:123]\n"
    "v_exp_f32 v100, v100\n"
    "v_mfma_f32_16x16x32_bf16 v[96:99], a[100:103], a[68:71], v[96:99]\n"
    "ds_read_b64_tr_b16 a[128:129], v129 offset:33024\n"
    "v_mul_f32 v143, s76, v143\n"
    "v_mfma_f32_16x16x32_bf16 v[96:99], a[104:107], a[72:75], v[96:99]\n"
    "v_exp_f32 v101, v101\n"
    "v_mfma_f32_16x16x32_bf16 v[96:99], a[108:111], a[76:79], v[96:99]\n"
    "ds_read_b64_tr_b16 a[130:131], v129 offset:37120\n"
    "v_cvt_pk_bf16_f32 v137, v142, v143\n"
    "v_mfma_f32_16x16x32_bf16 v[180:183], a[136:139], v[16:19], v[180:183]\n"
    "v_exp_f32 v102, v102\n"
    "v_mfma_f32_16x16x32_bf16 v[180:183], a[140:143], v[20:23], v[180:183]\n"
    "v_cvt_pk_bf16_f32 v72, v100, v101\n"
    "v_lshlrev_b32 v140, 16, v138\n"
    "v_mfma_f32_16x16x32_bf16 v[184:187], a[140:143], v[28:31], v[184:187]\n"
    "v_exp_f32 v103, v103\n"
    "v_mfma_f32_16x16x32_bf16 v[184:187], a[136:139], v[24:27], v[184:187]\n"
    "v_cvt_pk_bf16_f32 v73, v102, v103\n"
    "v_and_b32 v141, s68, v138\n"
    "v_mfma_f32_16x16x32_bf16 v[100:103], a[96:99], a[80:83], v[124:127]\n"
    "v_exp_f32 v96, v96\n"
    "v_mfma_f32_16x16x32_bf16 v[100:103], a[100:103], a[84:87], v[100:103]\n"
    "ds_read_b64_tr_b16 a[132:133], v129 offset:41216\n"
    "v_mul_f32 v140, s76, v140\n"
    "v_mfma_f32_16x16x32_bf16 v[100:103], a[104:107], a[88:91], v[100:103]\n"
    "v_exp_f32 v97, v97\n"
    "v_mfma_f32_16x16x32_bf16 v[100:103], a[108:111], a[92:95], v[100:103]\n"
    "ds_read_b64_tr_b16 a[134:135], v129 offset:45312\n"
    "v_mul_f32 v141, s76, v141\n"
    "v_mfma_f32_16x16x32_bf16 v[188:191], a[136:139], v[32:35], v[188:191]\n"
    "v_exp_f32 v98, v98\n"
    "v_mfma_f32_16x16x32_bf16 v[188:191], a[140:143], v[36:39], v[188:191]\n"
    "v_cvt_pk_bf16_f32 v80, v96, v97\n"
    "v_cvt_pk_bf16_f32 v138, v140, v141\n"
    "v_mfma_f32_16x16x32_bf16 v[192:195], a[140:143], v[44:47], v[192:195]\n"
    "v_exp_f32 v99, v99\n"
    "v_mfma_f32_16x16x32_bf16 v[192:195], a[136:139], v[40:43], v[192:195]\n"
    "s_waitcnt lgkmcnt(4)\n"
    "v_cvt_pk_bf16_f32 v81, v98, v99\n"
    "v_lshlrev_b32 v142, 16, v139\n"
    "v_mfma_f32_16x16x32_bf16 v[96:99], a[112:115], a[0:3], v[104:107]\n"
    "s_mov_b32 m0, s63\n"
    "v_exp_f32 v100, v100\n"
    "v_mfma_f32_16x16x32_bf16 v[96:99], a[116:119], a[4:7], v[96:99]\n"
    "s_add_u32 s56, s56, 16384\n"
    "buffer_load_dwordx4 v131, s[36:39], s58 offen offset:2048 lds\n"
    "v_mfma_f32_16x16x32_bf16 v[96:99], a[120:123], a[8:11], v[96:99]\n"
    "s_and_b32 s56, s56, 262143\n"
    "v_exp_f32 v101, v101\n"
    "v_mfma_f32_16x16x32_bf16 v[96:99], a[124:127], a[12:15], v[96:99]\n"
    "s_waitcnt lgkmcnt(0)\n"
    "v_and_b32 v143, s68, v139\n"
    "v_mul_f32 v142, s76, v142\n"
    "v_mfma_f32_16x16x32_bf16 v[196:199], a[128:131], v[0:3], v[196:199]\n"
    "s_or_b32 s56, s56, s61\n"
    "v_exp_f32 v102, v102\n"
    "v_mfma_f32_16x16x32_bf16 v[196:199], a[132:135], v[4:7], v[196:199]\n"
    "s_add_u32 s94, s56, 8192\n"
    "v_cvt_pk_bf16_f32 v88, v100, v101\n"
    "v_mul_f32 v143, s76, v143\n"
    "v_mfma_f32_16x16x32_bf16 v[200:203], a[132:135], v[12:15], v[200:203]\n"
    "s_add_u32 s57, s57, 16384\n"
    "v_exp_f32 v103, v103\n"
    "v_mfma_f32_16x16x32_bf16 v[200:203], a[128:131], v[8:11], v[200:203]\n"
    "s_and_b32 s57, s57, 262143\n"
    "v_cvt_pk_bf16_f32 v89, v102, v103\n"
    "v_cvt_pk_bf16_f32 v139, v142, v143\n"
    "v_mfma_f32_16x16x32_bf16 v[100:103], a[112:115], a[16:19], v[108:111]\n"
    "s_or_b32 s57, s57, s96\n"
    "v_exp_f32 v96, v96\n"
    "v_mfma_f32_16x16x32_bf16 v[100:103], a[116:119], a[20:23], v[100:103]\n"
    "s_add_u32 s70, s57, 8192\n"
    "ds_read_b64_tr_b16 a[136:137], v130 offset:33024\n"
    "v_mfma_f32_16x16x32_bf16 v[100:103], a[120:123], a[24:27], v[100:103]\n"
    "v_exp_f32 v97, v97\n"
    "v_mfma_f32_16x16x32_bf16 v[100:103], a[124:127], a[28:31], v[100:103]\n"
    "ds_read_b64_tr_b16 a[138:139], v130 offset:37120\n"
    "v_mfma_f32_16x16x32_bf16 v[204:207], a[128:131], v[16:19], v[204:207]\n"
    "v_exp_f32 v98, v98\n"
    "v_mfma_f32_16x16x32_bf16 v[204:207], a[132:135], v[20:23], v[204:207]\n"
    "v_cvt_pk_bf16_f32 v50, v96, v97\n"
    "v_mfma_f32_16x16x32_bf16 v[208:211], a[132:135], v[28:31], v[208:211]\n"
    "v_exp_f32 v99, v99\n"
    "v_mfma_f32_16x16x32_bf16 v[208:211], a[128:131], v[24:27], v[208:211]\n"
    "v_cvt_pk_bf16_f32 v51, v98, v99\n"
    "v_smfmac_f32_16x16x64_bf16 a[148:151], a[168:171], v[8:15], v133\n"
    "v_mfma_f32_16x16x32_bf16 v[96:99], a[112:115], a[32:35], v[112:115]\n"
    "v_exp_f32 v100, v100\n"
    "v_mfma_f32_16x16x32_bf16 v[96:99], a[116:119], a[36:39], v[96:99]\n"
    "ds_read_b64_tr_b16 a[140:141], v130 offset:41216\n"
    "v_mfma_f32_16x16x32_bf16 v[96:99], a[120:123], a[40:43], v[96:99]\n"
    "v_exp_f32 v101, v101\n"
    "v_mfma_f32_16x16x32_bf16 v[96:99], a[124:127], a[44:47], v[96:99]\n"
    "ds_read_b64_tr_b16 a[142:143], v130 offset:45312\n"
    "v_mfma_f32_16x16x32_bf16 v[212:215], a[128:131], v[32:35], v[212:215]\n"
    "v_exp_f32 v102, v102\n"
    "v_mfma_f32_16x16x32_bf16 v[212:215], a[132:135], v[36:39], v[212:215]\n"
    "v_cvt_pk_bf16_f32 v58, v100, v101\n"
    "v_mfma_f32_16x16x32_bf16 v[216:219], a[132:135], v[44:47], v[216:219]\n"
    "v_exp_f32 v103, v103\n"
    "v_mfma_f32_16x16x32_bf16 v[216:219], a[128:131], v[40:43], v[216:219]\n"
    "v_cvt_pk_bf16_f32 v59, v102, v103\n"
    "v_mfma_f32_16x16x32_bf16 v[100:103], a[112:115], a[48:51], v[116:119]\n"
    "v_exp_f32 v96, v96\n"
    "v_mfma_f32_16x16x32_bf16 v[100:103], a[116:119], a[52:55], v[100:103]\n"
    "ds_write_b64 v134, v[136:137] offset:2048\n"
    "v_mfma_f32_16x16x32_bf16 v[100:103], a[120:123], a[56:59], v[100:103]\n"
    "s_waitcnt vmcnt(11) lgkmcnt(1)\n"
    "v_exp_f32 v97, v97\n"
    "v_mfma_f32_16x16x32_bf16 v[100:103], a[124:127], a[60:63], v[100:103]\n"
    "s_barrier\n"
    "v_mfma_f32_16x16x32_bf16 v[220:223], a[136:139], v[0:3], v[220:223]\n"
    "v_exp_f32 v98, v98\n"
    "v_mfma_f32_16x16x32_bf16 v[220:223], a[140:143], v[4:7], v[220:223]\n"
    "ds_read_b128 a[96:99], v128 offset:24576\n"
    "v_cvt_pk_bf16_f32 v66, v96, v97\n"
    "v_mfma_f32_16x16x32_bf16 v[224:227], a[140:143], v[12:15], v[224:227]\n"
    "v_exp_f32 v99, v99\n"
    "v_mfma_f32_16x16x32_bf16 v[224:227], a[136:139], v[8:11], v[224:227]\n"
    "ds_read_b128 a[100:103], v128 offset:24832\n"
    "v_cvt_pk_bf16_f32 v67, v98, v99\n"
    "v_mfma_f32_16x16x32_bf16 v[96:99], a[112:115], a[64:67], v[120:123]\n"
    "v_exp_f32 v100, v100\n"
    "v_mfma_f32_16x16x32_bf16 v[96:99], a[116:119], a[68:71], v[96:99]\n"
    "ds_read_b64_tr_b16 a[128:129], v129 offset:33280\n"
    "ds_read_b64_tr_b16 a[130:131], v129 offset:37376\n"
    "v_mfma_f32_16x16x32_bf16 v[96:99], a[120:123], a[72:75], v[96:99]\n"
    "s_mov_b32 m0, s82\n"
    "v_exp_f32 v101, v101\n"
    "v_mfma_f32_16x16x32_bf16 v[96:99], a[124:127], a[76:79], v[96:99]\n"
    "buffer_load_dwordx4 v131, s[40:43], s56 offen lds\n"
    "v_mfma_f32_16x16x32_bf16 v[228:231], a[136:139], v[16:19], v[228:231]\n"
    "v_exp_f32 v102, v102\n"
    "v_mfma_f32_16x16x32_bf16 v[228:231], a[140:143], v[20:23], v[228:231]\n"
    "ds_read_b128 a[104:107], v128 offset:25088\n"
    "v_cvt_pk_bf16_f32 v74, v100, v101\n"
    "v_mfma_f32_16x16x32_bf16 v[232:235], a[140:143], v[28:31], v[232:235]\n"
    "v_exp_f32 v103, v103\n"
    "v_mfma_f32_16x16x32_bf16 v[232:235], a[136:139], v[24:27], v[232:235]\n"
    "ds_read_b128 a[108:111], v128 offset:25344\n"
    "v_cvt_pk_bf16_f32 v75, v102, v103\n"
    "v_mfma_f32_16x16x32_bf16 v[100:103], a[112:115], a[80:83], v[124:127]\n"
    "v_exp_f32 v96, v96\n"
    "v_mfma_f32_16x16x32_bf16 v[100:103], a[116:119], a[84:87], v[100:103]\n"
    "ds_read_b64_tr_b16 a[132:133], v129 offset:41472\n"
    "ds_read_b64_tr_b16 a[134:135], v129 offset:45568\n"
    "v_mfma_f32_16x16x32_bf16 v[100:103], a[120:123], a[88:91], v[100:103]\n"
    "v_exp_f32 v97, v97\n"
    "v_mfma_f32_16x16x32_bf16 v[100:103], a[124:127], a[92:95], v[100:103]\n"
    "buffer_load_dwordx4 v131, s[40:43], s56 offen offset:2048 lds\n"
    "v_mfma_f32_16x16x32_bf16 v[236:239], a[136:139], v[32:35], v[236:239]\n"
    "v_exp_f32 v98, v98\n"
    "v_mfma_f32_16x16x32_bf16 v[236:239], a[140:143], v[36:39], v[236:239]\n"
    "v_cvt_pk_bf16_f32 v82, v96, v97\n"
    "v_mfma_f32_16x16x32_bf16 v[240:243], a[140:143], v[44:47], v[240:243]\n"
    "v_exp_f32 v99, v99\n"
    "v_mfma_f32_16x16x32_bf16 v[240:243], a[136:139], v[40:43], v[240:243]\n"
    "s_waitcnt lgkmcnt(2)\n"
    "v_cvt_pk_bf16_f32 v83, v98, v99\n"
    "v_smfmac_f32_16x16x64_bf16 a[152:155], a[168:171], v[16:23], v133\n"
    "ds_write_b64 v134, v[138:139] offset:2056\n"
    "v_mfma_f32_16x16x32_bf16 v[96:99], a[96:99], a[0:3], v[104:107]\n"
    "v_exp_f32 v100, v100\n"
    "v_mfma_f32_16x16x32_bf16 v[96:99], a[100:103], a[4:7], v[96:99]\n"
    "ds_read_b128 v[136:139], v134 offset:3072\n"
    "v_mfma_f32_16x16x32_bf16 v[96:99], a[104:107], a[8:11], v[96:99]\n"
    "v_exp_f32 v101, v101\n"
    "v_mfma_f32_16x16x32_bf16 v[96:99], a[108:111], a[12:15], v[96:99]\n"
    "s_waitcnt lgkmcnt(2)\n"
    "v_mfma_f32_16x16x32_bf16 v[244:247], a[128:131], v[0:3], v[244:247]\n"
    "v_exp_f32 v102, v102\n"
    "v_mfma_f32_16x16x32_bf16 v[244:247], a[132:135], v[4:7], v[244:247]\n"
    "ds_read_b128 a[112:115], v128 offset:28672\n"
    "v_cvt_pk_bf16_f32 v90, v100, v101\n"
    "v_mfma_f32_16x16x32_bf16 v[248:251], a[132:135], v[12:15], v[248:251]\n"
    "v_exp_f32 v103, v103\n"
    "v_mfma_f32_16x16x32_bf16 v[248:251], a[128:131], v[8:11], v[248:251]\n"
    "ds_read_b128 a[116:119], v128 offset:28928\n"
    "v_cvt_pk_bf16_f32 v91, v102, v103\n"
    "v_mfma_f32_16x16x32_bf16 v[100:103], a[96:99], a[16:19], v[108:111]\n"
    "v_exp_f32 v96, v96\n"
    "v_mfma_f32_16x16x32_bf16 v[100:103], a[100:103], a[20:23], v[100:103]\n"
    "ds_read_b64_tr_b16 a[136:137], v130 offset:33280\n"
    "v_mfma_f32_16x16x32_bf16 v[100:103], a[104:107], a[24:27], v[100:103]\n"
    "v_exp_f32 v97, v97\n"
    "v_mfma_f32_16x16x32_bf16 v[100:103], a[108:111], a[28:31], v[100:103]\n"
    "ds_read_b64_tr_b16 a[138:139], v130 offset:37376\n"
    "v_mfma_f32_16x16x32_bf16 v[252:255], a[128:131], v[16:19], v[252:255]\n"
    "s_waitcnt lgkmcnt(4)\n"
    "v_exp_f32 v98, v98\n"
    "v_mfma_f32_16x16x32_bf16 v[252:255], a[132:135], v[20:23], v[252:255]\n"
    "v_cvt_pk_bf16_f32 v52, v96, v97\n"
    "v_lshlrev_b32 v140, 16, v136\n"
    "v_mfma_f32_16x16x32_bf16 a[172:175], a[132:135], v[28:31], a[172:175]\n"
    "v_exp_f32 v99, v99\n"
    "v_mfma_f32_16x16x32_bf16 a[172:175], a[128:131], v[24:27], a[172:175]\n"
    "v_cvt_pk_bf16_f32 v53, v98, v99\n"
    "v_and_b32 v141, s68, v136\n"
    "v_mfma_f32_16x16x32_bf16 v[96:99], a[96:99], a[32:35], v[112:115]\n"
    "v_exp_f32 v100, v100\n"
    "v_mfma_f32_16x16x32_bf16 v[96:99], a[100:103], a[36:39], v[96:99]\n"
    "ds_read_b64_tr_b16 a[140:141], v130 offset:41472\n"
    "v_mul_f32 v140, s76, v140\n"
    "v_mfma_f32_16x16x32_bf16 v[96:99], a[104:107], a[40:43], v[96:99]\n"
    "v_exp_f32 v101, v101\n"
    "v_mfma_f32_16x16x32_bf16 v[96:99], a[108:111], a[44:47], v[96:99]\n"
    "ds_read_b64_tr_b16 a[142:143], v130 offset:45568\n"
    "v_mul_f32 v141, s76, v141\n"
    "v_mfma_f32_16x16x32_bf16 a[176:179], a[128:131], v[32:35], a[176:179]\n"
    "v_exp_f32 v102, v102\n"
    "v_mfma_f32_16x16x32_bf16 a[176:179], a[132:135], v[36:39], a[176:179]\n"
    "v_cvt_pk_bf16_f32 v60, v100, v101\n"
    "v_cvt_pk_bf16_f32 v136, v140, v141\n"
    "v_mfma_f32_16x16x32_bf16 a[180:183], a[132:135], v[44:47], a[180:183]\n"
    "v_exp_f32 v103, v103\n"
    "v_mfma_f32_16x16x32_bf16 a[180:183], a[128:131], v[40:43], a[180:183]\n"
    "v_cvt_pk_bf16_f32 v61, v102, v103\n"
    "v_lshlrev_b32 v142, 16, v137\n"
    "v_mfma_f32_16x16x32_bf16 v[100:103], a[96:99], a[48:51], v[116:119]\n"
    "s_mov_b32 m0, s63\n"
    "v_exp_f32 v96, v96\n"
    "v_mfma_f32_16x16x32_bf16 v[100:103], a[100:103], a[52:55], v[100:103]\n"
    "buffer_load_dwordx4 v131, s[36:39], s58 offen offset:3072 lds\n"
    "v_mfma_f32_16x16x32_bf16 v[100:103], a[104:107], a[56:59], v[100:103]\n"
    "v_exp_f32 v97, v97\n"
    "v_mfma_f32_16x16x32_bf16 v[100:103], a[108:111], a[60:63], v[100:103]\n"
    "s_waitcnt lgkmcnt(0)\n"
    "v_and_b32 v143, s68, v137\n"
    "v_mul_f32 v142, s76, v142\n"
    "v_mfma_f32_16x16x32_bf16 a[184:187], a[136:139], v[0:3], a[184:187]\n"
    "v_exp_f32 v98, v98\n"
    "v_mfma_f32_16x16x32_bf16 a[184:187], a[140:143], v[4:7], a[184:187]\n"
    "ds_read_b128 a[120:123], v128 offset:29184\n"
    "v_cvt_pk_bf16_f32 v68, v96, v97\n"
    "v_mfma_f32_16x16x32_bf16 a[188:191], a[140:143], v[12:15], a[188:191]\n"
    "v_exp_f32 v99, v99\n"
    "v_mfma_f32_16x16x32_bf16 a[188:191], a[136:139], v[8:11], a[188:191]\n"
    "ds_read_b128 a[124:127], v128 offset:29440\n"
    "v_cvt_pk_bf16_f32 v69, v98, v99\n"
    "v_smfmac_f32_16x16x64_bf16 a[156:159], a[168:171], v[24:31], v133\n"
    "v_mul_f32 v143, s76, v143\n"
    "v_cvt_pk_bf16_f32 v137, v142, v143\n"
    "v_mfma_f32_16x16x32_bf16 v[96:99], a[96:99], a[64:67], v[120:123]\n"
    "v_exp_f32 v100, v100\n"
    "v_mfma_f32_16x16x32_bf16 v[96:99], a[100:103], a[68:71], v[96:99]\n"
    "ds_read_b64_tr_b16 a[128:129], v129 offset:33536\n"
    "v_lshlrev_b32 v140, 16, v138\n"
    "v_mfma_f32_16x16x32_bf16 v[96:99], a[104:107], a[72:75], v[96:99]\n"
    "v_exp_f32 v101, v101\n"
    "v_mfma_f32_16x16x32_bf16 v[96:99], a[108:111], a[76:79], v[96:99]\n"
    "ds_read_b64_tr_b16 a[130:131], v129 offset:37632\n"
    "v_and_b32 v141, s68, v138\n"
    "v_mfma_f32_16x16x32_bf16 a[192:195], a[136:139], v[16:19], a[192:195]\n"
    "v_exp_f32 v102, v102\n"
    "v_mfma_f32_16x16x32_bf16 a[192:195], a[140:143], v[20:23], a[192:195]\n"
    "v_cvt_pk_bf16_f32 v76, v100, v101\n"
    "v_mul_f32 v140, s76, v140\n"
    "v_mfma_f32_16x16x32_bf16 a[196:199], a[140:143], v[28:31], a[196:199]\n"
    "v_exp_f32 v103, v103\n"
    "v_mfma_f32_16x16x32_bf16 a[196:199], a[136:139], v[24:27], a[196:199]\n"
    "v_cvt_pk_bf16_f32 v77, v102, v103\n"
    "v_mul_f32 v141, s76, v141\n"
    "v_mfma_f32_16x16x32_bf16 v[100:103], a[96:99], a[80:83], v[124:127]\n"
    "v_exp_f32 v96, v96\n"
    "v_mfma_f32_16x16x32_bf16 v[100:103], a[100:103], a[84:87], v[100:103]\n"
    "ds_read_b64_tr_b16 a[132:133], v129 offset:41728\n"
    "v_cvt_pk_bf16_f32 v138, v140, v141\n"
    "v_mfma_f32_16x16x32_bf16 v[100:103], a[104:107], a[88:91], v[100:103]\n"
    "v_exp_f32 v97, v97\n"
    "v_mfma_f32_16x16x32_bf16 v[100:103], a[108:111], a[92:95], v[100:103]\n"
    "ds_read_b64_tr_b16 a[134:135], v129 offset:45824\n"
    "v_lshlrev_b32 v142, 16, v139\n"
    "v_mfma_f32_16x16x32_bf16 a[200:203], a[136:139], v[32:35], a[200:203]\n"
    "v_exp_f32 v98, v98\n"
    "v_mfma_f32_16x16x32_bf16 a[200:203], a[140:143], v[36:39], a[200:203]\n"
    "v_cvt_pk_bf16_f32 v84, v96, v97\n"
    "v_and_b32 v143, s68, v139\n"
    "v_mfma_f32_16x16x32_bf16 a[204:207], a[140:143], v[44:47], a[204:207]\n"
    "v_exp_f32 v99, v99\n"
    "v_mfma_f32_16x16x32_bf16 a[204:207], a[136:139], v[40:43], a[204:207]\n"
    "s_waitcnt lgkmcnt(4)\n"
    "v_cvt_pk_bf16_f32 v85, v98, v99\n"
    "v_mul_f32 v142, s76, v142\n"
    "v_mfma_f32_16x16x32_bf16 v[96:99], a[112:115], a[0:3], v[104:107]\n"
    "v_exp_f32 v100, v100\n"
    "v_mfma_f32_16x16x32_bf16 v[96:99], a[116:119], a[4:7], v[96:99]\n"
    "v_mul_f32 v143, s76, v143\n"
    "v_cvt_pk_bf16_f32 v139, v142, v143\n"
    "v_mfma_f32_16x16x32_bf16 v[96:99], a[120:123], a[8:11], v[96:99]\n"
    "v_exp_f32 v101, v101\n"
    "v_mfma_f32_16x16x32_bf16 v[96:99], a[124:127], a[12:15], v[96:99]\n"
    "s_waitcnt lgkmcnt(0)\n"
    "v_mfma_f32_16x16x32_bf16 a[208:211], a[128:131], v[0:3], a[208:211]\n"
    "v_exp_f32 v102, v102\n"
    "v_mfma_f32_16x16x32_bf16 a[208:211], a[132:135], v[4:7], a[208:211]\n"
    "v_cvt_pk_bf16_f32 v92, v100, v101\n"
    "v_mfma_f32_16x16x32_bf16 a[212:215], a[132:135], v[12:15], a[212:215]\n"
    "v_exp_f32 v103, v103\n"
    "v_mfma_f32_16x16x32_bf16 a[212:215], a[128:131], v[8:11], a[212:215]\n"
    "v_cvt_pk_bf16_f32 v93, v102, v103\n"
    "v_mfma_f32_16x16x32_bf16 v[100:103], a[112:115], a[16:19], v[108:111]\n"
    "v_exp_f32 v96, v96\n"
    "v_mfma_f32_16x16x32_bf16 v[100:103], a[116:119], a[20:23], v[100:103]\n"
    "ds_read_b64_tr_b16 a[136:137], v130 offset:33536\n"
    "v_mfma_f32_16x16x32_bf16 v[100:103], a[120:123], a[24:27], v[100:103]\n"
    "v_exp_f32 v97, v97\n"
    "v_mfma_f32_16x16x32_bf16 v[100:103], a[124:127], a[28:31], v[100:103]\n"
    "ds_read_b64_tr_b16 a[138:139], v130 offset:37632\n"
    "v_mfma_f32_16x16x32_bf16 a[216:219], a[128:131], v[16:19], a[216:219]\n"
    "v_exp_f32 v98, v98\n"
    "v_mfma_f32_16x16x32_bf16 a[216:219], a[132:135], v[20:23], a[216:219]\n"
    "v_cvt_pk_bf16_f32 v54, v96, v97\n"
    "v_mfma_f32_16x16x32_bf16 a[220:223], a[132:135], v[28:31], a[220:223]\n"
    "v_exp_f32 v99, v99\n"
    "v_mfma_f32_16x16x32_bf16 a[220:223], a[128:131], v[24:27], a[220:223]\n"
    "v_cvt_pk_bf16_f32 v55, v98, v99\n"
    "v_smfmac_f32_16x16x64_bf16 a[160:163], a[168:171], v[32:39], v133\n"
    "ds_write_b64 v134, v[136:137] offset:3072\n"
    "v_mfma_f32_16x16x32_bf16 v[96:99], a[112:115], a[32:35], v[112:115]\n"
    "v_exp_f32 v100, v100\n"
    "v_mfma_f32_16x16x32_bf16 v[96:99], a[116:119], a[36:39], v[96:99]\n"
    "ds_read_b64_tr_b16 a[140:141], v130 offset:41728\n"
    "v_mfma_f32_16x16x32_bf16 v[96:99], a[120:123], a[40:43], v[96:99]\n"
    "v_exp_f32 v101, v101\n"
    "v_mfma_f32_16x16x32_bf16 v[96:99], a[124:127], a[44:47], v[96:99]\n"
    "ds_read_b64_tr_b16 a[142:143], v130 offset:45824\n"
    "v_mfma_f32_16x16x32_bf16 a[224:227], a[128:131], v[32:35], a[224:227]\n"
    "v_exp_f32 v102, v102\n"
    "v_mfma_f32_16x16x32_bf16 a[224:227], a[132:135], v[36:39], a[224:227]\n"
    "v_cvt_pk_bf16_f32 v62, v100, v101\n"
    "v_mfma_f32_16x16x32_bf16 a[228:231], a[132:135], v[44:47], a[228:231]\n"
    "v_exp_f32 v103, v103\n"
    "v_mfma_f32_16x16x32_bf16 a[228:231], a[128:131], v[40:43], a[228:231]\n"
    "v_cvt_pk_bf16_f32 v63, v102, v103\n"
    "v_mfma_f32_16x16x32_bf16 v[100:103], a[112:115], a[48:51], v[116:119]\n"
    "v_exp_f32 v96, v96\n"
    "v_mfma_f32_16x16x32_bf16 v[100:103], a[116:119], a[52:55], v[100:103]\n"
    "ds_write_b64 v134, v[138:139] offset:3080\n"
    "v_mfma_f32_16x16x32_bf16 v[100:103], a[120:123], a[56:59], v[100:103]\n"
    "s_waitcnt vmcnt(6) lgkmcnt(1)\n"
    "v_exp_f32 v97, v97\n"
    "v_mfma_f32_16x16x32_bf16 v[100:103], a[124:127], a[60:63], v[100:103]\n"
    "s_barrier\n"
    "v_mfma_f32_16x16x32_bf16 a[232:235], a[136:139], v[0:3], a[232:235]\n"
    "v_exp_f32 v98, v98\n"
    "v_mfma_f32_16x16x32_bf16 a[232:235], a[140:143], v[4:7], a[232:235]\n"
    "ds_read_b128 a[96:99], v128 offset:0\n"
    "v_cvt_pk_bf16_f32 v70, v96, v97\n"
    "v_mfma_f32_16x16x32_bf16 a[236:239], a[140:143], v[12:15], a[236:239]\n"
    "v_exp_f32 v99, v99\n"
    "v_mfma_f32_16x16x32_bf16 a[236:239], a[136:139], v[8:11], a[236:239]\n"
    "ds_read_b128 a[100:103], v128 offset:256\n"
    "v_cvt_pk_bf16_f32 v71, v98, v99\n"
    "v_mfma_f32_16x16x32_bf16 v[96:99], a[112:115], a[64:67], v[120:123]\n"
    "s_add_u32 s60, s60, 1\n"
    "v_exp_f32 v100, v100\n"
    "v_mfma_f32_16x16x32_bf16 v[96:99], a[116:119], a[68:71], v[96:99]\n"
    "s_cmp_lt_u32 s60, 6\n"
    "ds_read_b64_tr_b16 a[128:129], v129 offset:49152\n"
    "ds_read_b64_tr_b16 a[130:131], v129 offset:53248\n"
    "v_mfma_f32_16x16x32_bf16 v[96:99], a[120:123], a[72:75], v[96:99]\n"
    "s_mov_b32 m0, s84\n"
    "v_exp_f32 v101, v101\n"
    "v_mfma_f32_16x16x32_bf16 v[96:99], a[124:127], a[76:79], v[96:99]\n"
    "buffer_load_dwordx4 v131, s[44:47], s57 offen lds\n"
    "v_mfma_f32_16x16x32_bf16 a[240:243], a[136:139], v[16:19], a[240:243]\n"
    "v_exp_f32 v102, v102\n"
    "v_mfma_f32_16x16x32_bf16 a[240:243], a[140:143], v[20:23], a[240:243]\n"
    "ds_read_b128 a[104:107], v128 offset:512\n"
    "v_cvt_pk_bf16_f32 v78, v100, v101\n"
    "v_mfma_f32_16x16x32_bf16 a[244:247], a[140:143], v[28:31], a[244:247]\n"
    "v_exp_f32 v103, v103\n"
    "v_mfma_f32_16x16x32_bf16 a[244:247], a[136:139], v[24:27], a[244:247]\n"
    "ds_read_b128 a[108:111], v128 offset:768\n"
    "v_cvt_pk_bf16_f32 v79, v102, v103\n"
    "v_mfma_f32_16x16x32_bf16 v[100:103], a[112:115], a[80:83], v[124:127]\n"
    "v_exp_f32 v96, v96\n"
    "v_mfma_f32_16x16x32_bf16 v[100:103], a[116:119], a[84:87], v[100:103]\n"
    "ds_read_b64_tr_b16 a[132:133], v129 offset:57344\n"
    "ds_read_b64_tr_b16 a[134:135], v129 offset:61440\n"
    "v_mfma_f32_16x16x32_bf16 v[100:103], a[120:123], a[88:91], v[100:103]\n"
    "v_exp_f32 v97, v97\n"
    "v_mfma_f32_16x16x32_bf16 v[100:103], a[124:127], a[92:95], v[100:103]\n"
    "buffer_load_dwordx4 v131, s[44:47], s57 offen offset:2048 lds\n"
    "v_mfma_f32_16x16x32_bf16 a[248:251], a[136:139], v[32:35], a[248:251]\n"
    "v_exp_f32 v98, v98\n"
    "v_mfma_f32_16x16x32_bf16 a[248:251], a[140:143], v[36:39], a[248:251]\n"
    "v_cvt_pk_bf16_f32 v86, v96, v97\n"
    "v_subrev_u32 v134, 4096, v134\n"
    "v_mfma_f32_16x16x32_bf16 a[252:255], a[140:143], v[44:47], a[252:255]\n"
    "v_exp_f32 v99, v99\n"
    "v_mfma_f32_16x16x32_bf16 a[252:255], a[136:139], v[40:43], a[252:255]\n"
    "s_waitcnt lgkmcnt(2)\n"
    "v_cvt_pk_bf16_f32 v87, v98, v99\n"
    "v_smfmac_f32_16x16x64_bf16 a[164:167], a[168:171], v[40:47], v133\n"
    "s_cbranch_scc1 Lloop1_%=\n"
    "v_mfma_f32_16x16x32_bf16 v[96:99], a[96:99], a[0:3], v[104:107]\n"
    "s_mov_b32 m0, s85\n"
    "v_exp_f32 v100, v100\n"
    "v_mfma_f32_16x16x32_bf16 v[96:99], a[100:103], a[4:7], v[96:99]\n"
    "buffer_load_dwordx4 v131, s[44:47], s70 offen lds\n"
    "v_mfma_f32_16x16x32_bf16 v[96:99], a[104:107], a[8:11], v[96:99]\n"
    "v_exp_f32 v101, v101\n"
    "v_mfma_f32_16x16x32_bf16 v[96:99], a[108:111], a[12:15], v[96:99]\n"
    "s_waitcnt lgkmcnt(0)\n"
    "ds_read_b128 v[136:139], v134 offset:4096\n"
    "v_mfma_f32_16x16x32_bf16 v[148:151], a[128:131], v[48:51], v[148:151]\n"
    "v_exp_f32 v102, v102\n"
    "v_mfma_f32_16x16x32_bf16 v[148:151], a[132:135], v[52:55], v[148:151]\n"
    "ds_read_b128 a[112:115], v128 offset:4096\n"
    "v_cvt_pk_bf16_f32 v94, v100, v101\n"
    "v_mfma_f32_16x16x32_bf16 v[152:155], a[132:135], v[60:63], v[152:155]\n"
    "v_exp_f32 v103, v103\n"
    "v_mfma_f32_16x16x32_bf16 v[152:155], a[128:131], v[56:59], v[152:155]\n"
    "ds_read_b128 a[116:119], v128 offset:4352\n"
    "v_cvt_pk_bf16_f32 v95, v102, v103\n"
    "v_mfma_f32_16x16x32_bf16 v[100:103], a[96:99], a[16:19], v[108:111]\n"
    "v_exp_f32 v96, v96\n"
    "v_mfma_f32_16x16x32_bf16 v[100:103], a[100:103], a[20:23], v[100:103]\n"
    "ds_read_b64_tr_b16 a[136:137], v130 offset:49152\n"
    "ds_read_b64_tr_b16 a[138:139], v130 offset:53248\n"
    "v_mfma_f32_16x16x32_bf16 v[100:103], a[104:107], a[24:27], v[100:103]\n"
    "v_exp_f32 v97, v97\n"
    "v_mfma_f32_16x16x32_bf16 v[100:103], a[108:111], a[28:31], v[100:103]\n"
    "buffer_load_dwordx4 v131, s[44:47], s70 offen offset:2048 lds\n"
    "v_mfma_f32_16x16x32_bf16 v[156:159], a[128:131], v[64:67], v[156:159]\n"
    "s_waitcnt lgkmcnt(4)\n"
    "v_exp_f32 v98, v98\n"
    "v_mfma_f32_16x16x32_bf16 v[156:159], a[132:135], v[68:71], v[156:159]\n"
    "v_cvt_pk_bf16_f32 v0, v96, v97\n"
    "v_lshlrev_b32 v140, 16, v136\n"
    "v_mfma_f32_16x16x32_bf16 v[160:163], a[132:135], v[76:79], v[160:163]\n"
    "v_exp_f32 v99, v99\n"
    "v_mfma_f32_16x16x32_bf16 v[160:163], a[128:131], v[72:75], v[160:163]\n"
    "v_cvt_pk_bf16_f32 v1, v98, v99\n"
    "v_and_b32 v141, s68, v136\n"
    "v_mfma_f32_16x16x32_bf16 v[96:99], a[96:99], a[32:35], v[112:115]\n"
    "v_exp_f32 v100, v100\n"
    "v_mfma_f32_16x16x32_bf16 v[96:99], a[100:103], a[36:39], v[96:99]\n"
    "ds_read_b64_tr_b16 a[140:141], v130 offset:57344\n"
    "ds_read_b64_tr_b16 a[142:143], v130 offset:61440\n"
    "v_mfma_f32_16x16x32_bf16 v[96:99], a[104:107], a[40:43], v[96:99]\n"
    "s_mov_b32 m0, s83\n"
    "v_exp_f32 v101, v101\n"
    "v_mfma_f32_16x16x32_bf16 v[96:99], a[108:111], a[44:47], v[96:99]\n"
    "buffer_load_dwordx4 v131, s[40:43], s94 offen lds\n"
    "v_mfma_f32_16x16x32_bf16 v[164:167], a[128:131], v[80:83], v[164:167]\n"
    "v_exp_f32 v102, v102\n"
    "v_mfma_f32_16x16x32_bf16 v[164:167], a[132:135], v[84:87], v[164:167]\n"
    "v_cvt_pk_bf16_f32 v8, v100, v101\n"
    "v_mul_f32 v140, s76, v140\n"
    "v_mfma_f32_16x16x32_bf16 v[168:171], a[132:135], v[92:95], v[168:171]\n"
    "v_exp_f32 v103, v103\n"
    "v_mfma_f32_16x16x32_bf16 v[168:171], a[128:131], v[88:91], v[168:171]\n"
    "v_cvt_pk_bf16_f32 v9, v102, v103\n"
    "v_mul_f32 v141, s76, v141\n"
    "v_mfma_f32_16x16x32_bf16 v[100:103], a[96:99], a[48:51], v[116:119]\n"
    "v_exp_f32 v96, v96\n"
    "v_mfma_f32_16x16x32_bf16 v[100:103], a[100:103], a[52:55], v[100:103]\n"
    "buffer_load_dwordx4 v131, s[40:43], s94 offen offset:2048 lds\n"
    "v_mfma_f32_16x16x32_bf16 v[100:103], a[104:107], a[56:59], v[100:103]\n"
    "v_exp_f32 v97, v97\n"
    "v_mfma_f32_16x16x32_bf16 v[100:103], a[108:111], a[60:63], v[100:103]\n"
    "s_waitcnt lgkmcnt(0)\n"
    "v_cvt_pk_bf16_f32 v136, v140, v141\n"
    "v_lshlrev_b32 v142, 16, v137\n"
    "v_mfma_f32_16x16x32_bf16 v[172:175], a[136:139], v[48:51], v[172:175]\n"
    "v_exp_f32 v98, v98\n"
    "v_mfma_f32_16x16x32_bf16 v[172:175], a[140:143], v[52:55], v[172:175]\n"
    "ds_read_b128 a[120:123], v128 offset:4608\n"
    "v_cvt_pk_bf16_f32 v16, v96, v97\n"
    "v_mfma_f32_16x16x32_bf16 v[176:179], a[140:143], v[60:63], v[176:179]\n"
    "v_exp_f32 v99, v99\n"
    "v_mfma_f32_16x16x32_bf16 v[176:179], a[136:139], v[56:59], v[176:179]\n"
    "ds_read_b128 a[124:127], v128 offset:4864\n"
    "v_cvt_pk_bf16_f32 v17, v98, v99\n"
    "v_smfmac_f32_16x16x64_bf16 a[144:147], a[168:171], v[48:55], v133\n"
    "v_and_b32 v143, s68, v137\n"
    "v_mul_f32 v142, s76, v142\n"
    "v_mfma_f32_16x16x32_bf16 v[96:99], a[96:99], a[64:67], v[120:123]\n"
    "v_exp_f32 v100, v100\n"
    "v_mfma_f32_16x16x32_bf16 v[96:99], a[100:103], a[68:71], v[96:99]\n"
    "ds_read_b64_tr_b16 a[128:129], v129 offset:49408\n"
    "v_mul_f32 v143, s76, v143\n"
    "v_mfma_f32_16x16x32_bf16 v[96:99], a[104:107], a[72:75], v[96:99]\n"
    "v_exp_f32 v101, v101\n"
    "v_mfma_f32_16x16x32_bf16 v[96:99], a[108:111], a[76:79], v[96:99]\n"
    "ds_read_b64_tr_b16 a[130:131], v129 offset:53504\n"
    "v_cvt_pk_bf16_f32 v137, v142, v143\n"
    "v_mfma_f32_16x16x32_bf16 v[180:183], a[136:139], v[64:67], v[180:183]\n"
    "v_exp_f32 v102, v102\n"
    "v_mfma_f32_16x16x32_bf16 v[180:183], a[140:143], v[68:71], v[180:183]\n"
    "v_cvt_pk_bf16_f32 v24, v100, v101\n"
    "v_lshlrev_b32 v140, 16, v138\n"
    "v_mfma_f32_16x16x32_bf16 v[184:187], a[140:143], v[76:79], v[184:187]\n"
    "v_exp_f32 v103, v103\n"
    "v_mfma_f32_16x16x32_bf16 v[184:187], a[136:139], v[72:75], v[184:187]\n"
    "v_cvt_pk_bf16_f32 v25, v102, v103\n"
    "v_and_b32 v141, s68, v138\n"
    "v_mfma_f32_16x16x32_bf16 v[100:103], a[96:99], a[80:83], v[124:127]\n"
    "v_exp_f32 v96, v96\n"
    "v_mfma_f32_16x16x32_bf16 v[100:103], a[100:103], a[84:87], v[100:103]\n"
    "ds_read_b64_tr_b16 a[132:133], v129 offset:57600\n"
    "v_mul_f32 v140, s76, v140\n"
    "v_mfma_f32_16x16x32_bf16 v[100:103], a[104:107], a[88:91], v[100:103]\n"
    "v_exp_f32 v97, v97\n"
    "v_mfma_f32_16x16x32_bf16 v[100:103], a[108:111], a[92:95], v[100:103]\n"
    "ds_read_b64_tr_b16 a[134:135], v129 offset:61696\n"
    "v_mul_f32 v141, s76, v141\n"
    "v_mfma_f32_16x16x32_bf16 v[188:191], a[136:139], v[80:83], v[188:191]\n"
    "v_exp_f32 v98, v98\n"
    "v_mfma_f32_16x16x32_bf16 v[188:191], a[140:143], v[84:87], v[188:191]\n"
    "v_cvt_pk_bf16_f32 v32, v96, v97\n"
    "v_cvt_pk_bf16_f32 v138, v140, v141\n"
    "v_mfma_f32_16x16x32_bf16 v[192:195], a[140:143], v[92:95], v[192:195]\n"
    "v_exp_f32 v99, v99\n"
    "v_mfma_f32_16x16x32_bf16 v[192:195], a[136:139], v[88:91], v[192:195]\n"
    "s_waitcnt lgkmcnt(4)\n"
    "v_cvt_pk_bf16_f32 v33, v98, v99\n"
    "v_lshlrev_b32 v142, 16, v139\n"
    "v_mfma_f32_16x16x32_bf16 v[96:99], a[112:115], a[0:3], v[104:107]\n"
    "s_mov_b32 s61, 67108864\n"
    "v_exp_f32 v100, v100\n"
    "v_mfma_f32_16x16x32_bf16 v[96:99], a[116:119], a[4:7], v[96:99]\n"
    "s_add_u32 s56, s56, 16384\n"
    "v_and_b32 v143, s68, v139\n"
    "v_mul_f32 v142, s76, v142\n"
    "v_mfma_f32_16x16x32_bf16 v[96:99], a[120:123], a[8:11], v[96:99]\n"
    "s_and_b32 s56, s56, 262143\n"
    "v_exp_f32 v101, v101\n"
    "v_mfma_f32_16x16x32_bf16 v[96:99], a[124:127], a[12:15], v[96:99]\n"
    "s_waitcnt lgkmcnt(0)\n"
    "v_mul_f32 v143, s76, v143\n"
    "v_cvt_pk_bf16_f32 v139, v142, v143\n"
    "v_mfma_f32_16x16x32_bf16 v[196:199], a[128:131], v[48:51], v[196:199]\n"
    "s_or_b32 s56, s56, s61\n"
    "v_exp_f32 v102, v102\n"
    "v_mfma_f32_16x16x32_bf16 v[196:199], a[132:135], v[52:55], v[196:199]\n"
    "s_add_u32 s94, s56, 8192\n"
    "v_cvt_pk_bf16_f32 v40, v100, v101\n"
    "v_mfma_f32_16x16x32_bf16 v[200:203], a[132:135], v[60:63], v[200:203]\n"
    "s_add_u32 s57, s57, 16384\n"
    "v_exp_f32 v103, v103\n"
    "v_mfma_f32_16x16x32_bf16 v[200:203], a[128:131], v[56:59], v[200:203]\n"
    "s_and_b32 s57, s57, 262143\n"
    "v_cvt_pk_bf16_f32 v41, v102, v103\n"
    "v_mfma_f32_16x16x32_bf16 v[100:103], a[112:115], a[16:19], v[108:111]\n"
    "s_or_b32 s57, s57, s96\n"
    "v_exp_f32 v96, v96\n"
    "v_mfma_f32_16x16x32_bf16 v[100:103], a[116:119], a[20:23], v[100:103]\n"
    "s_add_u32 s70, s57, 8192\n"
    "ds_read_b64_tr_b16 a[136:137], v130 offset:49408\n"
    "v_mfma_f32_16x16x32_bf16 v[100:103], a[120:123], a[24:27], v[100:103]\n"
    "v_exp_f32 v97, v97\n"
    "v_mfma_f32_16x16x32_bf16 v[100:103], a[124:127], a[28:31], v[100:103]\n"
    "ds_read_b64_tr_b16 a[138:139], v130 offset:53504\n"
    "v_mfma_f32_16x16x32_bf16 v[204:207], a[128:131], v[64:67], v[204:207]\n"
    "v_exp_f32 v98, v98\n"
    "v_mfma_f32_16x16x32_bf16 v[204:207], a[132:135], v[68:71], v[204:207]\n"
    "v_cvt_pk_bf16_f32 v2, v96, v97\n"
    "v_mfma_f32_16x16x32_bf16 v[208:211], a[132:135], v[76:79], v[208:211]\n"
    "v_exp_f32 v99, v99\n"
    "v_mfma_f32_16x16x32_bf16 v[208:211], a[128:131], v[72:75], v[208:211]\n"
    "v_cvt_pk_bf16_f32 v3, v98, v99\n"
    "v_smfmac_f32_16x16x64_bf16 a[148:151], a[168:171], v[56:63], v133\n"
    "v_mfma_f32_16x16x32_bf16 v[96:99], a[112:115], a[32:35], v[112:115]\n"
    "v_exp_f32 v100, v100\n"
    "v_mfma_f32_16x16x32_bf16 v[96:99], a[116:119], a[36:39], v[96:99]\n"
    "ds_read_b64_tr_b16 a[140:141], v130 offset:57600\n"
    "v_mfma_f32_16x16x32_bf16 v[96:99], a[120:123], a[40:43], v[96:99]\n"
    "v_exp_f32 v101, v101\n"
    "v_mfma_f32_16x16x32_bf16 v[96:99], a[124:127], a[44:47], v[96:99]\n"
    "ds_read_b64_tr_b16 a[142:143], v130 offset:61696\n"
    "v_mfma_f32_16x16x32_bf16 v[212:215], a[128:131], v[80:83], v[212:215]\n"
    "v_exp_f32 v102, v102\n"
    "v_mfma_f32_16x16x32_bf16 v[212:215], a[132:135], v[84:87], v[212:215]\n"
    "v_cvt_pk_bf16_f32 v10, v100, v101\n"
    "v_mfma_f32_16x16x32_bf16 v[216:219], a[132:135], v[92:95], v[216:219]\n"
    "v_exp_f32 v103, v103\n"
    "v_mfma_f32_16x16x32_bf16 v[216:219], a[128:131], v[88:91], v[216:219]\n"
    "v_cvt_pk_bf16_f32 v11, v102, v103\n"
    "v_mfma_f32_16x16x32_bf16 v[100:103], a[112:115], a[48:51], v[116:119]\n"
    "v_exp_f32 v96, v96\n"
    "v_mfma_f32_16x16x32_bf16 v[100:103], a[116:119], a[52:55], v[100:103]\n"
    "ds_write_b64 v134, v[136:137] offset:4096\n"
    "v_mfma_f32_16x16x32_bf16 v[100:103], a[120:123], a[56:59], v[100:103]\n"
    "s_waitcnt vmcnt(10) lgkmcnt(1)\n"
    "v_exp_f32 v97, v97\n"
    "v_mfma_f32_16x16x32_bf16 v[100:103], a[124:127], a[60:63], v[100:103]\n"
    "s_barrier\n"
    "v_mfma_f32_16x16x32_bf16 v[220:223], a[136:139], v[48:51], v[220:223]\n"
    "v_exp_f32 v98, v98\n"
    "v_mfma_f32_16x16x32_bf16 v[220:223], a[140:143], v[52:55], v[220:223]\n"
    "ds_read_b128 a[96:99], v128 offset:8192\n"
    "v_cvt_pk_bf16_f32 v18, v96, v97\n"
    "v_mfma_f32_16x16x32_bf16 v[224:227], a[140:143], v[60:63], v[224:227]\n"
    "v_exp_f32 v99, v99\n"
    "v_mfma_f32_16x16x32_bf16 v[224:227], a[136:139], v[56:59], v[224:227]\n"
    "ds_read_b128 a[100:103], v128 offset:8448\n"
    "v_cvt_pk_bf16_f32 v19, v98, v99\n"
    "v_mfma_f32_16x16x32_bf16 v[96:99], a[112:115], a[64:67], v[120:123]\n"
    "v_exp_f32 v100, v100\n"
    "v_mfma_f32_16x16x32_bf16 v[96:99], a[116:119], a[68:71], v[96:99]\n"
    "ds_read_b64_tr_b16 a[128:129], v129 offset:49664\n"
    "ds_read_b64_tr_b16 a[130:131], v129 offset:53760\n"
    "v_mfma_f32_16x16x32_bf16 v[96:99], a[120:123], a[72:75], v[96:99]\n"
    "s_mov_b32 m0, s80\n"
    "v_exp_f32 v101, v101\n"
    "v_mfma_f32_16x16x32_bf16 v[96:99], a[124:127], a[76:79], v[96:99]\n"
    "buffer_load_dwordx4 v131, s[40:43], s56 offen lds\n"
    "v_mfma_f32_16x16x32_bf16 v[228:231], a[136:139], v[64:67], v[228:231]\n"
    "v_exp_f32 v102, v102\n"
    "v_mfma_f32_16x16x32_bf16 v[228:231], a[140:143], v[68:71], v[228:231]\n"
    "ds_read_b128 a[104:107], v128 offset:8704\n"
    "v_cvt_pk_bf16_f32 v26, v100, v101\n"
    "v_mfma_f32_16x16x32_bf16 v[232:235], a[140:143], v[76:79], v[232:235]\n"
    "v_exp_f32 v103, v103\n"
    "v_mfma_f32_16x16x32_bf16 v[232:235], a[136:139], v[72:75], v[232:235]\n"
    "ds_read_b128 a[108:111], v128 offset:8960\n"
    "v_cvt_pk_bf16_f32 v27, v102, v103\n"
    "v_mfma_f32_16x16x32_bf16 v[100:103], a[112:115], a[80:83], v[124:127]\n"
    "v_exp_f32 v96, v96\n"
    "v_mfma_f32_16x16x32_bf16 v[100:103], a[116:119], a[84:87], v[100:103]\n"
    "ds_read_b64_tr_b16 a[132:133], v129 offset:57856\n"
    "ds_read_b64_tr_b16 a[134:135], v129 offset:61952\n"
    "v_mfma_f32_16x16x32_bf16 v[100:103], a[120:123], a[88:91], v[100:103]\n"
    "v_exp_f32 v97, v97\n"
    "v_mfma_f32_16x16x32_bf16 v[100:103], a[124:127], a[92:95], v[100:103]\n"
    "buffer_load_dwordx4 v131, s[40:43], s56 offen offset:2048 lds\n"
    "v_mfma_f32_16x16x32_bf16 v[236:239], a[136:139], v[80:83], v[236:239]\n"
    "v_exp_f32 v98, v98\n"
    "v_mfma_f32_16x16x32_bf16 v[236:239], a[140:143], v[84:87], v[236:239]\n"
    "v_cvt_pk_bf16_f32 v34, v96, v97\n"
    "v_mfma_f32_16x16x32_bf16 v[240:243], a[140:143], v[92:95], v[240:243]\n"
    "v_exp_f32 v99, v99\n"
    "v_mfma_f32_16x16x32_bf16 v[240:243], a[136:139], v[88:91], v[240:243]\n"
    "s_waitcnt lgkmcnt(2)\n"
    "v_cvt_pk_bf16_f32 v35, v98, v99\n"
    "v_smfmac_f32_16x16x64_bf16 a[152:155], a[168:171], v[64:71], v133\n"
    "ds_write_b64 v134, v[138:139] offset:4104\n"
    "v_mfma_f32_16x16x32_bf16 v[96:99], a[96:99], a[0:3], v[104:107]\n"
    "v_exp_f32 v100, v100\n"
    "v_mfma_f32_16x16x32_bf16 v[96:99], a[100:103], a[4:7], v[96:99]\n"
    "ds_read_b128 v[136:139], v134 offset:5120\n"
    "v_mfma_f32_16x16x32_bf16 v[96:99], a[104:107], a[8:11], v[96:99]\n"
    "v_exp_f32 v101, v101\n"
    "v_mfma_f32_16x16x32_bf16 v[96:99], a[108:111], a[12:15], v[96:99]\n"
    "s_waitcnt lgkmcnt(2)\n"
    "v_mfma_f32_16x16x32_bf16 v[244:247], a[128:131], v[48:51], v[244:247]\n"
    "v_exp_f32 v102, v102\n"
    "v_mfma_f32_16x16x32_bf16 v[244:247], a[132:135], v[52:55], v[244:247]\n"
    "ds_read_b128 a[112:115], v128 offset:12288\n"
    "v_cvt_pk_bf16_f32 v42, v100, v101\n"
    "v_mfma_f32_16x16x32_bf16 v[248:251], a[132:135], v[60:63], v[248:251]\n"
    "v_exp_f32 v103, v103\n"
    "v_mfma_f32_16x16x32_bf16 v[248:251], a[128:131], v[56:59], v[248:251]\n"
    "ds_read_b128 a[116:119], v128 offset:12544\n"
    "v_cvt_pk_bf16_f32 v43, v102, v103\n"
    "v_mfma_f32_16x16x32_bf16 v[100:103], a[96:99], a[16:19], v[108:111]\n"
    "v_exp_f32 v96, v96\n"
    "v_mfma_f32_16x16x32_bf16 v[100:103], a[100:103], a[20:23], v[100:103]\n"
    "ds_read_b64_tr_b16 a[136:137], v130 offset:49664\n"
    "v_mfma_f32_16x16x32_bf16 v[100:103], a[104:107], a[24:27], v[100:103]\n"
    "v_exp_f32 v97, v97\n"
    "v_mfma_f32_16x16x32_bf16 v[100:103], a[108:111], a[28:31], v[100:103]\n"
    "ds_read_b64_tr_b16 a[138:139], v130 offset:53760\n"
    "v_mfma_f32_16x16x32_bf16 v[252:255], a[128:131], v[64:67], v[252:255]\n"
    "s_waitcnt lgkmcnt(4)\n"
    "v_exp_f32 v98, v98\n"
    "v_mfma_f32_16x16x32_bf16 v[252:255], a[132:135], v[68:71], v[252:255]\n"
    "v_cvt_pk_bf16_f32 v4, v96, v97\n"
    "v_lshlrev_b32 v140, 16, v136\n"
    "v_mfma_f32_16x16x32_bf16 a[172:175], a[132:135], v[76:79], a[172:175]\n"
    "v_exp_f32 v99, v99\n"
    "v_mfma_f32_16x16x32_bf16 a[172:175], a[128:131], v[72:75], a[172:175]\n"
    "v_cvt_pk_bf16_f32 v5, v98, v99\n"
    "v_and_b32 v141, s68, v136\n"
    "v_mfma_f32_16x16x32_bf16 v[96:99], a[96:99], a[32:35], v[112:115]\n"
    "v_exp_f32 v100, v100\n"
    "v_mfma_f32_16x16x32_bf16 v[96:99], a[100:103], a[36:39], v[96:99]\n"
    "ds_read_b64_tr_b16 a[140:141], v130 offset:57856\n"
    "v_mul_f32 v140, s76, v140\n"
    "v_mfma_f32_16x16x32_bf16 v[96:99], a[104:107], a[40:43], v[96:99]\n"
    "v_exp_f32 v101, v101\n"
    "v_mfma_f32_16x16x32_bf16 v[96:99], a[108:111], a[44:47], v[96:99]\n"
    "ds_read_b64_tr_b16 a[142:143], v130 offset:61952\n"
    "v_mul_f32 v141, s76, v141\n"
    "v_mfma_f32_16x16x32_bf16 a[176:179], a[128:131], v[80:83], a[176:179]\n"
    "v_exp_f32 v102, v102\n"
    "v_mfma_f32_16x16x32_bf16 a[176:179], a[132:135], v[84:87], a[176:179]\n"
    "v_cvt_pk_bf16_f32 v12, v100, v101\n"
    "v_cvt_pk_bf16_f32 v136, v140, v141\n"
    "v_mfma_f32_16x16x32_bf16 a[180:183], a[132:135], v[92:95], a[180:183]\n"
    "v_exp_f32 v103, v103\n"
    "v_mfma_f32_16x16x32_bf16 a[180:183], a[128:131], v[88:91], a[180:183]\n"
    "v_cvt_pk_bf16_f32 v13, v102, v103\n"
    "v_lshlrev_b32 v142, 16, v137\n"
    "v_mfma_f32_16x16x32_bf16 v[100:103], a[96:99], a[48:51], v[116:119]\n"
    "v_exp_f32 v96, v96\n"
    "v_mfma_f32_16x16x32_bf16 v[100:103], a[100:103], a[52:55], v[100:103]\n"
    "v_and_b32 v143, s68, v137\n"
    "v_mul_f32 v142, s76, v142\n"
    "v_mfma_f32_16x16x32_bf16 v[100:103], a[104:107], a[56:59], v[100:103]\n"
    "v_exp_f32 v97, v97\n"
    "v_mfma_f32_16x16x32_bf16 v[100:103], a[108:111], a[60:63], v[100:103]\n"
    "s_waitcnt lgkmcnt(0)\n"
    "v_mul_f32 v143, s76, v143\n"
    "v_cvt_pk_bf16_f32 v137, v142, v143\n"
    "v_mfma_f32_16x16x32_bf16 a[184:187], a[136:139], v[48:51], a[184:187]\n"
    "v_exp_f32 v98, v98\n"
    "v_mfma_f32_16x16x32_bf16 a[184:187], a[140:143], v[52:55], a[184:187]\n"
    "ds_read_b128 a[120:123], v128 offset:12800\n"
    "v_cvt_pk_bf16_f32 v20, v96, v97\n"
    "v_mfma_f32_16x16x32_bf16 a[188:191], a[140:143], v[60:63], a[188:191]\n"
    "v_exp_f32 v99, v99\n"
    "v_mfma_f32_16x16x32_bf16 a[188:191], a[136:139], v[56:59], a[188:191]\n"
    "ds_read_b128 a[124:127], v128 offset:13056\n"
    "v_cvt_pk_bf16_f32 v21, v98, v99\n"
    "v_smfmac_f32_16x16x64_bf16 a[156:159], a[168:171], v[72:79], v133\n"
    "v_lshlrev_b32 v140, 16, v138\n"
    "v_and_b32 v141, s68, v138\n"
    "v_mfma_f32_16x16x32_bf16 v[96:99], a[96:99], a[64:67], v[120:123]\n"
    "v_exp_f32 v100, v100\n"
    "v_mfma_f32_16x16x32_bf16 v[96:99], a[100:103], a[68:71], v[96:99]\n"
    "ds_read_b64_tr_b16 a[128:129], v129 offset:49920\n"
    "v_mul_f32 v140, s76, v140\n"
    "v_mfma_f32_16x16x32_bf16 v[96:99], a[104:107], a[72:75], v[96:99]\n"
    "v_exp_f32 v101, v101\n"
    "v_mfma_f32_16x16x32_bf16 v[96:99], a[108:111], a[76:79], v[96:99]\n"
    "ds_read_b64_tr_b16 a[130:131], v129 offset:54016\n"
    "v_mul_f32 v141, s76, v141\n"
    "v_mfma_f32_16x16x32_bf16 a[192:195], a[136:139], v[64:67], a[192:195]\n"
    "v_exp_f32 v102, v102\n"
    "v_mfma_f32_16x16x32_bf16 a[192:195], a[140:143], v[68:71], a[192:195]\n"
    "v_cvt_pk_bf16_f32 v28, v100, v101\n"
    "v_cvt_pk_bf16_f32 v138, v140, v141\n"
    "v_mfma_f32_16x16x32_bf16 a[196:199], a[140:143], v[76:79], a[196:199]\n"
    "v_exp_f32 v103, v103\n"
    "v_mfma_f32_16x16x32_bf16 a[196:199], a[136:139], v[72:75], a[196:199]\n"
    "v_cvt_pk_bf16_f32 v29, v102, v103\n"
    "v_lshlrev_b32 v142, 16, v139\n"
    "v_mfma_f32_16x16x32_bf16 v[100:103], a[96:99], a[80:83], v[124:127]\n"
    "v_exp_f32 v96, v96\n"
    "v_mfma_f32_16x16x32_bf16 v[100:103], a[100:103], a[84:87], v[100:103]\n"
    "ds_read_b64_tr_b16 a[132:133], v129 offset:58112\n"
    "v_and_b32 v143, s68, v139\n"
    "v_mfma_f32_16x16x32_bf16 v[100:103], a[104:107], a[88:91], v[100:103]\n"
    "v_exp_f32 v97, v97\n"
    "v_mfma_f32_16x16x32_bf16 v[100:103], a[108:111], a[92:95], v[100:103]\n"
    "ds_read_b64_tr_b16 a[134:135], v129 offset:62208\n"
    "v_mul_f32 v142, s76, v142\n"
    "v_mfma_f32_16x16x32_bf16 a[200:203], a[136:139], v[80:83], a[200:203]\n"
    "v_exp_f32 v98, v98\n"
    "v_mfma_f32_16x16x32_bf16 a[200:203], a[140:143], v[84:87], a[200:203]\n"
    "v_cvt_pk_bf16_f32 v36, v96, v97\n"
    "v_mul_f32 v143, s76, v143\n"
    "v_mfma_f32_16x16x32_bf16 a[204:207], a[140:143], v[92:95], a[204:207]\n"
    "v_exp_f32 v99, v99\n"
    "v_mfma_f32_16x16x32_bf16 a[204:207], a[136:139], v[88:91], a[204:207]\n"
    "s_waitcnt lgkmcnt(4)\n"
    "v_cvt_pk_bf16_f32 v37, v98, v99\n"
    "v_cvt_pk_bf16_f32 v139, v142, v143\n"
    "v_mfma_f32_16x16x32_bf16 v[96:99], a[112:115], a[0:3], v[104:107]\n"
    "v_exp_f32 v100, v100\n"
    "v_mfma_f32_16x16x32_bf16 v[96:99], a[116:119], a[4:7], v[96:99]\n"
    "v_mfma_f32_16x16x32_bf16 v[96:99], a[120:123], a[8:11], v[96:99]\n"
    "v_exp_f32 v101, v101\n"
    "v_mfma_f32_16x16x32_bf16 v[96:99], a[124:127], a[12:15], v[96:99]\n"
    "s_waitcnt lgkmcnt(0)\n"
    "v_mfma_f32_16x16x32_bf16 a[208:211], a[128:131], v[48:51], a[208:211]\n"
    "v_exp_f32 v102, v102\n"
    "v_mfma_f32_16x16x32_bf16 a[208:211], a[132:135], v[52:55], a[208:211]\n"
    "v_cvt_pk_bf16_f32 v44, v100, v101\n"
    "v_mfma_f32_16x16x32_bf16 a[212:215], a[132:135], v[60:63], a[212:215]\n"
    "v_exp_f32 v103, v103\n"
    "v_mfma_f32_16x16x32_bf16 a[212:215], a[128:131], v[56:59], a[212:215]\n"
    "v_cvt_pk_bf16_f32 v45, v102, v103\n"
    "v_mfma_f32_16x16x32_bf16 v[100:103], a[112:115], a[16:19], v[108:111]\n"
    "v_exp_f32 v96, v96\n"
    "v_mfma_f32_16x16x32_bf16 v[100:103], a[116:119], a[20:23], v[100:103]\n"
    "ds_read_b64_tr_b16 a[136:137], v130 offset:49920\n"
    "v_mfma_f32_16x16x32_bf16 v[100:103], a[120:123], a[24:27], v[100:103]\n"
    "v_exp_f32 v97, v97\n"
    "v_mfma_f32_16x16x32_bf16 v[100:103], a[124:127], a[28:31], v[100:103]\n"
    "ds_read_b64_tr_b16 a[138:139], v130 offset:54016\n"
    "v_mfma_f32_16x16x32_bf16 a[216:219], a[128:131], v[64:67], a[216:219]\n"
    "v_exp_f32 v98, v98\n"
    "v_mfma_f32_16x16x32_bf16 a[216:219], a[132:135], v[68:71], a[216:219]\n"
    "v_cvt_pk_bf16_f32 v6, v96, v97\n"
    "v_mfma_f32_16x16x32_bf16 a[220:223], a[132:135], v[76:79], a[220:223]\n"
    "v_exp_f32 v99, v99\n"
    "v_mfma_f32_16x16x32_bf16 a[220:223], a[128:131], v[72:75], a[220:223]\n"
    "v_cvt_pk_bf16_f32 v7, v98, v99\n"
    "v_smfmac_f32_16x16x64_bf16 a[160:163], a[168:171], v[80:87], v133\n"
    "ds_write_b64 v134, v[136:137] offset:5120\n"
    "v_mfma_f32_16x16x32_bf16 v[96:99], a[112:115], a[32:35], v[112:115]\n"
    "v_exp_f32 v100, v100\n"
    "v_mfma_f32_16x16x32_bf16 v[96:99], a[116:119], a[36:39], v[96:99]\n"
    "ds_read_b64_tr_b16 a[140:141], v130 offset:58112\n"
    "v_mfma_f32_16x16x32_bf16 v[96:99], a[120:123], a[40:43], v[96:99]\n"
    "v_exp_f32 v101, v101\n"
    "v_mfma_f32_16x16x32_bf16 v[96:99], a[124:127], a[44:47], v[96:99]\n"
    "ds_read_b64_tr_b16 a[142:143], v130 offset:62208\n"
    "v_mfma_f32_16x16x32_bf16 a[224:227], a[128:131], v[80:83], a[224:227]\n"
    "v_exp_f32 v102, v102\n"
    "v_mfma_f32_16x16x32_bf16 a[224:227], a[132:135], v[84:87], a[224:227]\n"
    "v_cvt_pk_bf16_f32 v14, v100, v101\n"
    "v_mfma_f32_16x16x32_bf16 a[228:231], a[132:135], v[92:95], a[228:231]\n"
    "v_exp_f32 v103, v103\n"
    "v_mfma_f32_16x16x32_bf16 a[228:231], a[128:131], v[88:91], a[228:231]\n"
    "v_cvt_pk_bf16_f32 v15, v102, v103\n"
    "v_mfma_f32_16x16x32_bf16 v[100:103], a[112:115], a[48:51], v[116:119]\n"
    "v_exp_f32 v96, v96\n"
    "v_mfma_f32_16x16x32_bf16 v[100:103], a[116:119], a[52:55], v[100:103]\n"
    "ds_write_b64 v134, v[138:139] offset:5128\n"
    "v_mfma_f32_16x16x32_bf16 v[100:103], a[120:123], a[56:59], v[100:103]\n"
    "s_waitcnt vmcnt(4) lgkmcnt(1)\n"
    "v_exp_f32 v97, v97\n"
    "v_mfma_f32_16x16x32_bf16 v[100:103], a[124:127], a[60:63], v[100:103]\n"
    "s_barrier\n"
    "v_mfma_f32_16x16x32_bf16 a[232:235], a[136:139], v[48:51], a[232:235]\n"
    "v_exp_f32 v98, v98\n"
    "v_mfma_f32_16x16x32_bf16 a[232:235], a[140:143], v[52:55], a[232:235]\n"
    "ds_read_b128 a[96:99], v128 offset:16384\n"
    "v_cvt_pk_bf16_f32 v22, v96, v97\n"
    "v_mfma_f32_16x16x32_bf16 a[236:239], a[140:143], v[60:63], a[236:239]\n"
    "v_exp_f32 v99, v99\n"
    "v_mfma_f32_16x16x32_bf16 a[236:239], a[136:139], v[56:59], a[236:239]\n"
    "ds_read_b128 a[100:103], v128 offset:16640\n"
    "v_cvt_pk_bf16_f32 v23, v98, v99\n"
    "v_mfma_f32_16x16x32_bf16 v[96:99], a[112:115], a[64:67], v[120:123]\n"
    "v_exp_f32 v100, v100\n"
    "v_mfma_f32_16x16x32_bf16 v[96:99], a[116:119], a[68:71], v[96:99]\n"
    "ds_read_b64_tr_b16 a[128:129], v129 offset:32768\n"
    "ds_read_b64_tr_b16 a[130:131], v129 offset:36864\n"
    "v_mfma_f32_16x16x32_bf16 v[96:99], a[120:123], a[72:75], v[96:99]\n"
    "s_mov_b32 m0, s86\n"
    "v_exp_f32 v101, v101\n"
    "v_mfma_f32_16x16x32_bf16 v[96:99], a[124:127], a[76:79], v[96:99]\n"
    "buffer_load_dwordx4 v131, s[44:47], s57 offen lds\n"
    "v_mfma_f32_16x16x32_bf16 a[240:243], a[136:139], v[64:67], a[240:243]\n"
    "v_exp_f32 v102, v102\n"
    "v_mfma_f32_16x16x32_bf16 a[240:243], a[140:143], v[68:71], a[240:243]\n"
    "ds_read_b128 a[104:107], v128 offset:16896\n"
    "v_cvt_pk_bf16_f32 v30, v100, v101\n"
    "v_mfma_f32_16x16x32_bf16 a[244:247], a[140:143], v[76:79], a[244:247]\n"
    "v_exp_f32 v103, v103\n"
    "v_mfma_f32_16x16x32_bf16 a[244:247], a[136:139], v[72:75], a[244:247]\n"
    "ds_read_b128 a[108:111], v128 offset:17152\n"
    "v_cvt_pk_bf16_f32 v31, v102, v103\n"
    "v_mfma_f32_16x16x32_bf16 v[100:103], a[112:115], a[80:83], v[124:127]\n"
    "v_exp_f32 v96, v96\n"
    "v_mfma_f32_16x16x32_bf16 v[100:103], a[116:119], a[84:87], v[100:103]\n"
    "ds_read_b64_tr_b16 a[132:133], v129 offset:40960\n"
    "ds_read_b64_tr_b16 a[134:135], v129 offset:45056\n"
    "v_mfma_f32_16x16x32_bf16 v[100:103], a[120:123], a[88:91], v[100:103]\n"
    "v_exp_f32 v97, v97\n"
    "v_mfma_f32_16x16x32_bf16 v[100:103], a[124:127], a[92:95], v[100:103]\n"
    "buffer_load_dwordx4 v131, s[44:47], s57 offen offset:2048 lds\n"
    "v_mfma_f32_16x16x32_bf16 a[248:251], a[136:139], v[80:83], a[248:251]\n"
    "v_exp_f32 v98, v98\n"
    "v_mfma_f32_16x16x32_bf16 a[248:251], a[140:143], v[84:87], a[248:251]\n"
    "v_cvt_pk_bf16_f32 v38, v96, v97\n"
    "v_mfma_f32_16x16x32_bf16 a[252:255], a[140:143], v[92:95], a[252:255]\n"
    "v_exp_f32 v99, v99\n"
    "v_mfma_f32_16x16x32_bf16 a[252:255], a[136:139], v[88:91], a[252:255]\n"
    "s_waitcnt lgkmcnt(2)\n"
    "v_cvt_pk_bf16_f32 v39, v98, v99\n"
    "v_smfmac_f32_16x16x64_bf16 a[164:167], a[168:171], v[88:95], v133\n"
    "v_mfma_f32_16x16x32_bf16 v[96:99], a[96:99], a[0:3], v[104:107]\n"
    "s_mov_b32 m0, s87\n"
    "v_exp_f32 v100, v100\n"
    "v_mfma_f32_16x16x32_bf16 v[96:99], a[100:103], a[4:7], v[96:99]\n"
    "buffer_load_dwordx4 v131, s[44:47], s70 offen nt lds\n"
    "v_mfma_f32_16x16x32_bf16 v[96:99], a[104:107], a[8:11], v[96:99]\n"
    "v_exp_f32 v101, v101\n"
    "v_mfma_f32_16x16x32_bf16 v[96:99], a[108:111], a[12:15], v[96:99]\n"
    "s_waitcnt lgkmcnt(0)\n"
    "v_mfma_f32_16x16x32_bf16 v[148:151], a[128:131], v[0:3], v[148:151]\n"
    "v_exp_f32 v102, v102\n"
    "v_mfma_f32_16x16x32_bf16 v[148:151], a[132:135], v[4:7], v[148:151]\n"
    "ds_read_b128 a[112:115], v128 offset:20480\n"
    "v_cvt_pk_bf16_f32 v46, v100, v101\n"
    "v_mfma_f32_16x16x32_bf16 v[152:155], a[132:135], v[12:15], v[152:155]\n"
    "v_exp_f32 v103, v103\n"
    "v_mfma_f32_16x16x32_bf16 v[152:155], a[128:131], v[8:11], v[152:155]\n"
    "ds_read_b128 a[116:119], v128 offset:20736\n"
    "v_cvt_pk_bf16_f32 v47, v102, v103\n"
    "v_mfma_f32_16x16x32_bf16 v[100:103], a[96:99], a[16:19], v[108:111]\n"
    "v_exp_f32 v96, v96\n"
    "v_mfma_f32_16x16x32_bf16 v[100:103], a[100:103], a[20:23], v[100:103]\n"
    "ds_read_b64_tr_b16 a[136:137], v130 offset:32768\n"
    "ds_read_b64_tr_b16 a[138:139], v130 offset:36864\n"
    "v_mfma_f32_16x16x32_bf16 v[100:103], a[104:107], a[24:27], v[100:103]\n"
    "v_exp_f32 v97, v97\n"
    "v_mfma_f32_16x16x32_bf16 v[100:103], a[108:111], a[28:31], v[100:103]\n"
    "buffer_load_dwordx4 v131, s[44:47], s70 offen offset:2048 nt lds\n"
    "v_mfma_f32_16x16x32_bf16 v[156:159], a[128:131], v[16:19], v[156:159]\n"
    "v_exp_f32 v98, v98\n"
    "v_mfma_f32_16x16x32_bf16 v[156:159], a[132:135], v[20:23], v[156:159]\n"
    "v_cvt_pk_bf16_f32 v48, v96, v97\n"
    "v_mfma_f32_16x16x32_bf16 v[160:163], a[132:135], v[28:31], v[160:163]\n"
    "v_exp_f32 v99, v99\n"
    "v_mfma_f32_16x16x32_bf16 v[160:163], a[128:131], v[24:27], v[160:163]\n"
    "v_cvt_pk_bf16_f32 v49, v98, v99\n"
    "v_mfma_f32_16x16x32_bf16 v[96:99], a[96:99], a[32:35], v[112:115]\n"
    "v_exp_f32 v100, v100\n"
    "v_mfma_f32_16x16x32_bf16 v[96:99], a[100:103], a[36:39], v[96:99]\n"
    "ds_read_b64_tr_b16 a[140:141], v130 offset:40960\n"
    "ds_read_b64_tr_b16 a[142:143], v130 offset:45056\n"
    "v_mfma_f32_16x16x32_bf16 v[96:99], a[104:107], a[40:43], v[96:99]\n"
    "s_mov_b32 m0, s81\n"
    "v_exp_f32 v101, v101\n"
    "v_mfma_f32_16x16x32_bf16 v[96:99], a[108:111], a[44:47], v[96:99]\n"
    "buffer_load_dwordx4 v131, s[40:43], s94 offen lds\n"
    "v_mfma_f32_16x16x32_bf16 v[164:167], a[128:131], v[32:35], v[164:167]\n"
    "v_exp_f32 v102, v102\n"
    "v_mfma_f32_16x16x32_bf16 v[164:167], a[132:135], v[36:39], v[164:167]\n"
    "v_cvt_pk_bf16_f32 v56, v100, v101\n"
    "v_mfma_f32_16x16x32_bf16 v[168:171], a[132:135], v[44:47], v[168:171]\n"
    "v_exp_f32 v103, v103\n"
    "v_mfma_f32_16x16x32_bf16 v[168:171], a[128:131], v[40:43], v[168:171]\n"
    "v_cvt_pk_bf16_f32 v57, v102, v103\n"
    "v_mfma_f32_16x16x32_bf16 v[100:103], a[96:99], a[48:51], v[116:119]\n"
    "v_exp_f32 v96, v96\n"
    "v_mfma_f32_16x16x32_bf16 v[100:103], a[100:103], a[52:55], v[100:103]\n"
    "buffer_load_dwordx4 v131, s[40:43], s94 offen offset:2048 lds\n"
    "v_mfma_f32_16x16x32_bf16 v[100:103], a[104:107], a[56:59], v[100:103]\n"
    "v_exp_f32 v97, v97\n"
    "v_mfma_f32_16x16x32_bf16 v[100:103], a[108:111], a[60:63], v[100:103]\n"
    "s_waitcnt lgkmcnt(0)\n"
    "v_mfma_f32_16x16x32_bf16 v[172:175], a[136:139], v[0:3], v[172:175]\n"
    "v_exp_f32 v98, v98\n"
    "v_mfma_f32_16x16x32_bf16 v[172:175], a[140:143], v[4:7], v[172:175]\n"
    "ds_read_b128 a[120:123], v128 offset:20992\n"
    "v_cvt_pk_bf16_f32 v64, v96, v97\n"
    "v_mfma_f32_16x16x32_bf16 v[176:179], a[140:143], v[12:15], v[176:179]\n"
    "v_exp_f32 v99, v99\n"
    "v_mfma_f32_16x16x32_bf16 v[176:179], a[136:139], v[8:11], v[176:179]\n"
    "ds_read_b128 a[124:127], v128 offset:21248\n"
    "v_cvt_pk_bf16_f32 v65, v98, v99\n"
    "v_smfmac_f32_16x16x64_bf16 a[144:147], a[168:171], v[0:7], v133\n"
    "v_mfma_f32_16x16x32_bf16 v[96:99], a[96:99], a[64:67], v[120:123]\n"
    "v_exp_f32 v100, v100\n"
    "v_mfma_f32_16x16x32_bf16 v[96:99], a[100:103], a[68:71], v[96:99]\n"
    "ds_read_b64_tr_b16 a[128:129], v129 offset:33024\n"
    "v_mfma_f32_16x16x32_bf16 v[96:99], a[104:107], a[72:75], v[96:99]\n"
    "v_exp_f32 v101, v101\n"
    "v_mfma_f32_16x16x32_bf16 v[96:99], a[108:111], a[76:79], v[96:99]\n"
    "ds_read_b64_tr_b16 a[130:131], v129 offset:37120\n"
    "v_mfma_f32_16x16x32_bf16 v[180:183], a[136:139], v[16:19], v[180:183]\n"
    "v_exp_f32 v102, v102\n"
    "v_mfma_f32_16x16x32_bf16 v[180:183], a[140:143], v[20:23], v[180:183]\n"
    "v_cvt_pk_bf16_f32 v72, v100, v101\n"
    "v_mfma_f32_16x16x32_bf16 v[184:187], a[140:143], v[28:31], v[184:187]\n"
    "v_exp_f32 v103, v103\n"
    "v_mfma_f32_16x16x32_bf16 v[184:187], a[136:139], v[24:27], v[184:187]\n"
    "v_cvt_pk_bf16_f32 v73, v102, v103\n"
    "v_mfma_f32_16x16x32_bf16 v[100:103], a[96:99], a[80:83], v[124:127]\n"
    "v_exp_f32 v96, v96\n"
    "v_mfma_f32_16x16x32_bf16 v[100:103], a[100:103], a[84:87], v[100:103]\n"
    "ds_read_b64_tr_b16 a[132:133], v129 offset:41216\n"
    "v_mfma_f32_16x16x32_bf16 v[100:103], a[104:107], a[88:91], v[100:103]\n"
    "v_exp_f32 v97, v97\n"
    "v_mfma_f32_16x16x32_bf16 v[100:103], a[108:111], a[92:95], v[100:103]\n"
    "ds_read_b64_tr_b16 a[134:135], v129 offset:45312\n"
    "v_mfma_f32_16x16x32_bf16 v[188:191], a[136:139], v[32:35], v[188:191]\n"
    "v_exp_f32 v98, v98\n"
    "v_mfma_f32_16x16x32_bf16 v[188:191], a[140:143], v[36:39], v[188:191]\n"
    "v_cvt_pk_bf16_f32 v80, v96, v97\n"
    "v_mfma_f32_16x16x32_bf16 v[192:195], a[140:143], v[44:47], v[192:195]\n"
    "v_exp_f32 v99, v99\n"
    "v_mfma_f32_16x16x32_bf16 v[192:195], a[136:139], v[40:43], v[192:195]\n"
    "s_waitcnt lgkmcnt(4)\n"
    "v_cvt_pk_bf16_f32 v81, v98, v99\n"
    "v_mfma_f32_16x16x32_bf16 v[96:99], a[112:115], a[0:3], v[104:107]\n"
    "s_add_u32 s56, s56, 16384\n"
    "v_exp_f32 v100, v100\n"
    "v_mfma_f32_16x16x32_bf16 v[96:99], a[116:119], a[4:7], v[96:99]\n"
    "s_and_b32 s56, s56, 262143\n"
    "v_mfma_f32_16x16x32_bf16 v[96:99], a[120:123], a[8:11], v[96:99]\n"
    "s_or_b32 s56, s56, s61\n"
    "v_exp_f32 v101, v101\n"
    "v_mfma_f32_16x16x32_bf16 v[96:99], a[124:127], a[12:15], v[96:99]\n"
    "s_waitcnt lgkmcnt(0)\n"
    "v_mfma_f32_16x16x32_bf16 v[196:199], a[128:131], v[0:3], v[196:199]\n"
    "s_add_u32 s94, s56, 8192\n"
    "v_exp_f32 v102, v102\n"
    "v_mfma_f32_16x16x32_bf16 v[196:199], a[132:135], v[4:7], v[196:199]\n"
    "s_mov_b32 s96, 67108864\n"
    "v_cvt_pk_bf16_f32 v88, v100, v101\n"
    "v_mfma_f32_16x16x32_bf16 v[200:203], a[132:135], v[12:15], v[200:203]\n"
    "s_add_u32 s57, s57, 16384\n"
    "v_exp_f32 v103, v103\n"
    "v_mfma_f32_16x16x32_bf16 v[200:203], a[128:131], v[8:11], v[200:203]\n"
    "s_and_b32 s57, s57, 262143\n"
    "v_cvt_pk_bf16_f32 v89, v102, v103\n"
    "v_mfma_f32_16x16x32_bf16 v[100:103], a[112:115], a[16:19], v[108:111]\n"
    "s_or_b32 s57, s57, s96\n"
    "v_exp_f32 v96, v96\n"
    "v_mfma_f32_16x16x32_bf16 v[100:103], a[116:119], a[20:23], v[100:103]\n"
    "s_add_u32 s70, s57, 8192\n"
    "ds_read_b64_tr_b16 a[136:137], v130 offset:33024\n"
    "v_mfma_f32_16x16x32_bf16 v[100:103], a[120:123], a[24:27], v[100:103]\n"
    "v_exp_f32 v97, v97\n"
    "v_mfma_f32_16x16x32_bf16 v[100:103], a[124:127], a[28:31], v[100:103]\n"
    "ds_read_b64_tr_b16 a[138:139], v130 offset:37120\n"
    "v_mfma_f32_16x16x32_bf16 v[204:207], a[128:131], v[16:19], v[204:207]\n"
    "v_exp_f32 v98, v98\n"
    "v_mfma_f32_16x16x32_bf16 v[204:207], a[132:135], v[20:23], v[204:207]\n"
    "v_cvt_pk_bf16_f32 v50, v96, v97\n"
    "v_mfma_f32_16x16x32_bf16 v[208:211], a[132:135], v[28:31], v[208:211]\n"
    "v_exp_f32 v99, v99\n"
    "v_mfma_f32_16x16x32_bf16 v[208:211], a[128:131], v[24:27], v[208:211]\n"
    "v_cvt_pk_bf16_f32 v51, v98, v99\n"
    "v_smfmac_f32_16x16x64_bf16 a[148:151], a[168:171], v[8:15], v133\n"
    "v_mfma_f32_16x16x32_bf16 v[96:99], a[112:115], a[32:35], v[112:115]\n"
    "v_exp_f32 v100, v100\n"
    "v_mfma_f32_16x16x32_bf16 v[96:99], a[116:119], a[36:39], v[96:99]\n"
    "ds_read_b64_tr_b16 a[140:141], v130 offset:41216\n"
    "v_mfma_f32_16x16x32_bf16 v[96:99], a[120:123], a[40:43], v[96:99]\n"
    "v_exp_f32 v101, v101\n"
    "v_mfma_f32_16x16x32_bf16 v[96:99], a[124:127], a[44:47], v[96:99]\n"
    "ds_read_b64_tr_b16 a[142:143], v130 offset:45312\n"
    "v_mfma_f32_16x16x32_bf16 v[212:215], a[128:131], v[32:35], v[212:215]\n"
    "v_exp_f32 v102, v102\n"
    "v_mfma_f32_16x16x32_bf16 v[212:215], a[132:135], v[36:39], v[212:215]\n"
    "v_cvt_pk_bf16_f32 v58, v100, v101\n"
    "v_mfma_f32_16x16x32_bf16 v[216:219], a[132:135], v[44:47], v[216:219]\n"
    "v_exp_f32 v103, v103\n"
    "v_mfma_f32_16x16x32_bf16 v[216:219], a[128:131], v[40:43], v[216:219]\n"
    "v_cvt_pk_bf16_f32 v59, v102, v103\n"
    "v_mfma_f32_16x16x32_bf16 v[100:103], a[112:115], a[48:51], v[116:119]\n"
    "v_exp_f32 v96, v96\n"
    "v_mfma_f32_16x16x32_bf16 v[100:103], a[116:119], a[52:55], v[100:103]\n"
    "v_mfma_f32_16x16x32_bf16 v[100:103], a[120:123], a[56:59], v[100:103]\n"
    "s_waitcnt vmcnt(8) lgkmcnt(0)\n"
    "v_exp_f32 v97, v97\n"
    "v_mfma_f32_16x16x32_bf16 v[100:103], a[124:127], a[60:63], v[100:103]\n"
    "s_barrier\n"
    "v_mfma_f32_16x16x32_bf16 v[220:223], a[136:139], v[0:3], v[220:223]\n"
    "v_exp_f32 v98, v98\n"
    "v_mfma_f32_16x16x32_bf16 v[220:223], a[140:143], v[4:7], v[220:223]\n"
    "ds_read_b128 a[96:99], v128 offset:24576\n"
    "v_cvt_pk_bf16_f32 v66, v96, v97\n"
    "v_mfma_f32_16x16x32_bf16 v[224:227], a[140:143], v[12:15], v[224:227]\n"
    "v_exp_f32 v99, v99\n"
    "v_mfma_f32_16x16x32_bf16 v[224:227], a[136:139], v[8:11], v[224:227]\n"
    "ds_read_b128 a[100:103], v128 offset:24832\n"
    "v_cvt_pk_bf16_f32 v67, v98, v99\n"
    "v_mfma_f32_16x16x32_bf16 v[96:99], a[112:115], a[64:67], v[120:123]\n"
    "v_exp_f32 v100, v100\n"
    "v_mfma_f32_16x16x32_bf16 v[96:99], a[116:119], a[68:71], v[96:99]\n"
    "ds_read_b64_tr_b16 a[128:129], v129 offset:33280\n"
    "ds_read_b64_tr_b16 a[130:131], v129 offset:37376\n"
    "v_mfma_f32_16x16x32_bf16 v[96:99], a[120:123], a[72:75], v[96:99]\n"
    "s_mov_b32 m0, s82\n"
    "v_exp_f32 v101, v101\n"
    "v_mfma_f32_16x16x32_bf16 v[96:99], a[124:127], a[76:79], v[96:99]\n"
    "buffer_load_dwordx4 v131, s[40:43], s56 offen lds\n"
    "v_mfma_f32_16x16x32_bf16 v[228:231], a[136:139], v[16:19], v[228:231]\n"
    "v_exp_f32 v102, v102\n"
    "v_mfma_f32_16x16x32_bf16 v[228:231], a[140:143], v[20:23], v[228:231]\n"
    "ds_read_b128 a[104:107], v128 offset:25088\n"
    "v_cvt_pk_bf16_f32 v74, v100, v101\n"
    "v_mfma_f32_16x16x32_bf16 v[232:235], a[140:143], v[28:31], v[232:235]\n"
    "v_exp_f32 v103, v103\n"
    "v_mfma_f32_16x16x32_bf16 v[232:235], a[136:139], v[24:27], v[232:235]\n"
    "ds_read_b128 a[108:111], v128 offset:25344\n"
    "v_cvt_pk_bf16_f32 v75, v102, v103\n"
    "v_mfma_f32_16x16x32_bf16 v[100:103], a[112:115], a[80:83], v[124:127]\n"
    "v_exp_f32 v96, v96\n"
    "v_mfma_f32_16x16x32_bf16 v[100:103], a[116:119], a[84:87], v[100:103]\n"
    "ds_read_b64_tr_b16 a[132:133], v129 offset:41472\n"
    "ds_read_b64_tr_b16 a[134:135], v129 offset:45568\n"
    "v_mfma_f32_16x16x32_bf16 v[100:103], a[120:123], a[88:91], v[100:103]\n"
    "v_exp_f32 v97, v97\n"
    "v_mfma_f32_16x16x32_bf16 v[100:103], a[124:127], a[92:95], v[100:103]\n"
    "buffer_load_dwordx4 v131, s[40:43], s56 offen offset:2048 lds\n"
    "v_mfma_f32_16x16x32_bf16 v[236:239], a[136:139], v[32:35], v[236:239]\n"
    "v_exp_f32 v98, v98\n"
    "v_mfma_f32_16x16x32_bf16 v[236:239], a[140:143], v[36:39], v[236:239]\n"
    "v_cvt_pk_bf16_f32 v82, v96, v97\n"
    "v_mfma_f32_16x16x32_bf16 v[240:243], a[140:143], v[44:47], v[240:243]\n"
    "v_exp_f32 v99, v99\n"
    "v_mfma_f32_16x16x32_bf16 v[240:243], a[136:139], v[40:43], v[240:243]\n"
    "s_waitcnt lgkmcnt(2)\n"
    "v_cvt_pk_bf16_f32 v83, v98, v99\n"
    "v_smfmac_f32_16x16x64_bf16 a[152:155], a[168:171], v[16:23], v133\n"
    "v_mfma_f32_16x16x32_bf16 v[96:99], a[96:99], a[0:3], v[104:107]\n"
    "v_exp_f32 v100, v100\n"
    "v_mfma_f32_16x16x32_bf16 v[96:99], a[100:103], a[4:7], v[96:99]\n"
    "v_mbcnt_lo_u32_b32 v136, -1, 0\n"
    "v_mbcnt_hi_u32_b32 v136, -1, v136\n"
    "v_mfma_f32_16x16x32_bf16 v[96:99], a[104:107], a[8:11], v[96:99]\n"
    "v_exp_f32 v101, v101\n"
    "v_mfma_f32_16x16x32_bf16 v[96:99], a[108:111], a[12:15], v[96:99]\n"
    "s_waitcnt lgkmcnt(0)\n"
    "v_bfe_u32 v137, v136, 2, 2\n"
    "v_lshlrev_b32 v134, 10, v137\n"
    "v_mfma_f32_16x16x32_bf16 v[244:247], a[128:131], v[0:3], v[244:247]\n"
    "v_exp_f32 v102, v102\n"
    "v_mfma_f32_16x16x32_bf16 v[244:247], a[132:135], v[4:7], v[244:247]\n"
    "ds_read_b128 a[112:115], v128 offset:28672\n"
    "v_cvt_pk_bf16_f32 v90, v100, v101\n"
    "v_mfma_f32_16x16x32_bf16 v[248:251], a[132:135], v[12:15], v[248:251]\n"
    "v_exp_f32 v103, v103\n"
    "v_mfma_f32_16x16x32_bf16 v[248:251], a[128:131], v[8:11], v[248:251]\n"
    "ds_read_b128 a[116:119], v128 offset:28928\n"
    "v_cvt_pk_bf16_f32 v91, v102, v103\n"
    "v_mfma_f32_16x16x32_bf16 v[100:103], a[96:99], a[16:19], v[108:111]\n"
    "v_exp_f32 v96, v96\n"
    "v_mfma_f32_16x16x32_bf16 v[100:103], a[100:103], a[20:23], v[100:103]\n"
    "ds_read_b64_tr_b16 a[136:137], v130 offset:33280\n"
    "v_and_b32 v137, 3, v136\n"
    "v_mfma_f32_16x16x32_bf16 v[100:103], a[104:107], a[24:27], v[100:103]\n"
    "v_exp_f32 v97, v97\n"
    "v_mfma_f32_16x16x32_bf16 v[100:103], a[108:111], a[28:31], v[100:103]\n"
    "ds_read_b64_tr_b16 a[138:139], v130 offset:37376\n"
    "v_lshl_add_u32 v134, v137, 6, v134\n"
    "v_mfma_f32_16x16x32_bf16 v[252:255], a[128:131], v[16:19], v[252:255]\n"
    "v_exp_f32 v98, v98\n"
    "v_mfma_f32_16x16x32_bf16 v[252:255], a[132:135], v[20:23], v[252:255]\n"
    "v_cvt_pk_bf16_f32 v52, v96, v97\n"
    "v_and_b32 v137, 48, v136\n"
    "v_mfma_f32_16x16x32_bf16 a[172:175], a[132:135], v[28:31], a[172:175]\n"
    "v_exp_f32 v99, v99\n"
    "v_mfma_f32_16x16x32_bf16 a[172:175], a[128:131], v[24:27], a[172:175]\n"
    "v_cvt_pk_bf16_f32 v53, v98, v99\n"
    "v_xor_b32 v137, s71, v137\n"
    "v_mfma_f32_16x16x32_bf16 v[96:99], a[96:99], a[32:35], v[112:115]\n"
    "v_exp_f32 v100, v100\n"
    "v_mfma_f32_16x16x32_bf16 v[96:99], a[100:103], a[36:39], v[96:99]\n"
    "ds_read_b64_tr_b16 a[140:141], v130 offset:41472\n"
    "v_add_u32 v134, v137, v134\n"
    "v_mfma_f32_16x16x32_bf16 v[96:99], a[104:107], a[40:43], v[96:99]\n"
    "v_exp_f32 v101, v101\n"
    "v_mfma_f32_16x16x32_bf16 v[96:99], a[108:111], a[44:47], v[96:99]\n"
    "ds_read_b64_tr_b16 a[142:143], v130 offset:45568\n"
    "v_add_u32 v134, s62, v134\n"
    "v_mfma_f32_16x16x32_bf16 a[176:179], a[128:131], v[32:35], a[176:179]\n"
    "v_exp_f32 v102, v102\n"
    "v_mfma_f32_16x16x32_bf16 a[176:179], a[132:135], v[36:39], a[176:179]\n"
    "v_cvt_pk_bf16_f32 v60, v100, v101\n"
    "v_mfma_f32_16x16x32_bf16 a[180:183], a[132:135], v[44:47], a[180:183]\n"
    "v_exp_f32 v103, v103\n"
    "v_mfma_f32_16x16x32_bf16 a[180:183], a[128:131], v[40:43], a[180:183]\n"
    "v_cvt_pk_bf16_f32 v61, v102, v103\n"
    "v_mfma_f32_16x16x32_bf16 v[100:103], a[96:99], a[48:51], v[116:119]\n"
    "v_exp_f32 v96, v96\n"
    "v_mfma_f32_16x16x32_bf16 v[100:103], a[100:103], a[52:55], v[100:103]\n"
    "v_mfma_f32_16x16x32_bf16 v[100:103], a[104:107], a[56:59], v[100:103]\n"
    "v_exp_f32 v97, v97\n"
    "v_mfma_f32_16x16x32_bf16 v[100:103], a[108:111], a[60:63], v[100:103]\n"
    "s_waitcnt lgkmcnt(0)\n"
    "v_mfma_f32_16x16x32_bf16 a[184:187], a[136:139], v[0:3], a[184:187]\n"
    "v_exp_f32 v98, v98\n"
    "v_mfma_f32_16x16x32_bf16 a[184:187], a[140:143], v[4:7], a[184:187]\n"
    "ds_read_b128 a[120:123], v128 offset:29184\n"
    "v_cvt_pk_bf16_f32 v68, v96, v97\n"
    "v_mfma_f32_16x16x32_bf16 a[188:191], a[140:143], v[12:15], a[188:191]\n"
    "v_exp_f32 v99, v99\n"
    "v_mfma_f32_16x16x32_bf16 a[188:191], a[136:139], v[8:11], a[188:191]\n"
    "ds_read_b128 a[124:127], v128 offset:29440\n"
    "v_cvt_pk_bf16_f32 v69, v98, v99\n"
    "v_smfmac_f32_16x16x64_bf16 a[156:159], a[168:171], v[24:31], v133\n"
    "v_mfma_f32_16x16x32_bf16 v[96:99], a[96:99], a[64:67], v[120:123]\n"
    "v_exp_f32 v100, v100\n"
    "v_mfma_f32_16x16x32_bf16 v[96:99], a[100:103], a[68:71], v[96:99]\n"
    "ds_read_b64_tr_b16 a[128:129], v129 offset:33536\n"
    "v_mfma_f32_16x16x32_bf16 v[96:99], a[104:107], a[72:75], v[96:99]\n"
    "v_exp_f32 v101, v101\n"
    "v_mfma_f32_16x16x32_bf16 v[96:99], a[108:111], a[76:79], v[96:99]\n"
    "ds_read_b64_tr_b16 a[130:131], v129 offset:37632\n"
    "v_mfma_f32_16x16x32_bf16 a[192:195], a[136:139], v[16:19], a[192:195]\n"
    "v_exp_f32 v102, v102\n"
    "v_mfma_f32_16x16x32_bf16 a[192:195], a[140:143], v[20:23], a[192:195]\n"
    "v_cvt_pk_bf16_f32 v76, v100, v101\n"
    "v_mfma_f32_16x16x32_bf16 a[196:199], a[140:143], v[28:31], a[196:199]\n"
    "v_exp_f32 v103, v103\n"
    "v_mfma_f32_16x16x32_bf16 a[196:199], a[136:139], v[24:27], a[196:199]\n"
    "v_cvt_pk_bf16_f32 v77, v102, v103\n"
    "v_mfma_f32_16x16x32_bf16 v[100:103], a[96:99], a[80:83], v[124:127]\n"
    "v_exp_f32 v96, v96\n"
    "v_mfma_f32_16x16x32_bf16 v[100:103], a[100:103], a[84:87], v[100:103]\n"
    "ds_read_b64_tr_b16 a[132:133], v129 offset:41728\n"
    "v_mfma_f32_16x16x32_bf16 v[100:103], a[104:107], a[88:91], v[100:103]\n"
    "v_exp_f32 v97, v97\n"
    "v_mfma_f32_16x16x32_bf16 v[100:103], a[108:111], a[92:95], v[100:103]\n"
    "ds_read_b64_tr_b16 a[134:135], v129 offset:45824\n"
    "v_mfma_f32_16x16x32_bf16 a[200:203], a[136:139], v[32:35], a[200:203]\n"
    "v_exp_f32 v98, v98\n"
    "v_mfma_f32_16x16x32_bf16 a[200:203], a[140:143], v[36:39], a[200:203]\n"
    "v_cvt_pk_bf16_f32 v84, v96, v97\n"
    "v_mfma_f32_16x16x32_bf16 a[204:207], a[140:143], v[44:47], a[204:207]\n"
    "v_exp_f32 v99, v99\n"
    "v_mfma_f32_16x16x32_bf16 a[204:207], a[136:139], v[40:43], a[204:207]\n"
    "s_waitcnt lgkmcnt(4)\n"
    "v_cvt_pk_bf16_f32 v85, v98, v99\n"
    "v_mfma_f32_16x16x32_bf16 v[96:99], a[112:115], a[0:3], v[104:107]\n"
    "v_exp_f32 v100, v100\n"
    "v_mfma_f32_16x16x32_bf16 v[96:99], a[116:119], a[4:7], v[96:99]\n"
    "v_mfma_f32_16x16x32_bf16 v[96:99], a[120:123], a[8:11], v[96:99]\n"
    "v_exp_f32 v101, v101\n"
    "v_mfma_f32_16x16x32_bf16 v[96:99], a[124:127], a[12:15], v[96:99]\n"
    "s_waitcnt lgkmcnt(0)\n"
    "ds_read_b128 a[0:3], v134 offset:0\n"
    "ds_read_b128 a[4:7], v134 offset:256\n"
    "v_mfma_f32_16x16x32_bf16 a[208:211], a[128:131], v[0:3], a[208:211]\n"
    "v_exp_f32 v102, v102\n"
    "v_mfma_f32_16x16x32_bf16 a[208:211], a[132:135], v[4:7], a[208:211]\n"
    "ds_read_b128 a[8:11], v134 offset:512\n"
    "v_cvt_pk_bf16_f32 v92, v100, v101\n"
    "v_mfma_f32_16x16x32_bf16 a[212:215], a[132:135], v[12:15], a[212:215]\n"
    "v_exp_f32 v103, v103\n"
    "v_mfma_f32_16x16x32_bf16 a[212:215], a[128:131], v[8:11], a[212:215]\n"
    "ds_read_b128 a[12:15], v134 offset:768\n"
    "v_cvt_pk_bf16_f32 v93, v102, v103\n"
    "v_mfma_f32_16x16x32_bf16 v[100:103], a[112:115], a[16:19], v[108:111]\n"
    "s_waitcnt lgkmcnt(8)\n"
    "v_exp_f32 v96, v96\n"
    "v_mfma_f32_16x16x32_bf16 v[100:103], a[116:119], a[20:23], v[100:103]\n"
    "ds_read_b64_tr_b16 a[136:137], v130 offset:33536\n"
    "v_mfma_f32_16x16x32_bf16 v[100:103], a[120:123], a[24:27], v[100:103]\n"
    "v_exp_f32 v97, v97\n"
    "v_mfma_f32_16x16x32_bf16 v[100:103], a[124:127], a[28:31], v[100:103]\n"
    "ds_read_b64_tr_b16 a[138:139], v130 offset:37632\n"
    "ds_read_b128 a[16:19], v134 offset:4096\n"
    "v_mfma_f32_16x16x32_bf16 a[216:219], a[128:131], v[16:19], a[216:219]\n"
    "s_waitcnt lgkmcnt(8)\n"
    "v_exp_f32 v98, v98\n"
    "v_mfma_f32_16x16x32_bf16 a[216:219], a[132:135], v[20:23], a[216:219]\n"
    "ds_read_b128 a[20:23], v134 offset:4352\n"
    "v_cvt_pk_bf16_f32 v54, v96, v97\n"
    "v_mfma_f32_16x16x32_bf16 a[220:223], a[132:135], v[28:31], a[220:223]\n"
    "v_exp_f32 v99, v99\n"
    "v_mfma_f32_16x16x32_bf16 a[220:223], a[128:131], v[24:27], a[220:223]\n"
    "ds_read_b128 a[24:27], v134 offset:4608\n"
    "v_cvt_pk_bf16_f32 v55, v98, v99\n"
    "v_smfmac_f32_16x16x64_bf16 a[160:163], a[168:171], v[32:39], v133\n"
    "ds_read_b128 a[28:31], v134 offset:4864\n"
    "v_mfma_f32_16x16x32_bf16 v[96:99], a[112:115], a[32:35], v[112:115]\n"
    "s_waitcnt lgkmcnt(8)\n"
    "v_exp_f32 v100, v100\n"
    "v_mfma_f32_16x16x32_bf16 v[96:99], a[116:119], a[36:39], v[96:99]\n"
    "ds_read_b64_tr_b16 a[140:141], v130 offset:41728\n"
    "v_mfma_f32_16x16x32_bf16 v[96:99], a[120:123], a[40:43], v[96:99]\n"
    "v_exp_f32 v101, v101\n"
    "v_mfma_f32_16x16x32_bf16 v[96:99], a[124:127], a[44:47], v[96:99]\n"
    "ds_read_b64_tr_b16 a[142:143], v130 offset:45824\n"
    "ds_read_b128 a[32:35], v134 offset:8192\n"
    "v_mfma_f32_16x16x32_bf16 a[224:227], a[128:131], v[32:35], a[224:227]\n"
    "s_waitcnt lgkmcnt(8)\n"
    "v_exp_f32 v102, v102\n"
    "v_mfma_f32_16x16x32_bf16 a[224:227], a[132:135], v[36:39], a[224:227]\n"
    "ds_read_b128 a[36:39], v134 offset:8448\n"
    "v_cvt_pk_bf16_f32 v62, v100, v101\n"
    "v_mfma_f32_16x16x32_bf16 a[228:231], a[132:135], v[44:47], a[228:231]\n"
    "v_exp_f32 v103, v103\n"
    "v_mfma_f32_16x16x32_bf16 a[228:231], a[128:131], v[40:43], a[228:231]\n"
    "ds_read_b128 a[40:43], v134 offset:8704\n"
    "v_cvt_pk_bf16_f32 v63, v102, v103\n"
    "v_mfma_f32_16x16x32_bf16 v[100:103], a[112:115], a[48:51], v[116:119]\n"
    "v_exp_f32 v96, v96\n"
    "v_mfma_f32_16x16x32_bf16 v[100:103], a[116:119], a[52:55], v[100:103]\n"
    "ds_read_b128 a[44:47], v134 offset:8960\n"
    "v_mfma_f32_16x16x32_bf16 v[100:103], a[120:123], a[56:59], v[100:103]\n"
    "s_waitcnt vmcnt(4) lgkmcnt(4)\n"
    "v_exp_f32 v97, v97\n"
    "v_mfma_f32_16x16x32_bf16 v[100:103], a[124:127], a[60:63], v[100:103]\n"
    "s_barrier\n"
    "v_mfma_f32_16x16x32_bf16 a[232:235], a[136:139], v[0:3], a[232:235]\n"
    "v_exp_f32 v98, v98\n"
    "v_mfma_f32_16x16x32_bf16 a[232:235], a[140:143], v[4:7], a[232:235]\n"
    "ds_read_b128 a[96:99], v128 offset:0\n"
    "v_cvt_pk_bf16_f32 v70, v96, v97\n"
    "v_mfma_f32_16x16x32_bf16 a[236:239], a[140:143], v[12:15], a[236:239]\n"
    "v_exp_f32 v99, v99\n"
    "v_mfma_f32_16x16x32_bf16 a[236:239], a[136:139], v[8:11], a[236:239]\n"
    "ds_read_b128 a[100:103], v128 offset:256\n"
    "v_cvt_pk_bf16_f32 v71, v98, v99\n"
    "v_mfma_f32_16x16x32_bf16 v[96:99], a[112:115], a[64:67], v[120:123]\n"
    "s_waitcnt lgkmcnt(8)\n"
    "v_exp_f32 v100, v100\n"
    "v_mfma_f32_16x16x32_bf16 v[96:99], a[116:119], a[68:71], v[96:99]\n"
    "ds_read_b64_tr_b16 a[128:129], v129 offset:49152\n"
    "ds_read_b64_tr_b16 a[130:131], v129 offset:53248\n"
    "v_mfma_f32_16x16x32_bf16 v[96:99], a[120:123], a[72:75], v[96:99]\n"
    "s_mov_b32 m0, s84\n"
    "v_exp_f32 v101, v101\n"
    "v_mfma_f32_16x16x32_bf16 v[96:99], a[124:127], a[76:79], v[96:99]\n"
    "buffer_load_dwordx4 v131, s[44:47], s57 offen nt lds\n"
    "v_mfma_f32_16x16x32_bf16 a[240:243], a[136:139], v[16:19], a[240:243]\n"
    "s_waitcnt lgkmcnt(8)\n"
    "v_exp_f32 v102, v102\n"
    "v_mfma_f32_16x16x32_bf16 a[240:243], a[140:143], v[20:23], a[240:243]\n"
    "ds_read_b128 a[104:107], v128 offset:512\n"
    "v_cvt_pk_bf16_f32 v78, v100, v101\n"
    "v_mfma_f32_16x16x32_bf16 a[244:247], a[140:143], v[28:31], a[244:247]\n"
    "v_exp_f32 v103, v103\n"
    "v_mfma_f32_16x16x32_bf16 a[244:247], a[136:139], v[24:27], a[244:247]\n"
    "ds_read_b128 a[108:111], v128 offset:768\n"
    "v_cvt_pk_bf16_f32 v79, v102, v103\n"
    "v_mfma_f32_16x16x32_bf16 v[100:103], a[112:115], a[80:83], v[124:127]\n"
    "s_waitcnt lgkmcnt(8)\n"
    "v_exp_f32 v96, v96\n"
    "v_mfma_f32_16x16x32_bf16 v[100:103], a[116:119], a[84:87], v[100:103]\n"
    "ds_read_b64_tr_b16 a[132:133], v129 offset:57344\n"
    "ds_read_b64_tr_b16 a[134:135], v129 offset:61440\n"
    "v_mfma_f32_16x16x32_bf16 v[100:103], a[120:123], a[88:91], v[100:103]\n"
    "v_exp_f32 v97, v97\n"
    "v_mfma_f32_16x16x32_bf16 v[100:103], a[124:127], a[92:95], v[100:103]\n"
    "buffer_load_dwordx4 v131, s[44:47], s57 offen offset:2048 nt lds\n"
    "v_mfma_f32_16x16x32_bf16 a[248:251], a[136:139], v[32:35], a[248:251]\n"
    "s_waitcnt lgkmcnt(8)\n"
    "v_exp_f32 v98, v98\n"
    "v_mfma_f32_16x16x32_bf16 a[248:251], a[140:143], v[36:39], a[248:251]\n"
    "ds_read_b128 a[48:51], v134 offset:12288\n"
    "v_cvt_pk_bf16_f32 v86, v96, v97\n"
    "v_mfma_f32_16x16x32_bf16 a[252:255], a[140:143], v[44:47], a[252:255]\n"
    "v_exp_f32 v99, v99\n"
    "v_mfma_f32_16x16x32_bf16 a[252:255], a[136:139], v[40:43], a[252:255]\n"
    "s_waitcnt lgkmcnt(3)\n"
    "ds_read_b128 a[52:55], v134 offset:12544\n"
    "v_cvt_pk_bf16_f32 v87, v98, v99\n"
    "v_smfmac_f32_16x16x64_bf16 a[164:167], a[168:171], v[40:47], v133\n"
    "s_waitcnt lgkmcnt(15)\n"
    "ds_read_b128 a[56:59], v134 offset:12800\n"
    "ds_read_b128 a[60:63], v134 offset:13056\n"
    "v_mfma_f32_16x16x32_bf16 v[96:99], a[96:99], a[0:3], 0\n"
    "s_mov_b32 m0, s85\n"
    "v_exp_f32 v100, v100\n"
    "v_mfma_f32_16x16x32_bf16 v[96:99], a[100:103], a[4:7], v[96:99]\n"
    "s_waitcnt lgkmcnt(8)\n"
    "buffer_load_dwordx4 v131, s[44:47], s70 offen nt lds\n"
    "v_mfma_f32_16x16x32_bf16 v[96:99], a[104:107], a[8:11], v[96:99]\n"
    "v_exp_f32 v101, v101\n"
    "v_mfma_f32_16x16x32_bf16 v[96:99], a[108:111], a[12:15], v[96:99]\n"
    "s_waitcnt lgkmcnt(4)\n"
    "ds_read_b128 a[64:67], v134 offset:16384\n"
    "ds_read_b128 a[68:71], v134 offset:16640\n"
    "v_mfma_f32_16x16x32_bf16 v[148:151], a[128:131], v[48:51], v[148:151]\n"
    "v_exp_f32 v102, v102\n"
    "v_mfma_f32_16x16x32_bf16 v[148:151], a[132:135], v[52:55], v[148:151]\n"
    "s_waitcnt lgkmcnt(8)\n"
    "ds_read_b128 a[112:115], v128 offset:4096\n"
    "v_cvt_pk_bf16_f32 v94, v100, v101\n"
    "v_mfma_f32_16x16x32_bf16 v[152:155], a[132:135], v[60:63], v[152:155]\n"
    "v_exp_f32 v103, v103\n"
    "v_mfma_f32_16x16x32_bf16 v[152:155], a[128:131], v[56:59], v[152:155]\n"
    "ds_read_b128 a[116:119], v128 offset:4352\n"
    "v_cvt_pk_bf16_f32 v95, v102, v103\n"
    "v_smfmac_f32_16x16x64_bf16 a[144:147], a[168:171], v[48:55], v133\n"
    "s_waitcnt lgkmcnt(15)\n"
    "ds_read_b128 a[72:75], v134 offset:16896\n"
    "v_mfma_f32_16x16x32_bf16 v[100:103], a[96:99], a[16:19], 0\n"
    "s_waitcnt lgkmcnt(8)\n"
    "ds_read_b128 a[76:79], v134 offset:17152\n"
    "v_max3_f32 v136, v96, v97, v98\n"
    "v_max_f32 v136, v136, v99\n"
    "v_mov_b32 v137, v136\n"
    "v_mfma_f32_16x16x32_bf16 v[100:103], a[100:103], a[20:23], v[100:103]\n"
    "ds_read_b64_tr_b16 a[136:137], v130 offset:49152\n"
    "ds_read_b64_tr_b16 a[138:139], v130 offset:53248\n"
    "v_permlane32_swap_b32 v137, v136\n"
    "v_mfma_f32_16x16x32_bf16 v[100:103], a[104:107], a[24:27], v[100:103]\n"
    "ds_read_b128 a[80:83], v134 offset:20480\n"
    "v_max_f32 v136, v136, v137\n"
    "v_mov_b32 v137, v136\n"
    "s_nop 1\n"
    "v_permlane16_swap_b32 v137, v136\n"
    "v_mfma_f32_16x16x32_bf16 v[100:103], a[108:111], a[28:31], v[100:103]\n"
    "buffer_load_dwordx4 v131, s[44:47], s70 offen offset:2048 nt lds\n"
    "v_max_f32 v136, v136, v137\n"
    "v_add_f32 v136, 0x41200000, v136\n"
    "v_mfma_f32_16x16x32_bf16 v[156:159], a[128:131], v[64:67], v[156:159]\n"
    "ds_read_b128 a[84:87], v134 offset:20736\n"
    "v_sub_f32 v96, v96, v136\n"
    "v_sub_f32 v97, v97, v136\n"
    "v_sub_f32 v98, v98, v136\n"
    "v_mfma_f32_16x16x32_bf16 v[156:159], a[132:135], v[68:71], v[156:159]\n"
    "s_waitcnt lgkmcnt(8)\n"
    "ds_read_b128 a[88:91], v134 offset:20992\n"
    "v_sub_f32 v99, v99, v136\n"
    "v_exp_f32 v96, v96\n"
    "v_mfma_f32_16x16x32_bf16 v[160:163], a[132:135], v[76:79], v[160:163]\n"
    "ds_read_b128 a[92:95], v134 offset:21248\n"
    "v_sub_f32 v104, 0, v136\n"
    "v_exp_f32 v97, v97\n"
    "v_sub_f32 v105, 0, v136\n"
    "v_mfma_f32_16x16x32_bf16 v[160:163], a[128:131], v[72:75], v[160:163]\n"
    "v_exp_f32 v98, v98\n"
    "v_cvt_pk_bf16_f32 v0, v96, v97\n"
    "v_sub_f32 v106, 0, v136\n"
    "v_smfmac_f32_16x16x64_bf16 a[148:151], a[168:171], v[56:63], v133\n"
    "s_waitcnt lgkmcnt(15)\n"
    "v_exp_f32 v99, v99\n"
    "v_sub_f32 v107, 0, v136\n"
    "v_cvt_pk_bf16_f32 v1, v98, v99\n"
    "v_mfma_f32_16x16x32_bf16 v[96:99], a[96:99], a[32:35], 0\n"
    "s_waitcnt lgkmcnt(8)\n"
    "v_max3_f32 v136, v100, v101, v102\n"
    "v_accvgpr_read_b32 v138, a144\n"
    "v_max_f32 v136, v136, v103\n"
    "v_mov_b32 v137, v136\n"
    "v_accvgpr_read_b32 v139, a145\n"
    "v_mfma_f32_16x16x32_bf16 v[96:99], a[100:103], a[36:39], v[96:99]\n"
    "ds_read_b64_tr_b16 a[140:141], v130 offset:57344\n"
    "ds_read_b64_tr_b16 a[142:143], v130 offset:61440\n"
    "v_permlane32_swap_b32 v137, v136\n"
    "v_mfma_f32_16x16x32_bf16 v[96:99], a[104:107], a[40:43], v[96:99]\n"
    "s_mov_b32 m0, s83\n"
    "v_max_f32 v136, v136, v137\n"
    "v_mov_b32 v137, v136\n"
    "v_accvgpr_write_b32 a144, 0\n"
    "s_nop 0\n"
    "v_permlane16_swap_b32 v137, v136\n"
    "v_mfma_f32_16x16x32_bf16 v[96:99], a[108:111], a[44:47], v[96:99]\n"
    "buffer_load_dwordx4 v131, s[40:43], s94 offen lds\n"
    "v_max_f32 v136, v136, v137\n"
    "v_add_f32 v136, 0x41200000, v136\n"
    "v_mfma_f32_16x16x32_bf16 v[164:167], a[128:131], v[80:83], v[164:167]\n"
    "v_add_f32 v138, v138, v139\n"
    "v_sub_f32 v100, v100, v136\n"
    "v_sub_f32 v101, v101, v136\n"
    "v_sub_f32 v102, v102, v136\n"
    "v_accvgpr_write_b32 a145, 0\n"
    "v_mfma_f32_16x16x32_bf16 v[164:167], a[132:135], v[84:87], v[164:167]\n"
    "s_waitcnt lgkmcnt(8)\n"
    "v_sub_f32 v103, v103, v136\n"
    "v_exp_f32 v100, v100\n"
    "v_sub_f32 v108, 0, v136\n"
    "v_mfma_f32_16x16x32_bf16 v[168:171], a[132:135], v[92:95], v[168:171]\n"
    "v_rcp_f32 v140, v138\n"
    "v_exp_f32 v101, v101\n"
    "v_sub_f32 v109, 0, v136\n"
    "v_mfma_f32_16x16x32_bf16 v[168:171], a[128:131], v[88:91], v[168:171]\n"
    "v_exp_f32 v102, v102\n"
    "v_cmp_ngt_f32 vcc, s67, v138\n"
    "v_cvt_pk_bf16_f32 v8, v100, v101\n"
    "v_smfmac_f32_16x16x64_bf16 a[152:155], a[168:171], v[64:71], v133\n"
    "s_waitcnt lgkmcnt(14)\n"
    "v_sub_f32 v110, 0, v136\n"
    "v_exp_f32 v103, v103\n"
    "v_sub_f32 v111, 0, v136\n"
    "v_cvt_pk_bf16_f32 v9, v102, v103\n"
    "s_mov_b64 s[78:79], vcc\n"
    "v_mfma_f32_16x16x32_bf16 v[100:103], a[96:99], a[48:51], 0\n"
    "v_max3_f32 v136, v96, v97, v98\n"
    "v_accvgpr_read_b32 v138, a148\n"
    "v_max_f32 v136, v136, v99\n"
    "v_mov_b32 v137, v136\n"
    "v_accvgpr_read_b32 v139, a149\n"
    "v_mfma_f32_16x16x32_bf16 v[100:103], a[100:103], a[52:55], v[100:103]\n"
    "buffer_load_dwordx4 v131, s[40:43], s94 offen offset:2048 lds\n"
    "v_permlane32_swap_b32 v137, v136\n"
    "v_mfma_f32_16x16x32_bf16 v[100:103], a[104:107], a[56:59], v[100:103]\n"
    "v_max_f32 v136, v136, v137\n"
    "v_mov_b32 v137, v136\n"
    "v_accvgpr_write_b32 a148, 0\n"
    "s_nop 0\n"
    "v_permlane16_swap_b32 v137, v136\n"
    "v_mfma_f32_16x16x32_bf16 v[100:103], a[108:111], a[60:63], v[100:103]\n"
    "s_waitcnt lgkmcnt(0)\n"
    "v_max_f32 v136, v136, v137\n"
    "v_add_f32 v136, 0x41200000, v136\n"
    "v_add_f32 v138, v138, v139\n"
    "v_sub_f32 v96, v96, v136\n"
    "v_mfma_f32_16x16x32_bf16 v[172:175], a[136:139], v[48:51], v[172:175]\n"
    "v_sub_f32 v97, v97, v136\n"
    "v_sub_f32 v98, v98, v136\n"
    "v_accvgpr_write_b32 a149, 0\n"
    "v_sub_f32 v99, v99, v136\n"
    "v_mfma_f32_16x16x32_bf16 v[172:175], a[140:143], v[52:55], v[172:175]\n"
    "ds_read_b128 a[120:123], v128 offset:4608\n"
    "v_exp_f32 v96, v96\n"
    "v_sub_f32 v112, 0, v136\n"
    "v_rcp_f32 v141, v138\n"
    "v_mfma_f32_16x16x32_bf16 v[176:179], a[140:143], v[60:63], v[176:179]\n"
    "v_exp_f32 v97, v97\n"
    "v_sub_f32 v113, 0, v136\n"
    "v_mfma_f32_16x16x32_bf16 v[176:179], a[136:139], v[56:59], v[176:179]\n"
    "ds_read_b128 a[124:127], v128 offset:4864\n"
    "v_exp_f32 v98, v98\n"
    "v_cmp_ngt_f32 vcc, s67, v138\n"
    "v_cvt_pk_bf16_f32 v16, v96, v97\n"
    "v_smfmac_f32_16x16x64_bf16 a[156:159], a[168:171], v[72:79], v133\n"
    "s_waitcnt lgkmcnt(10)\n"
    "v_sub_f32 v114, 0, v136\n"
    "v_exp_f32 v99, v99\n"
    "v_sub_f32 v115, 0, v136\n"
    "v_cvt_pk_bf16_f32 v17, v98, v99\n"
    "s_or_b64 s[78:79], s[78:79], vcc\n"
    "v_mfma_f32_16x16x32_bf16 v[96:99], a[96:99], a[64:67], 0\n"
    "v_max3_f32 v136, v100, v101, v102\n"
    "v_accvgpr_read_b32 v138, a152\n"
    "v_max_f32 v136, v136, v103\n"
    "v_mov_b32 v137, v136\n"
    "v_mfma_f32_16x16x32_bf16 v[96:99], a[100:103], a[68:71], v[96:99]\n"
    "ds_read_b64_tr_b16 a[128:129], v129 offset:49408\n"
    "v_accvgpr_read_b32 v139, a153\n"
    "v_permlane32_swap_b32 v137, v136\n"
    "v_max_f32 v136, v136, v137\n"
    "v_mfma_f32_16x16x32_bf16 v[96:99], a[104:107], a[72:75], v[96:99]\n"
    "v_mov_b32 v137, v136\n"
    "v_accvgpr_write_b32 a152, 0\n"
    "s_nop 0\n"
    "v_permlane16_swap_b32 v137, v136\n"
    "v_mfma_f32_16x16x32_bf16 v[96:99], a[108:111], a[76:79], v[96:99]\n"
    "ds_read_b64_tr_b16 a[130:131], v129 offset:53504\n"
    "v_max_f32 v136, v136, v137\n"
    "v_add_f32 v136, 0x41200000, v136\n"
    "v_add_f32 v138, v138, v139\n"
    "v_mfma_f32_16x16x32_bf16 v[180:183], a[136:139], v[64:67], v[180:183]\n"
    "v_sub_f32 v100, v100, v136\n"
    "v_sub_f32 v101, v101, v136\n"
    "v_sub_f32 v102, v102, v136\n"
    "v_accvgpr_write_b32 a153, 0\n"
    "v_sub_f32 v103, v103, v136\n"
    "v_mfma_f32_16x16x32_bf16 v[180:183], a[140:143], v[68:71], v[180:183]\n"
    "v_exp_f32 v100, v100\n"
    "v_sub_f32 v116, 0, v136\n"
    "v_rcp_f32 v142, v138\n"
    "v_mfma_f32_16x16x32_bf16 v[184:187], a[140:143], v[76:79], v[184:187]\n"
    "v_exp_f32 v101, v101\n"
    "v_sub_f32 v117, 0, v136\n"
    "v_mfma_f32_16x16x32_bf16 v[184:187], a[136:139], v[72:75], v[184:187]\n"
    "v_exp_f32 v102, v102\n"
    "v_cmp_ngt_f32 vcc, s67, v138\n"
    "v_cvt_pk_bf16_f32 v24, v100, v101\n"
    "v_sub_f32 v118, 0, v136\n"
    "v_smfmac_f32_16x16x64_bf16 a[160:163], a[168:171], v[80:87], v133\n"
    "s_waitcnt lgkmcnt(6)\n"
    "v_exp_f32 v103, v103\n"
    "v_sub_f32 v119, 0, v136\n"
    "v_cvt_pk_bf16_f32 v25, v102, v103\n"
    "s_or_b64 s[78:79], s[78:79], vcc\n"
    "v_mfma_f32_16x16x32_bf16 v[100:103], a[96:99], a[80:83], 0\n"
    "v_max3_f32 v136, v96, v97, v98\n"
    "v_accvgpr_read_b32 v138, a156\n"
    "v_max_f32 v136, v136, v99\n"
    "v_mov_b32 v137, v136\n"
    "v_mfma_f32_16x16x32_bf16 v[100:103], a[100:103], a[84:87], v[100:103]\n"
    "ds_read_b64_tr_b16 a[132:133], v129 offset:57600\n"
    "v_accvgpr_read_b32 v139, a157\n"
    "v_permlane32_swap_b32 v137, v136\n"
    "v_max_f32 v136, v136, v137\n"
    "v_mfma_f32_16x16x32_bf16 v[100:103], a[104:107], a[88:91], v[100:103]\n"
    "v_mov_b32 v137, v136\n"
    "v_accvgpr_write_b32 a156, 0\n"
    "s_nop 0\n"
    "v_permlane16_swap_b32 v137, v136\n"
    "v_mfma_f32_16x16x32_bf16 v[100:103], a[108:111], a[92:95], v[100:103]\n"
    "ds_read_b64_tr_b16 a[134:135], v129 offset:61696\n"
    "v_max_f32 v136, v136, v137\n"
    "v_add_f32 v136, 0x41200000, v136\n"
    "v_add_f32 v138, v138, v139\n"
    "v_mfma_f32_16x16x32_bf16 v[188:191], a[136:139], v[80:83], v[188:191]\n"
    "v_sub_f32 v96, v96, v136\n"
    "v_sub_f32 v97, v97, v136\n"
    "v_sub_f32 v98, v98, v136\n"
    "v_accvgpr_write_b32 a157, 0\n"
    "v_sub_f32 v99, v99, v136\n"
    "v_mfma_f32_16x16x32_bf16 v[188:191], a[140:143], v[84:87], v[188:191]\n"
    "v_exp_f32 v96, v96\n"
    "v_sub_f32 v120, 0, v136\n"
    "v_rcp_f32 v143, v138\n"
    "v_mfma_f32_16x16x32_bf16 v[192:195], a[140:143], v[92:95], v[192:195]\n"
    "v_exp_f32 v97, v97\n"
    "v_sub_f32 v121, 0, v136\n"
    "v_mfma_f32_16x16x32_bf16 v[192:195], a[136:139], v[88:91], v[192:195]\n"
    "s_waitcnt lgkmcnt(4)\n"
    "v_exp_f32 v98, v98\n"
    "v_cmp_ngt_f32 vcc, s67, v138\n"
    "v_cvt_pk_bf16_f32 v32, v96, v97\n"
    "v_sub_f32 v122, 0, v136\n"
    "v_smfmac_f32_16x16x64_bf16 a[164:167], a[168:171], v[88:95], v133\n"
    "v_exp_f32 v99, v99\n"
    "v_sub_f32 v123, 0, v136\n"
    "v_cvt_pk_bf16_f32 v33, v98, v99\n"
    "s_or_b64 s[78:79], s[78:79], vcc\n"
    "v_mfma_f32_16x16x32_bf16 v[96:99], a[112:115], a[0:3], v[104:107]\n"
    "s_add_u32 s56, s56, 16384\n"
    "v_max3_f32 v136, v100, v101, v102\n"
    "v_accvgpr_read_b32 v138, a160\n"
    "v_max_f32 v136, v136, v103\n"
    "v_mov_b32 v137, v136\n"
    "v_accvgpr_read_b32 v139, a161\n"
    "v_mfma_f32_16x16x32_bf16 v[96:99], a[116:119], a[4:7], v[96:99]\n"
    "s_and_b32 s56, s56, 262143\n"
    "v_permlane32_swap_b32 v137, v136\n"
    "v_max_f32 v136, v136, v137\n"
    "v_mov_b32 v137, v136\n"
    "v_mfma_f32_16x16x32_bf16 v[96:99], a[120:123], a[8:11], v[96:99]\n"
    "s_or_b32 s56, s56, s61\n"
    "v_accvgpr_write_b32 a160, 0\n"
    "v_permlane16_swap_b32 v137, v136\n"
    "v_max_f32 v136, v136, v137\n"
    "v_add_f32 v136, 0x41200000, v136\n"
    "v_mfma_f32_16x16x32_bf16 v[96:99], a[124:127], a[12:15], v[96:99]\n"
    "s_waitcnt lgkmcnt(0)\n"
    "v_add_f32 v138, v138, v139\n"
    "v_sub_f32 v100, v100, v136\n"
    "v_sub_f32 v101, v101, v136\n"
    "v_sub_f32 v102, v102, v136\n"
    "v_accvgpr_write_b32 a161, 0\n"
    "v_mfma_f32_16x16x32_bf16 v[196:199], a[128:131], v[48:51], v[196:199]\n"
    "s_add_u32 s94, s56, 8192\n"
    "v_sub_f32 v103, v103, v136\n"
    "v_exp_f32 v100, v100\n"
    "v_sub_f32 v124, 0, v136\n"
    "v_mfma_f32_16x16x32_bf16 v[196:199], a[132:135], v[52:55], v[196:199]\n"
    "s_add_u32 s57, s57, 16384\n"
    "v_rcp_f32 v144, v138\n"
    "v_exp_f32 v101, v101\n"
    "v_sub_f32 v125, 0, v136\n"
    "v_mfma_f32_16x16x32_bf16 v[200:203], a[132:135], v[60:63], v[200:203]\n"
    "s_and_b32 s57, s57, 262143\n"
    "v_exp_f32 v102, v102\n"
    "v_cmp_ngt_f32 vcc, s67, v138\n"
    "v_cvt_pk_bf16_f32 v40, v100, v101\n"
    "v_mfma_f32_16x16x32_bf16 v[200:203], a[128:131], v[56:59], v[200:203]\n"
    "s_or_b32 s57, s57, s96\n"
    "v_sub_f32 v126, 0, v136\n"
    "v_exp_f32 v103, v103\n"
    "v_sub_f32 v127, 0, v136\n"
    "v_cvt_pk_bf16_f32 v41, v102, v103\n"
    "s_or_b64 s[78:79], s[78:79], vcc\n"
    "v_mfma_f32_16x16x32_bf16 v[100:103], a[112:115], a[16:19], v[108:111]\n"
    "s_add_u32 s70, s57, 8192\n"
    "v_exp_f32 v96, v96\n"
    "v_accvgpr_read_b32 v138, a164\n"
    "v_mfma_f32_16x16x32_bf16 v[100:103], a[116:119], a[20:23], v[100:103]\n"
    "ds_read_b64_tr_b16 a[136:137], v130 offset:49408\n"
    "v_accvgpr_read_b32 v139, a165\n"
    "v_accvgpr_write_b32 a164, 0\n"
    "v_mfma_f32_16x16x32_bf16 v[100:103], a[120:123], a[24:27], v[100:103]\n"
    "v_exp_f32 v97, v97\n"
    "v_add_f32 v138, v138, v139\n"
    "v_accvgpr_write_b32 a165, 0\n"
    "v_mfma_f32_16x16x32_bf16 v[100:103], a[124:127], a[28:31], v[100:103]\n"
    "ds_read_b64_tr_b16 a[138:139], v130 offset:53504\n"
    "v_rcp_f32 v145, v138\n"
    "v_mfma_f32_16x16x32_bf16 v[204:207], a[128:131], v[64:67], v[204:207]\n"
    "v_exp_f32 v98, v98\n"
    "v_cmp_ngt_f32 vcc, s67, v138\n"
    "v_mfma_f32_16x16x32_bf16 v[204:207], a[132:135], v[68:71], v[204:207]\n"
    "s_or_b64 s[78:79], s[78:79], vcc\n"
    "v_cvt_pk_bf16_f32 v2, v96, v97\n"
    "v_mul_f32 v148, v140, v148\n"
    "v_mul_f32 v149, v140, v149\n"
    "v_mfma_f32_16x16x32_bf16 v[208:211], a[132:135], v[76:79], v[208:211]\n"
    "v_exp_f32 v99, v99\n"
    "v_mul_f32 v150, v140, v150\n"
    "v_mul_f32 v151, v140, v151\n"
    "v_mfma_f32_16x16x32_bf16 v[208:211], a[128:131], v[72:75], v[208:211]\n"
    "v_cvt_pk_bf16_f32 v3, v98, v99\n"
    "v_cvt_pk_bf16_f32 v148, v148, v149\n"
    "v_cvt_pk_bf16_f32 v149, v150, v151\n"
    "v_mfma_f32_16x16x32_bf16 v[96:99], a[112:115], a[32:35], v[112:115]\n"
    "v_exp_f32 v100, v100\n"
    "v_mfma_f32_16x16x32_bf16 v[96:99], a[116:119], a[36:39], v[96:99]\n"
    "ds_read_b64_tr_b16 a[140:141], v130 offset:57600\n"
    "ds_write_b64 v135, v[148:149] offset:0\n"
    "v_mfma_f32_16x16x32_bf16 v[96:99], a[120:123], a[40:43], v[96:99]\n"
    "v_exp_f32 v101, v101\n"
    "v_mul_f32 v152, v141, v152\n"
    "v_mfma_f32_16x16x32_bf16 v[96:99], a[124:127], a[44:47], v[96:99]\n"
    "ds_read_b64_tr_b16 a[142:143], v130 offset:61696\n"
    "v_mul_f32 v153, v141, v153\n"
    "v_mul_f32 v154, v141, v154\n"
    "v_mul_f32 v155, v141, v155\n"
    "v_mfma_f32_16x16x32_bf16 v[212:215], a[128:131], v[80:83], v[212:215]\n"
    "v_exp_f32 v102, v102\n"
    "v_cvt_pk_bf16_f32 v152, v152, v153\n"
    "v_mfma_f32_16x16x32_bf16 v[212:215], a[132:135], v[84:87], v[212:215]\n"
    "v_cvt_pk_bf16_f32 v10, v100, v101\n"
    "v_cvt_pk_bf16_f32 v153, v154, v155\n"
    "v_mul_f32 v156, v142, v156\n"
    "v_mfma_f32_16x16x32_bf16 v[216:219], a[132:135], v[92:95], v[216:219]\n"
    "v_exp_f32 v103, v103\n"
    "v_mfma_f32_16x16x32_bf16 v[216:219], a[128:131], v[88:91], v[216:219]\n"
    "ds_write_b64 v135, v[152:153] offset:4096\n"
    "v_cvt_pk_bf16_f32 v11, v102, v103\n"
    "v_mfma_f32_16x16x32_bf16 v[100:103], a[112:115], a[48:51], v[116:119]\n"
    "v_exp_f32 v96, v96\n"
    "v_mul_f32 v157, v142, v157\n"
    "v_mul_f32 v158, v142, v158\n"
    "v_mfma_f32_16x16x32_bf16 v[100:103], a[116:119], a[52:55], v[100:103]\n"
    "v_mul_f32 v159, v142, v159\n"
    "v_cvt_pk_bf16_f32 v156, v156, v157\n"
    "v_cvt_pk_bf16_f32 v157, v158, v159\n"
    "v_mfma_f32_16x16x32_bf16 v[100:103], a[120:123], a[56:59], v[100:103]\n"
    "s_waitcnt vmcnt(8) lgkmcnt(1)\n"
    "v_exp_f32 v97, v97\n"
    "v_mfma_f32_16x16x32_bf16 v[100:103], a[124:127], a[60:63], v[100:103]\n"
    "s_barrier\n"
    "ds_write_b64 v135, v[156:157] offset:8192\n"
    "v_mfma_f32_16x16x32_bf16 v[220:223], a[136:139], v[48:51], v[220:223]\n"
    "v_exp_f32 v98, v98\n"
    "v_mul_f32 v160, v143, v160\n"
    "v_mfma_f32_16x16x32_bf16 v[220:223], a[140:143], v[52:55], v[220:223]\n"
    "ds_read_b128 a[96:99], v128 offset:8192\n"
    "v_cvt_pk_bf16_f32 v18, v96, v97\n"
    "v_mul_f32 v161, v143, v161\n"
    "v_mfma_f32_16x16x32_bf16 v[224:227], a[140:143], v[60:63], v[224:227]\n"
    "v_exp_f32 v99, v99\n"
    "v_mul_f32 v162, v143, v162\n"
    "v_mfma_f32_16x16x32_bf16 v[224:227], a[136:139], v[56:59], v[224:227]\n"
    "ds_read_b128 a[100:103], v128 offset:8448\n"
    "v_cvt_pk_bf16_f32 v19, v98, v99\n"
    "v_mul_f32 v163, v143, v163\n"
    "v_mfma_f32_16x16x32_bf16 v[96:99], a[112:115], a[64:67], v[120:123]\n"
    "v_exp_f32 v100, v100\n"
    "v_cvt_pk_bf16_f32 v160, v160, v161\n"
    "v_mfma_f32_16x16x32_bf16 v[96:99], a[116:119], a[68:71], v[96:99]\n"
    "ds_read_b64_tr_b16 a[128:129], v129 offset:49664\n"
    "ds_read_b64_tr_b16 a[130:131], v129 offset:53760\n"
    "v_cvt_pk_bf16_f32 v161, v162, v163\n"
    "v_mul_f32 v164, v144, v164\n"
    "v_mfma_f32_16x16x32_bf16 v[96:99], a[120:123], a[72:75], v[96:99]\n"
    "s_mov_b32 m0, s80\n"
    "v_exp_f32 v101, v101\n"
    "v_mfma_f32_16x16x32_bf16 v[96:99], a[124:127], a[76:79], v[96:99]\n"
    "buffer_load_dwordx4 v131, s[40:43], s56 offen lds\n"
    "ds_write_b64 v135, v[160:161] offset:12288\n"
    "v_mfma_f32_16x16x32_bf16 v[228:231], a[136:139], v[64:67], v[228:231]\n"
    "v_exp_f32 v102, v102\n"
    "v_mfma_f32_16x16x32_bf16 v[228:231], a[140:143], v[68:71], v[228:231]\n"
    "ds_read_b128 a[104:107], v128 offset:8704\n"
    "v_cvt_pk_bf16_f32 v26, v100, v101\n"
    "v_mul_f32 v165, v144, v165\n"
    "v_mul_f32 v166, v144, v166\n"
    "v_mfma_f32_16x16x32_bf16 v[232:235], a[140:143], v[76:79], v[232:235]\n"
    "v_exp_f32 v103, v103\n"
    "v_mul_f32 v167, v144, v167\n"
    "v_mfma_f32_16x16x32_bf16 v[232:235], a[136:139], v[72:75], v[232:235]\n"
    "ds_read_b128 a[108:111], v128 offset:8960\n"
    "v_cvt_pk_bf16_f32 v27, v102, v103\n"
    "v_cvt_pk_bf16_f32 v164, v164, v165\n"
    "v_mfma_f32_16x16x32_bf16 v[100:103], a[112:115], a[80:83], v[124:127]\n"
    "v_exp_f32 v96, v96\n"
    "v_cvt_pk_bf16_f32 v165, v166, v167\n"
    "v_mfma_f32_16x16x32_bf16 v[100:103], a[116:119], a[84:87], v[100:103]\n"
    "ds_read_b64_tr_b16 a[132:133], v129 offset:57856\n"
    "ds_read_b64_tr_b16 a[134:135], v129 offset:61952\n"
    "v_mfma_f32_16x16x32_bf16 v[100:103], a[120:123], a[88:91], v[100:103]\n"
    "ds_write_b64 v135, v[164:165] offset:16384\n"
    "v_exp_f32 v97, v97\n"
    "v_mfma_f32_16x16x32_bf16 v[100:103], a[124:127], a[92:95], v[100:103]\n"
    "buffer_load_dwordx4 v131, s[40:43], s56 offen offset:2048 lds\n"
    "v_mul_f32 v168, v145, v168\n"
    "v_mfma_f32_16x16x32_bf16 v[236:239], a[136:139], v[80:83], v[236:239]\n"
    "v_exp_f32 v98, v98\n"
    "v_mul_f32 v169, v145, v169\n"
    "v_mfma_f32_16x16x32_bf16 v[236:239], a[140:143], v[84:87], v[236:239]\n"
    "v_cvt_pk_bf16_f32 v34, v96, v97\n"
    "v_mul_f32 v170, v145, v170\n"
    "v_mul_f32 v171, v145, v171\n"
    "v_mfma_f32_16x16x32_bf16 v[240:243], a[140:143], v[92:95], v[240:243]\n"
    "v_exp_f32 v99, v99\n"
    "v_cvt_pk_bf16_f32 v168, v168, v169\n"
    "v_mfma_f32_16x16x32_bf16 v[240:243], a[136:139], v[88:91], v[240:243]\n"
    "s_waitcnt lgkmcnt(3)\n"
    "v_cvt_pk_bf16_f32 v35, v98, v99\n"
    "v_cvt_pk_bf16_f32 v169, v170, v171\n"
    "v_mul_f32 v172, v140, v172\n"
    "v_mul_f32 v173, v140, v173\n"
    "v_mfma_f32_16x16x32_bf16 v[96:99], a[96:99], a[0:3], v[104:107]\n"
    "v_exp_f32 v100, v100\n"
    "v_mfma_f32_16x16x32_bf16 v[96:99], a[100:103], a[4:7], v[96:99]\n"
    "ds_write_b64 v135, v[168:169] offset:20480\n"
    "v_mul_f32 v174, v140, v174\n"
    "v_mfma_f32_16x16x32_bf16 v[96:99], a[104:107], a[8:11], v[96:99]\n"
    "v_exp_f32 v101, v101\n"
    "v_mul_f32 v175, v140, v175\n"
    "v_mfma_f32_16x16x32_bf16 v[96:99], a[108:111], a[12:15], v[96:99]\n"
    "s_waitcnt lgkmcnt(2)\n"
    "v_cvt_pk_bf16_f32 v172, v172, v173\n"
    "v_cvt_pk_bf16_f32 v173, v174, v175\n"
    "v_mul_f32 v176, v141, v176\n"
    "v_mul_f32 v177, v141, v177\n"
    "v_mfma_f32_16x16x32_bf16 v[244:247], a[128:131], v[48:51], v[244:247]\n"
    "v_exp_f32 v102, v102\n"
    "v_mfma_f32_16x16x32_bf16 v[244:247], a[132:135], v[52:55], v[244:247]\n"
    "ds_read_b128 a[112:115], v128 offset:12288\n"
    "ds_write_b64 v135, v[172:173] offset:512\n"
    "v_cvt_pk_bf16_f32 v42, v100, v101\n"
    "v_mfma_f32_16x16x32_bf16 v[248:251], a[132:135], v[60:63], v[248:251]\n"
    "v_exp_f32 v103, v103\n"
    "v_mfma_f32_16x16x32_bf16 v[248:251], a[128:131], v[56:59], v[248:251]\n"
    "ds_read_b128 a[116:119], v128 offset:12544\n"
    "v_cvt_pk_bf16_f32 v43, v102, v103\n"
    "v_mul_f32 v178, v141, v178\n"
    "v_mfma_f32_16x16x32_bf16 v[100:103], a[96:99], a[16:19], v[108:111]\n"
    "v_exp_f32 v96, v96\n"
    "v_mul_f32 v179, v141, v179\n"
    "v_cvt_pk_bf16_f32 v176, v176, v177\n"
    "v_mfma_f32_16x16x32_bf16 v[100:103], a[100:103], a[20:23], v[100:103]\n"
    "ds_read_b64_tr_b16 a[136:137], v130 offset:49664\n"
    "v_cvt_pk_bf16_f32 v177, v178, v179\n"
    "v_mul_f32 v180, v142, v180\n"
    "v_mfma_f32_16x16x32_bf16 v[100:103], a[104:107], a[24:27], v[100:103]\n"
    "v_exp_f32 v97, v97\n"
    "v_mfma_f32_16x16x32_bf16 v[100:103], a[108:111], a[28:31], v[100:103]\n"
    "ds_read_b64_tr_b16 a[138:139], v130 offset:53760\n"
    "ds_write_b64 v135, v[176:177] offset:4608\n"
    "v_mfma_f32_16x16x32_bf16 v[252:255], a[128:131], v[64:67], v[252:255]\n"
    "v_exp_f32 v98, v98\n"
    "v_mul_f32 v181, v142, v181\n"
    "v_mul_f32 v182, v142, v182\n"
    "v_mfma_f32_16x16x32_bf16 v[252:255], a[132:135], v[68:71], v[252:255]\n"
    "v_cvt_pk_bf16_f32 v4, v96, v97\n"
    "v_mul_f32 v183, v142, v183\n"
    "v_cvt_pk_bf16_f32 v180, v180, v181\n"
    "v_mfma_f32_16x16x32_bf16 a[172:175], a[132:135], v[76:79], a[172:175]\n"
    "v_exp_f32 v99, v99\n"
    "v_cvt_pk_bf16_f32 v181, v182, v183\n"
    "v_mfma_f32_16x16x32_bf16 a[172:175], a[128:131], v[72:75], a[172:175]\n"
    "ds_write_b64 v135, v[180:181] offset:8704\n"
    "v_cvt_pk_bf16_f32 v5, v98, v99\n"
    "v_mfma_f32_16x16x32_bf16 v[96:99], a[96:99], a[32:35], v[112:115]\n"
    "v_exp_f32 v100, v100\n"
    "v_mfma_f32_16x16x32_bf16 v[96:99], a[100:103], a[36:39], v[96:99]\n"
    "ds_read_b64_tr_b16 a[140:141], v130 offset:57856\n"
    "v_mul_f32 v184, v143, v184\n"
    "v_mul_f32 v185, v143, v185\n"
    "v_mul_f32 v186, v143, v186\n"
    "v_mfma_f32_16x16x32_bf16 v[96:99], a[104:107], a[40:43], v[96:99]\n"
    "v_exp_f32 v101, v101\n"
    "v_mul_f32 v187, v143, v187\n"
    "v_mfma_f32_16x16x32_bf16 v[96:99], a[108:111], a[44:47], v[96:99]\n"
    "ds_read_b64_tr_b16 a[142:143], v130 offset:61952\n"
    "v_cvt_pk_bf16_f32 v184, v184, v185\n"
    "v_cvt_pk_bf16_f32 v185, v186, v187\n"
    "v_mfma_f32_16x16x32_bf16 a[176:179], a[128:131], v[80:83], a[176:179]\n"
    "v_exp_f32 v102, v102\n"
    "v_mfma_f32_16x16x32_bf16 a[176:179], a[132:135], v[84:87], a[176:179]\n"
    "ds_write_b64 v135, v[184:185] offset:12800\n"
    "v_cvt_pk_bf16_f32 v12, v100, v101\n"
    "v_mfma_f32_16x16x32_bf16 a[180:183], a[132:135], v[92:95], a[180:183]\n"
    "v_exp_f32 v103, v103\n"
    "v_mul_f32 v188, v144, v188\n"
    "v_mul_f32 v189, v144, v189\n"
    "v_mfma_f32_16x16x32_bf16 a[180:183], a[128:131], v[88:91], a[180:183]\n"
    "v_cvt_pk_bf16_f32 v13, v102, v103\n"
    "v_mul_f32 v190, v144, v190\n"
    "v_mul_f32 v191, v144, v191\n"
    "v_mfma_f32_16x16x32_bf16 v[100:103], a[96:99], a[48:51], v[116:119]\n"
    "v_exp_f32 v96, v96\n"
    "v_cvt_pk_bf16_f32 v188, v188, v189\n"
    "v_mfma_f32_16x16x32_bf16 v[100:103], a[100:103], a[52:55], v[100:103]\n"
    "v_cvt_pk_bf16_f32 v189, v190, v191\n"
    "v_mul_f32 v192, v145, v192\n"
    "v_mul_f32 v193, v145, v193\n"
    "v_mfma_f32_16x16x32_bf16 v[100:103], a[104:107], a[56:59], v[100:103]\n"
    "ds_write_b64 v135, v[188:189] offset:16896\n"
    "v_exp_f32 v97, v97\n"
    "v_mfma_f32_16x16x32_bf16 v[100:103], a[108:111], a[60:63], v[100:103]\n"
    "s_waitcnt lgkmcnt(2)\n"
    "v_mul_f32 v194, v145, v194\n"
    "v_mul_f32 v195, v145, v195\n"
    "v_mfma_f32_16x16x32_bf16 a[184:187], a[136:139], v[48:51], a[184:187]\n"
    "v_exp_f32 v98, v98\n"
    "v_cvt_pk_bf16_f32 v192, v192, v193\n"
    "v_mfma_f32_16x16x32_bf16 a[184:187], a[140:143], v[52:55], a[184:187]\n"
    "ds_read_b128 a[120:123], v128 offset:12800\n"
    "v_cvt_pk_bf16_f32 v20, v96, v97\n"
    "v_cvt_pk_bf16_f32 v193, v194, v195\n"
    "v_mfma_f32_16x16x32_bf16 a[188:191], a[140:143], v[60:63], a[188:191]\n"
    "v_exp_f32 v99, v99\n"
    "v_mfma_f32_16x16x32_bf16 a[188:191], a[136:139], v[56:59], a[188:191]\n"
    "ds_read_b128 a[124:127], v128 offset:13056\n"
    "ds_write_b64 v135, v[192:193] offset:20992\n"
    "v_cvt_pk_bf16_f32 v21, v98, v99\n"
    "v_mfma_f32_16x16x32_bf16 v[96:99], a[96:99], a[64:67], v[120:123]\n"
    "v_exp_f32 v100, v100\n"
    "v_mul_f32 v196, v140, v196\n"
    "v_mfma_f32_16x16x32_bf16 v[96:99], a[100:103], a[68:71], v[96:99]\n"
    "ds_read_b64_tr_b16 a[128:129], v129 offset:49920\n"
    "v_mul_f32 v197, v140, v197\n"
    "v_mul_f32 v198, v140, v198\n"
    "v_mfma_f32_16x16x32_bf16 v[96:99], a[104:107], a[72:75], v[96:99]\n"
    "v_exp_f32 v101, v101\n"
    "v_mul_f32 v199, v140, v199\n"
    "v_mfma_f32_16x16x32_bf16 v[96:99], a[108:111], a[76:79], v[96:99]\n"
    "ds_read_b64_tr_b16 a[130:131], v129 offset:54016\n"
    "v_cvt_pk_bf16_f32 v196, v196, v197\n"
    "v_cvt_pk_bf16_f32 v197, v198, v199\n"
    "v_mul_f32 v200, v141, v200\n"
    "v_mfma_f32_16x16x32_bf16 a[192:195], a[136:139], v[64:67], a[192:195]\n"
    "v_exp_f32 v102, v102\n"
    "v_mfma_f32_16x16x32_bf16 a[192:195], a[140:143], v[68:71], a[192:195]\n"
    "ds_write_b64 v135, v[196:197] offset:1024\n"
    "v_cvt_pk_bf16_f32 v28, v100, v101\n"
    "v_mfma_f32_16x16x32_bf16 a[196:199], a[140:143], v[76:79], a[196:199]\n"
    "v_exp_f32 v103, v103\n"
    "v_mul_f32 v201, v141, v201\n"
    "v_mfma_f32_16x16x32_bf16 a[196:199], a[136:139], v[72:75], a[196:199]\n"
    "v_cvt_pk_bf16_f32 v29, v102, v103\n"
    "v_mul_f32 v202, v141, v202\n"
    "v_mul_f32 v203, v141, v203\n"
    "v_mfma_f32_16x16x32_bf16 v[100:103], a[96:99], a[80:83], v[124:127]\n"
    "v_exp_f32 v96, v96\n"
    "v_cvt_pk_bf16_f32 v200, v200, v201\n"
    "v_cvt_pk_bf16_f32 v201, v202, v203\n"
    "v_mfma_f32_16x16x32_bf16 v[100:103], a[100:103], a[84:87], v[100:103]\n"
    "ds_read_b64_tr_b16 a[132:133], v129 offset:58112\n"
    "ds_write_b64 v135, v[200:201] offset:5120\n"
    "v_mfma_f32_16x16x32_bf16 v[100:103], a[104:107], a[88:91], v[100:103]\n"
    "v_exp_f32 v97, v97\n"
    "v_mfma_f32_16x16x32_bf16 v[100:103], a[108:111], a[92:95], v[100:103]\n"
    "ds_read_b64_tr_b16 a[134:135], v129 offset:62208\n"
    "v_mul_f32 v204, v142, v204\n"
    "v_mul_f32 v205, v142, v205\n"
    "v_mfma_f32_16x16x32_bf16 a[200:203], a[136:139], v[80:83], a[200:203]\n"
    "v_exp_f32 v98, v98\n"
    "v_mul_f32 v206, v142, v206\n"
    "v_mul_f32 v207, v142, v207\n"
    "v_mfma_f32_16x16x32_bf16 a[200:203], a[140:143], v[84:87], a[200:203]\n"
    "v_cvt_pk_bf16_f32 v36, v96, v97\n"
    "v_cvt_pk_bf16_f32 v204, v204, v205\n"
    "v_cvt_pk_bf16_f32 v205, v206, v207\n"
    "v_mfma_f32_16x16x32_bf16 a[204:207], a[140:143], v[92:95], a[204:207]\n"
    "v_exp_f32 v99, v99\n"
    "v_mfma_f32_16x16x32_bf16 a[204:207], a[136:139], v[88:91], a[204:207]\n"
    "s_waitcnt lgkmcnt(7)\n"
    "ds_write_b64 v135, v[204:205] offset:9216\n"
    "v_cvt_pk_bf16_f32 v37, v98, v99\n"
    "v_mfma_f32_16x16x32_bf16 v[96:99], a[112:115], a[0:3], v[104:107]\n"
    "v_exp_f32 v100, v100\n"
    "v_mul_f32 v208, v143, v208\n"
    "v_mfma_f32_16x16x32_bf16 v[96:99], a[116:119], a[4:7], v[96:99]\n"
    "v_mul_f32 v209, v143, v209\n"
    "v_mul_f32 v210, v143, v210\n"
    "v_mul_f32 v211, v143, v211\n"
    "v_cvt_pk_bf16_f32 v208, v208, v209\n"
    "v_mfma_f32_16x16x32_bf16 v[96:99], a[120:123], a[8:11], v[96:99]\n"
    "v_exp_f32 v101, v101\n"
    "v_cvt_pk_bf16_f32 v209, v210, v211\n"
    "v_mfma_f32_16x16x32_bf16 v[96:99], a[124:127], a[12:15], v[96:99]\n"
    "s_waitcnt lgkmcnt(1)\n"
    "ds_write_b64 v135, v[208:209] offset:13312\n"
    "v_mfma_f32_16x16x32_bf16 a[208:211], a[128:131], v[48:51], a[208:211]\n"
    "v_exp_f32 v102, v102\n"
    "v_mul_f32 v212, v144, v212\n"
    "v_mfma_f32_16x16x32_bf16 a[208:211], a[132:135], v[52:55], a[208:211]\n"
    "v_cvt_pk_bf16_f32 v44, v100, v101\n"
    "v_mul_f32 v213, v144, v213\n"
    "v_mul_f32 v214, v144, v214\n"
    "v_mfma_f32_16x16x32_bf16 a[212:215], a[132:135], v[60:63], a[212:215]\n"
    "v_exp_f32 v103, v103\n"
    "v_mul_f32 v215, v144, v215\n"
    "v_cvt_pk_bf16_f32 v212, v212, v213\n"
    "v_mfma_f32_16x16x32_bf16 a[212:215], a[128:131], v[56:59], a[212:215]\n"
    "v_cvt_pk_bf16_f32 v45, v102, v103\n"
    "v_cvt_pk_bf16_f32 v213, v214, v215\n"
    "v_mul_f32 v216, v145, v216\n"
    "v_mfma_f32_16x16x32_bf16 v[100:103], a[112:115], a[16:19], v[108:111]\n"
    "v_exp_f32 v96, v96\n"
    "v_mfma_f32_16x16x32_bf16 v[100:103], a[116:119], a[20:23], v[100:103]\n"
    "ds_read_b64_tr_b16 a[136:137], v130 offset:49920\n"
    "ds_write_b64 v135, v[212:213] offset:17408\n"
    "v_mfma_f32_16x16x32_bf16 v[100:103], a[120:123], a[24:27], v[100:103]\n"
    "v_exp_f32 v97, v97\n"
    "v_mul_f32 v217, v145, v217\n"
    "v_mul_f32 v218, v145, v218\n"
    "v_mfma_f32_16x16x32_bf16 v[100:103], a[124:127], a[28:31], v[100:103]\n"
    "ds_read_b64_tr_b16 a[138:139], v130 offset:54016\n"
    "v_mul_f32 v219, v145, v219\n"
    "v_cvt_pk_bf16_f32 v216, v216, v217\n"
    "v_mfma_f32_16x16x32_bf16 a[216:219], a[128:131], v[64:67], a[216:219]\n"
    "v_exp_f32 v98, v98\n"
    "v_cvt_pk_bf16_f32 v217, v218, v219\n"
    "v_mfma_f32_16x16x32_bf16 a[216:219], a[132:135], v[68:71], a[216:219]\n"
    "ds_write_b64 v135, v[216:217] offset:21504\n"
    "v_cvt_pk_bf16_f32 v6, v96, v97\n"
    "v_mfma_f32_16x16x32_bf16 a[220:223], a[132:135], v[76:79], a[220:223]\n"
    "v_exp_f32 v99, v99\n"
    "v_mfma_f32_16x16x32_bf16 a[220:223], a[128:131], v[72:75], a[220:223]\n"
    "v_cvt_pk_bf16_f32 v7, v98, v99\n"
    "v_mul_f32 v220, v140, v220\n"
    "v_mul_f32 v221, v140, v221\n"
    "v_mul_f32 v222, v140, v222\n"
    "v_mfma_f32_16x16x32_bf16 v[96:99], a[112:115], a[32:35], v[112:115]\n"
    "v_exp_f32 v100, v100\n"
    "v_mul_f32 v223, v140, v223\n"
    "v_mfma_f32_16x16x32_bf16 v[96:99], a[116:119], a[36:39], v[96:99]\n"
    "ds_read_b64_tr_b16 a[140:141], v130 offset:58112\n"
    "v_cvt_pk_bf16_f32 v220, v220, v221\n"
    "v_cvt_pk_bf16_f32 v221, v222, v223\n"
    "v_mfma_f32_16x16x32_bf16 v[96:99], a[120:123], a[40:43], v[96:99]\n"
    "v_exp_f32 v101, v101\n"
    "v_mfma_f32_16x16x32_bf16 v[96:99], a[124:127], a[44:47], v[96:99]\n"
    "ds_read_b64_tr_b16 a[142:143], v130 offset:62208\n"
    "ds_write_b64 v135, v[220:221] offset:1536\n"
    "v_mul_f32 v224, v141, v224\n"
    "v_mfma_f32_16x16x32_bf16 a[224:227], a[128:131], v[80:83], a[224:227]\n"
    "v_exp_f32 v102, v102\n"
    "v_mul_f32 v225, v141, v225\n"
    "v_mfma_f32_16x16x32_bf16 a[224:227], a[132:135], v[84:87], a[224:227]\n"
    "v_cvt_pk_bf16_f32 v14, v100, v101\n"
    "v_mul_f32 v226, v141, v226\n"
    "v_mul_f32 v227, v141, v227\n"
    "v_mfma_f32_16x16x32_bf16 a[228:231], a[132:135], v[92:95], a[228:231]\n"
    "v_exp_f32 v103, v103\n"
    "v_cvt_pk_bf16_f32 v224, v224, v225\n"
    "v_mfma_f32_16x16x32_bf16 a[228:231], a[128:131], v[88:91], a[228:231]\n"
    "v_cvt_pk_bf16_f32 v15, v102, v103\n"
    "v_cvt_pk_bf16_f32 v225, v226, v227\n"
    "v_mul_f32 v228, v142, v228\n"
    "v_mfma_f32_16x16x32_bf16 v[100:103], a[112:115], a[48:51], v[116:119]\n"
    "ds_write_b64 v135, v[224:225] offset:5632\n"
    "v_exp_f32 v96, v96\n"
    "v_mfma_f32_16x16x32_bf16 v[100:103], a[116:119], a[52:55], v[100:103]\n"
    "v_mul_f32 v229, v142, v229\n"
    "v_mul_f32 v230, v142, v230\n"
    "v_mfma_f32_16x16x32_bf16 v[100:103], a[120:123], a[56:59], v[100:103]\n"
    "s_waitcnt vmcnt(4) lgkmcnt(2)\n"
    "v_exp_f32 v97, v97\n"
    "v_mul_f32 v231, v142, v231\n"
    "v_mfma_f32_16x16x32_bf16 v[100:103], a[124:127], a[60:63], v[100:103]\n"
    "s_barrier\n"
    "v_cvt_pk_bf16_f32 v228, v228, v229\n"
    "v_mfma_f32_16x16x32_bf16 a[232:235], a[136:139], v[48:51], a[232:235]\n"
    "v_exp_f32 v98, v98\n"
    "v_cvt_pk_bf16_f32 v229, v230, v231\n"
    "v_mfma_f32_16x16x32_bf16 a[232:235], a[140:143], v[52:55], a[232:235]\n"
    "ds_read_b128 a[96:99], v128 offset:16384\n"
    "ds_write_b64 v135, v[228:229] offset:9728\n"
    "v_cvt_pk_bf16_f32 v22, v96, v97\n"
    "v_mfma_f32_16x16x32_bf16 a[236:239], a[140:143], v[60:63], a[236:239]\n"
    "v_exp_f32 v99, v99\n"
    "v_mfma_f32_16x16x32_bf16 a[236:239], a[136:139], v[56:59], a[236:239]\n"
    "ds_read_b128 a[100:103], v128 offset:16640\n"
    "v_cvt_pk_bf16_f32 v23, v98, v99\n"
    "v_mul_f32 v232, v143, v232\n"
    "v_mfma_f32_16x16x32_bf16 v[96:99], a[112:115], a[64:67], v[120:123]\n"
    "v_exp_f32 v100, v100\n"
    "v_mul_f32 v233, v143, v233\n"
    "v_mfma_f32_16x16x32_bf16 v[96:99], a[116:119], a[68:71], v[96:99]\n"
    "ds_read_b64_tr_b16 a[128:129], v129 offset:32768\n"
    "ds_read_b64_tr_b16 a[130:131], v129 offset:36864\n"
    "v_mul_f32 v234, v143, v234\n"
    "v_mul_f32 v235, v143, v235\n"
    "v_mfma_f32_16x16x32_bf16 v[96:99], a[120:123], a[72:75], v[96:99]\n"
    "s_mov_b32 m0, s86\n"
    "v_exp_f32 v101, v101\n"
    "v_cvt_pk_bf16_f32 v232, v232, v233\n"
    "v_mfma_f32_16x16x32_bf16 v[96:99], a[124:127], a[76:79], v[96:99]\n"
    "buffer_load_dwordx4 v131, s[44:47], s57 offen nt lds\n"
    "v_cvt_pk_bf16_f32 v233, v234, v235\n"
    "v_mfma_f32_16x16x32_bf16 a[240:243], a[136:139], v[64:67], a[240:243]\n"
    "v_exp_f32 v102, v102\n"
    "v_mfma_f32_16x16x32_bf16 a[240:243], a[140:143], v[68:71], a[240:243]\n"
    "ds_read_b128 a[104:107], v128 offset:16896\n"
    "ds_write_b64 v135, v[232:233] offset:13824\n"
    "v_cvt_pk_bf16_f32 v30, v100, v101\n"
    "v_mfma_f32_16x16x32_bf16 a[244:247], a[140:143], v[76:79], a[244:247]\n"
    "v_exp_f32 v103, v103\n"
    "v_mul_f32 v236, v144, v236\n"
    "v_mfma_f32_16x16x32_bf16 a[244:247], a[136:139], v[72:75], a[244:247]\n"
    "ds_read_b128 a[108:111], v128 offset:17152\n"
    "v_cvt_pk_bf16_f32 v31, v102, v103\n"
    "v_mul_f32 v237, v144, v237\n"
    "v_mfma_f32_16x16x32_bf16 v[100:103], a[112:115], a[80:83], v[124:127]\n"
    "v_exp_f32 v96, v96\n"
    "v_mul_f32 v238, v144, v238\n"
    "v_mfma_f32_16x16x32_bf16 v[100:103], a[116:119], a[84:87], v[100:103]\n"
    "ds_read_b64_tr_b16 a[132:133], v129 offset:40960\n"
    "ds_read_b64_tr_b16 a[134:135], v129 offset:45056\n"
    "v_mul_f32 v239, v144, v239\n"
    "v_mfma_f32_16x16x32_bf16 v[100:103], a[120:123], a[88:91], v[100:103]\n"
    "v_exp_f32 v97, v97\n"
    "v_cvt_pk_bf16_f32 v236, v236, v237\n"
    "v_cvt_pk_bf16_f32 v237, v238, v239\n"
    "v_mfma_f32_16x16x32_bf16 v[100:103], a[124:127], a[92:95], v[100:103]\n"
    "buffer_load_dwordx4 v131, s[44:47], s57 offen offset:2048 nt lds\n"
    "v_mfma_f32_16x16x32_bf16 a[248:251], a[136:139], v[80:83], a[248:251]\n"
    "ds_write_b64 v135, v[236:237] offset:17920\n"
    "v_exp_f32 v98, v98\n"
    "v_mfma_f32_16x16x32_bf16 a[248:251], a[140:143], v[84:87], a[248:251]\n"
    "v_cvt_pk_bf16_f32 v38, v96, v97\n"
    "v_mul_f32 v240, v145, v240\n"
    "v_mfma_f32_16x16x32_bf16 a[252:255], a[140:143], v[92:95], a[252:255]\n"
    "v_exp_f32 v99, v99\n"
    "v_mul_f32 v241, v145, v241\n"
    "v_mfma_f32_16x16x32_bf16 a[252:255], a[136:139], v[88:91], a[252:255]\n"
    "s_waitcnt lgkmcnt(3)\n"
    "v_cvt_pk_bf16_f32 v39, v98, v99\n"
    "v_mul_f32 v242, v145, v242\n"
    "v_mul_f32 v243, v145, v243\n"
    "v_cvt_pk_bf16_f32 v240, v240, v241\n"
    "v_mfma_f32_16x16x32_bf16 v[96:99], a[96:99], a[0:3], v[104:107]\n"
    "s_mov_b32 m0, s87\n"
    "v_exp_f32 v100, v100\n"
    "v_cvt_pk_bf16_f32 v241, v242, v243\n"
    "v_mfma_f32_16x16x32_bf16 v[96:99], a[100:103], a[4:7], v[96:99]\n"
    "buffer_load_dwordx4 v131, s[44:47], s70 offen nt lds\n"
    "v_mfma_f32_16x16x32_bf16 v[96:99], a[104:107], a[8:11], v[96:99]\n"
    "ds_write_b64 v135, v[240:241] offset:22016\n"
    "v_exp_f32 v101, v101\n"
    "v_mfma_f32_16x16x32_bf16 v[96:99], a[108:111], a[12:15], v[96:99]\n"
    "s_waitcnt lgkmcnt(2)\n"
    "v_mul_f32 v244, v140, v244\n"
    "v_mul_f32 v245, v140, v245\n"
    "v_mfma_f32_16x16x32_bf16 v[148:151], a[128:131], v[0:3], 0\n"
    "v_exp_f32 v102, v102\n"
    "v_mul_f32 v246, v140, v246\n"
    "v_mul_f32 v247, v140, v247\n"
    "v_mfma_f32_16x16x32_bf16 v[148:151], a[132:135], v[4:7], v[148:151]\n"
    "ds_read_b128 a[112:115], v128 offset:20480\n"
    "v_cvt_pk_bf16_f32 v46, v100, v101\n"
    "v_cvt_pk_bf16_f32 v244, v244, v245\n"
    "v_mfma_f32_16x16x32_bf16 v[152:155], a[132:135], v[12:15], 0\n"
    "v_exp_f32 v103, v103\n"
    "v_cvt_pk_bf16_f32 v245, v246, v247\n"
    "v_mfma_f32_16x16x32_bf16 v[152:155], a[128:131], v[8:11], v[152:155]\n"
    "ds_read_b128 a[116:119], v128 offset:20736\n"
    "v_cvt_pk_bf16_f32 v47, v102, v103\n"
    "v_mfma_f32_16x16x32_bf16 v[100:103], a[96:99], a[16:19], v[108:111]\n"
    "ds_write_b64 v135, v[244:245] offset:2048\n"
    "v_exp_f32 v96, v96\n"
    "v_mfma_f32_16x16x32_bf16 v[100:103], a[100:103], a[20:23], v[100:103]\n"
    "ds_read_b64_tr_b16 a[136:137], v130 offset:32768\n"
    "ds_read_b64_tr_b16 a[138:139], v130 offset:36864\n"
    "v_mul_f32 v248, v141, v248\n"
    "v_mfma_f32_16x16x32_bf16 v[100:103], a[104:107], a[24:27], v[100:103]\n"
    "v_exp_f32 v97, v97\n"
    "v_mul_f32 v249, v141, v249\n"
    "v_mfma_f32_16x16x32_bf16 v[100:103], a[108:111], a[28:31], v[100:103]\n"
    "buffer_load_dwordx4 v131, s[44:47], s70 offen offset:2048 nt lds\n"
    "v_mul_f32 v250, v141, v250\n"
    "v_mfma_f32_16x16x32_bf16 v[156:159], a[128:131], v[16:19], 0\n"
    "v_exp_f32 v98, v98\n"
    "v_mul_f32 v251, v141, v251\n"
    "v_mfma_f32_16x16x32_bf16 v[156:159], a[132:135], v[20:23], v[156:159]\n"
    "v_cvt_pk_bf16_f32 v48, v96, v97\n"
    "v_cvt_pk_bf16_f32 v248, v248, v249\n"
    "v_cvt_pk_bf16_f32 v249, v250, v251\n"
    "v_mul_f32 v252, v142, v252\n"
    "v_mfma_f32_16x16x32_bf16 v[160:163], a[132:135], v[28:31], 0\n"
    "v_exp_f32 v99, v99\n"
    "v_mfma_f32_16x16x32_bf16 v[160:163], a[128:131], v[24:27], v[160:163]\n"
    "ds_write_b64 v135, v[248:249] offset:6144\n"
    "v_cvt_pk_bf16_f32 v49, v98, v99\n"
    "v_mfma_f32_16x16x32_bf16 v[96:99], a[96:99], a[32:35], v[112:115]\n"
    "v_exp_f32 v100, v100\n"
    "v_mul_f32 v253, v142, v253\n"
    "v_mfma_f32_16x16x32_bf16 v[96:99], a[100:103], a[36:39], v[96:99]\n"
    "ds_read_b64_tr_b16 a[140:141], v130 offset:40960\n"
    "ds_read_b64_tr_b16 a[142:143], v130 offset:45056\n"
    "v_mul_f32 v254, v142, v254\n"
    "v_mul_f32 v255, v142, v255\n"
    "v_mfma_f32_16x16x32_bf16 v[96:99], a[104:107], a[40:43], v[96:99]\n"
    "s_mov_b32 m0, s81\n"
    "v_exp_f32 v101, v101\n"
    "v_cvt_pk_bf16_f32 v252, v252, v253\n"
    "v_mfma_f32_16x16x32_bf16 v[96:99], a[108:111], a[44:47], v[96:99]\n"
    "buffer_load_dwordx4 v131, s[40:43], s94 offen lds\n"
    "v_cvt_pk_bf16_f32 v253, v254, v255\n"
    "v_mfma_f32_16x16x32_bf16 v[164:167], a[128:131], v[32:35], 0\n"
    "v_exp_f32 v102, v102\n"
    "v_mfma_f32_16x16x32_bf16 v[164:167], a[132:135], v[36:39], v[164:167]\n"
    "ds_write_b64 v135, v[252:253] offset:10240\n"
    "v_cvt_pk_bf16_f32 v56, v100, v101\n"
    "v_mfma_f32_16x16x32_bf16 v[168:171], a[132:135], v[44:47], 0\n"
    "v_exp_f32 v103, v103\n"
    "v_accvgpr_read_b32 v138, a172\n"
    "v_accvgpr_read_b32 v139, a173\n"
    "v_mfma_f32_16x16x32_bf16 v[168:171], a[128:131], v[40:43], v[168:171]\n"
    "v_cvt_pk_bf16_f32 v57, v102, v103\n"
    "v_mul_f32 v138, v143, v138\n"
    "v_mul_f32 v139, v143, v139\n"
    "v_mfma_f32_16x16x32_bf16 v[100:103], a[96:99], a[48:51], v[116:119]\n"
    "v_exp_f32 v96, v96\n"
    "v_cvt_pk_bf16_f32 v136, v138, v139\n"
    "v_mfma_f32_16x16x32_bf16 v[100:103], a[100:103], a[52:55], v[100:103]\n"
    "buffer_load_dwordx4 v131, s[40:43], s94 offen offset:2048 lds\n"
    "v_accvgpr_read_b32 v138, a174\n"
    "v_mfma_f32_16x16x32_bf16 v[100:103], a[104:107], a[56:59], v[100:103]\n"
    "v_exp_f32 v97, v97\n"
    "v_accvgpr_read_b32 v139, a175\n"
    "v_mfma_f32_16x16x32_bf16 v[100:103], a[108:111], a[60:63], v[100:103]\n"
    "s_waitcnt lgkmcnt(1)\n"
    "v_mul_f32 v138, v143, v138\n"
    "v_mul_f32 v139, v143, v139\n"
    "v_cvt_pk_bf16_f32 v137, v138, v139\n"
    "v_accvgpr_read_b32 v138, a176\n"
    "v_mfma_f32_16x16x32_bf16 v[172:175], a[136:139], v[0:3], 0\n"
    "v_exp_f32 v98, v98\n"
    "v_mfma_f32_16x16x32_bf16 v[172:175], a[140:143], v[4:7], v[172:175]\n"
    "ds_read_b128 a[120:123], v128 offset:20992\n"
    "ds_write_b64 v135, v[136:137] offset:14336\n"
    "v_cvt_pk_bf16_f32 v64, v96, v97\n"
    "v_mfma_f32_16x16x32_bf16 v[176:179], a[140:143], v[12:15], 0\n"
    "v_exp_f32 v99, v99\n"
    "v_mfma_f32_16x16x32_bf16 v[176:179], a[136:139], v[8:11], v[176:179]\n"
    "ds_read_b128 a[124:127], v128 offset:21248\n"
    "v_cvt_pk_bf16_f32 v65, v98, v99\n"
    "v_accvgpr_read_b32 v139, a177\n"
    "v_mul_f32 v138, v144, v138\n"
    "v_smfmac_f32_16x16x64_bf16 a[144:147], a[168:171], v[0:7], v133\n"
    "v_mul_f32 v139, v144, v139\n"
    "v_cvt_pk_bf16_f32 v136, v138, v139\n"
    "v_accvgpr_read_b32 v138, a178\n"
    "v_mfma_f32_16x16x32_bf16 v[96:99], a[96:99], a[64:67], v[120:123]\n"
    "v_exp_f32 v100, v100\n"
    "v_accvgpr_read_b32 v139, a179\n"
    "v_mfma_f32_16x16x32_bf16 v[96:99], a[100:103], a[68:71], v[96:99]\n"
    "ds_read_b64_tr_b16 a[128:129], v129 offset:33024\n"
    "v_mul_f32 v138, v144, v138\n"
    "v_mul_f32 v139, v144, v139\n"
    "v_mfma_f32_16x16x32_bf16 v[96:99], a[104:107], a[72:75], v[96:99]\n"
    "v_exp_f32 v101, v101\n"
    "v_cvt_pk_bf16_f32 v137, v138, v139\n"
    "v_mfma_f32_16x16x32_bf16 v[96:99], a[108:111], a[76:79], v[96:99]\n"
    "ds_read_b64_tr_b16 a[130:131], v129 offset:37120\n"
    "ds_write_b64 v135, v[136:137] offset:18432\n"
    "v_mfma_f32_16x16x32_bf16 v[180:183], a[136:139], v[16:19], 0\n"
    "v_exp_f32 v102, v102\n"
    "v_accvgpr_read_b32 v138, a180\n"
    "v_mfma_f32_16x16x32_bf16 v[180:183], a[140:143], v[20:23], v[180:183]\n"
    "v_cvt_pk_bf16_f32 v72, v100, v101\n"
    "v_accvgpr_read_b32 v139, a181\n"
    "v_mul_f32 v138, v145, v138\n"
    "v_mfma_f32_16x16x32_bf16 v[184:187], a[140:143], v[28:31], 0\n"
    "v_exp_f32 v103, v103\n"
    "v_mul_f32 v139, v145, v139\n"
    "v_mfma_f32_16x16x32_bf16 v[184:187], a[136:139], v[24:27], v[184:187]\n"
    "v_cvt_pk_bf16_f32 v73, v102, v103\n"
    "v_cvt_pk_bf16_f32 v136, v138, v139\n"
    "v_accvgpr_read_b32 v138, a182\n"
    "v_accvgpr_read_b32 v139, a183\n"
    "v_mfma_f32_16x16x32_bf16 v[100:103], a[96:99], a[80:83], v[124:127]\n"
    "v_exp_f32 v96, v96\n"
    "v_mul_f32 v138, v145, v138\n"
    "v_mfma_f32_16x16x32_bf16 v[100:103], a[100:103], a[84:87], v[100:103]\n"
    "ds_read_b64_tr_b16 a[132:133], v129 offset:41216\n"
    "v_mul_f32 v139, v145, v139\n"
    "v_cvt_pk_bf16_f32 v137, v138, v139\n"
    "v_mfma_f32_16x16x32_bf16 v[100:103], a[104:107], a[88:91], v[100:103]\n"
    "v_exp_f32 v97, v97\n"
    "v_mfma_f32_16x16x32_bf16 v[100:103], a[108:111], a[92:95], v[100:103]\n"
    "ds_read_b64_tr_b16 a[134:135], v129 offset:45312\n"
    "ds_write_b64 v135, v[136:137] offset:22528\n"
    "v_mfma_f32_16x16x32_bf16 v[188:191], a[136:139], v[32:35], 0\n"
    "v_exp_f32 v98, v98\n"
    "v_accvgpr_read_b32 v138, a184\n"
    "v_accvgpr_read_b32 v139, a185\n"
    "v_mfma_f32_16x16x32_bf16 v[188:191], a[140:143], v[36:39], v[188:191]\n"
    "v_cvt_pk_bf16_f32 v80, v96, v97\n"
    "v_mul_f32 v138, v140, v138\n"
    "v_mul_f32 v139, v140, v139\n"
    "v_mfma_f32_16x16x32_bf16 v[192:195], a[140:143], v[44:47], 0\n"
    "v_exp_f32 v99, v99\n"
    "v_cvt_pk_bf16_f32 v136, v138, v139\n"
    "v_mfma_f32_16x16x32_bf16 v[192:195], a[136:139], v[40:43], v[192:195]\n"
    "s_waitcnt lgkmcnt(6)\n"
    "v_cvt_pk_bf16_f32 v81, v98, v99\n"
    "v_accvgpr_read_b32 v138, a186\n"
    "v_accvgpr_read_b32 v139, a187\n"
    "v_mfma_f32_16x16x32_bf16 v[96:99], a[112:115], a[0:3], v[104:107]\n"
    "s_add_u32 s56, s56, 16384\n"
    "v_exp_f32 v100, v100\n"
    "v_mul_f32 v138, v140, v138\n"
    "v_mfma_f32_16x16x32_bf16 v[96:99], a[116:119], a[4:7], v[96:99]\n"
    "s_and_b32 s56, s56, 262143\n"
    "v_mul_f32 v139, v140, v139\n"
    "v_cvt_pk_bf16_f32 v137, v138, v139\n"
    "v_accvgpr_read_b32 v138, a188\n"
    "v_accvgpr_read_b32 v139, a189\n"
    "v_mfma_f32_16x16x32_bf16 v[96:99], a[120:123], a[8:11], v[96:99]\n"
    "s_or_b32 s56, s56, s61\n"
    "v_exp_f32 v101, v101\n"
    "v_mfma_f32_16x16x32_bf16 v[96:99], a[124:127], a[12:15], v[96:99]\n"
    "s_waitcnt lgkmcnt(1)\n"
    "ds_write_b64 v135, v[136:137] offset:2560\n"
    "v_mul_f32 v138, v141, v138\n"
    "v_mfma_f32_16x16x32_bf16 v[196:199], a[128:131], v[0:3], 0\n"
    "s_add_u32 s94, s56, 8192\n"
    "v_exp_f32 v102, v102\n"
    "v_mul_f32 v139, v141, v139\n"
    "v_mfma_f32_16x16x32_bf16 v[196:199], a[132:135], v[4:7], v[196:199]\n"
    "s_add_u32 s57, s57, 16384\n"
    "v_cvt_pk_bf16_f32 v88, v100, v101\n"
    "v_cvt_pk_bf16_f32 v136, v138, v139\n"
    "v_accvgpr_read_b32 v138, a190\n"
    "v_accvgpr_read_b32 v139, a191\n"
    "v_mfma_f32_16x16x32_bf16 v[200:203], a[132:135], v[12:15], 0\n"
    "s_and_b32 s57, s57, 262143\n"
    "v_exp_f32 v103, v103\n"
    "v_mul_f32 v138, v141, v138\n"
    "v_mfma_f32_16x16x32_bf16 v[200:203], a[128:131], v[8:11], v[200:203]\n"
    "s_or_b32 s57, s57, s96\n"
    "v_cvt_pk_bf16_f32 v89, v102, v103\n"
    "v_mul_f32 v139, v141, v139\n"
    "v_cvt_pk_bf16_f32 v137, v138, v139\n"
    "v_mfma_f32_16x16x32_bf16 v[100:103], a[112:115], a[16:19], v[108:111]\n"
    "s_add_u32 s70, s57, 8192\n"
    "v_exp_f32 v96, v96\n"
    "v_mfma_f32_16x16x32_bf16 v[100:103], a[116:119], a[20:23], v[100:103]\n"
    "ds_read_b64_tr_b16 a[136:137], v130 offset:33024\n"
    "ds_write_b64 v135, v[136:137] offset:6656\n"
    "v_mfma_f32_16x16x32_bf16 v[100:103], a[120:123], a[24:27], v[100:103]\n"
    "v_exp_f32 v97, v97\n"
    "v_accvgpr_read_b32 v138, a192\n"
    "v_accvgpr_read_b32 v139, a193\n"
    "v_mfma_f32_16x16x32_bf16 v[100:103], a[124:127], a[28:31], v[100:103]\n"
    "ds_read_b64_tr_b16 a[138:139], v130 offset:37120\n"
    "v_mul_f32 v138, v142, v138\n"
    "v_mul_f32 v139, v142, v139\n"
    "v_mfma_f32_16x16x32_bf16 v[204:207], a[128:131], v[16:19], 0\n"
    "v_exp_f32 v98, v98\n"
    "v_cvt_pk_bf16_f32 v136, v138, v139\n"
    "v_mfma_f32_16x16x32_bf16 v[204:207], a[132:135], v[20:23], v[204:207]\n"
    "v_cvt_pk_bf16_f32 v50, v96, v97\n"
    "v_accvgpr_read_b32 v138, a194\n"
    "v_accvgpr_read_b32 v139, a195\n"
    "v_mfma_f32_16x16x32_bf16 v[208:211], a[132:135], v[28:31], 0\n"
    "v_exp_f32 v99, v99\n"
    "v_mul_f32 v138, v142, v138\n"
    "v_mul_f32 v139, v142, v139\n"
    "v_mfma_f32_16x16x32_bf16 v[208:211], a[128:131], v[24:27], v[208:211]\n"
    "v_cvt_pk_bf16_f32 v51, v98, v99\n"
    "v_cvt_pk_bf16_f32 v137, v138, v139\n"
    "v_accvgpr_read_b32 v138, a196\n"
    "v_smfmac_f32_16x16x64_bf16 a[148:151], a[168:171], v[8:15], v133\n"
    "ds_write_b64 v135, v[136:137] offset:10752\n"
    "v_mfma_f32_16x16x32_bf16 v[96:99], a[112:115], a[32:35], v[112:115]\n"
    "v_exp_f32 v100, v100\n"
    "v_accvgpr_read_b32 v139, a197\n"
    "v_mfma_f32_16x16x32_bf16 v[96:99], a[116:119], a[36:39], v[96:99]\n"
    "ds_read_b64_tr_b16 a[140:141], v130 offset:41216\n"
    "v_mul_f32 v138, v143, v138\n"
    "v_mul_f32 v139, v143, v139\n"
    "v_mfma_f32_16x16x32_bf16 v[96:99], a[120:123], a[40:43], v[96:99]\n"
    "v_exp_f32 v101, v101\n"
    "v_cvt_pk_bf16_f32 v136, v138, v139\n"
    "v_accvgpr_read_b32 v138, a198\n"
    "v_mfma_f32_16x16x32_bf16 v[96:99], a[124:127], a[44:47], v[96:99]\n"
    "ds_read_b64_tr_b16 a[142:143], v130 offset:45312\n"
    "v_accvgpr_read_b32 v139, a199\n"
    "v_mul_f32 v138, v143, v138\n"
    "v_mfma_f32_16x16x32_bf16 v[212:215], a[128:131], v[32:35], 0\n"
    "v_exp_f32 v102, v102\n"
    "v_mul_f32 v139, v143, v139\n"
    "v_mfma_f32_16x16x32_bf16 v[212:215], a[132:135], v[36:39], v[212:215]\n"
    "v_cvt_pk_bf16_f32 v58, v100, v101\n"
    "v_cvt_pk_bf16_f32 v137, v138, v139\n"
    "v_accvgpr_read_b32 v138, a200\n"
    "v_mfma_f32_16x16x32_bf16 v[216:219], a[132:135], v[44:47], 0\n"
    "v_exp_f32 v103, v103\n"
    "v_mfma_f32_16x16x32_bf16 v[216:219], a[128:131], v[40:43], v[216:219]\n"
    "ds_write_b64 v135, v[136:137] offset:14848\n"
    "v_cvt_pk_bf16_f32 v59, v102, v103\n"
    "v_accvgpr_read_b32 v139, a201\n"
    "v_mfma_f32_16x16x32_bf16 v[100:103], a[112:115], a[48:51], v[116:119]\n"
    "v_exp_f32 v96, v96\n"
    "v_mul_f32 v138, v144, v138\n"
    "v_mfma_f32_16x16x32_bf16 v[100:103], a[116:119], a[52:55], v[100:103]\n"
    "v_mul_f32 v139, v144, v139\n"
    "v_cvt_pk_bf16_f32 v136, v138, v139\n"
    "v_accvgpr_read_b32 v138, a202\n"
    "v_mfma_f32_16x16x32_bf16 v[100:103], a[120:123], a[56:59], v[100:103]\n"
    "s_waitcnt vmcnt(8) lgkmcnt(1)\n"
    "v_exp_f32 v97, v97\n"
    "v_accvgpr_read_b32 v139, a203\n"
    "v_mfma_f32_16x16x32_bf16 v[100:103], a[124:127], a[60:63], v[100:103]\n"
    "s_barrier\n"
    "v_mul_f32 v138, v144, v138\n"
    "v_mul_f32 v139, v144, v139\n"
    "v_mfma_f32_16x16x32_bf16 v[220:223], a[136:139], v[0:3], 0\n"
    "v_exp_f32 v98, v98\n"
    "v_cvt_pk_bf16_f32 v137, v138, v139\n"
    "v_mfma_f32_16x16x32_bf16 v[220:223], a[140:143], v[4:7], v[220:223]\n"
    "ds_read_b128 a[96:99], v128 offset:24576\n"
    "v_cvt_pk_bf16_f32 v66, v96, v97\n"
    "v_mfma_f32_16x16x32_bf16 v[224:227], a[140:143], v[12:15], 0\n"
    "ds_write_b64 v135, v[136:137] offset:18944\n"
    "v_exp_f32 v99, v99\n"
    "v_mfma_f32_16x16x32_bf16 v[224:227], a[136:139], v[8:11], v[224:227]\n"
    "ds_read_b128 a[100:103], v128 offset:24832\n"
    "v_cvt_pk_bf16_f32 v67, v98, v99\n"
    "v_mfma_f32_16x16x32_bf16 v[96:99], a[112:115], a[64:67], v[120:123]\n"
    "v_exp_f32 v100, v100\n"
    "v_accvgpr_read_b32 v138, a204\n"
    "v_accvgpr_read_b32 v139, a205\n"
    "v_mfma_f32_16x16x32_bf16 v[96:99], a[116:119], a[68:71], v[96:99]\n"
    "ds_read_b64_tr_b16 a[128:129], v129 offset:33280\n"
    "ds_read_b64_tr_b16 a[130:131], v129 offset:37376\n"
    "v_mul_f32 v138, v145, v138\n"
    "v_mfma_f32_16x16x32_bf16 v[96:99], a[120:123], a[72:75], v[96:99]\n"
    "s_mov_b32 m0, s82\n"
    "v_exp_f32 v101, v101\n"
    "v_mul_f32 v139, v145, v139\n"
    "v_mfma_f32_16x16x32_bf16 v[96:99], a[124:127], a[76:79], v[96:99]\n"
    "buffer_load_dwordx4 v131, s[40:43], s56 offen lds\n"
    "v_cvt_pk_bf16_f32 v136, v138, v139\n"
    "v_mfma_f32_16x16x32_bf16 v[228:231], a[136:139], v[16:19], 0\n"
    "v_exp_f32 v102, v102\n"
    "v_accvgpr_read_b32 v138, a206\n"
    "v_accvgpr_read_b32 v139, a207\n"
    "v_mfma_f32_16x16x32_bf16 v[228:231], a[140:143], v[20:23], v[228:231]\n"
    "ds_read_b128 a[104:107], v128 offset:25088\n"
    "v_cvt_pk_bf16_f32 v74, v100, v101\n"
    "v_mul_f32 v138, v145, v138\n"
    "v_mfma_f32_16x16x32_bf16 v[232:235], a[140:143], v[28:31], 0\n"
    "v_exp_f32 v103, v103\n"
    "v_mul_f32 v139, v145, v139\n"
    "v_mfma_f32_16x16x32_bf16 v[232:235], a[136:139], v[24:27], v[232:235]\n"
    "ds_read_b128 a[108:111], v128 offset:25344\n"
    "v_cvt_pk_bf16_f32 v75, v102, v103\n"
    "v_cvt_pk_bf16_f32 v137, v138, v139\n"
    "v_mfma_f32_16x16x32_bf16 v[100:103], a[112:115], a[80:83], v[124:127]\n"
    "v_exp_f32 v96, v96\n"
    "v_mfma_f32_16x16x32_bf16 v[100:103], a[116:119], a[84:87], v[100:103]\n"
    "ds_read_b64_tr_b16 a[132:133], v129 offset:41472\n"
    "ds_read_b64_tr_b16 a[134:135], v129 offset:45568\n"
    "ds_write_b64 v135, v[136:137] offset:23040\n"
    "v_mfma_f32_16x16x32_bf16 v[100:103], a[120:123], a[88:91], v[100:103]\n"
    "v_exp_f32 v97, v97\n"
    "v_accvgpr_read_b32 v138, a208\n"
    "v_mfma_f32_16x16x32_bf16 v[100:103], a[124:127], a[92:95], v[100:103]\n"
    "buffer_load_dwordx4 v131, s[40:43], s56 offen offset:2048 lds\n"
    "v_accvgpr_read_b32 v139, a209\n"
    "v_mfma_f32_16x16x32_bf16 v[236:239], a[136:139], v[32:35], 0\n"
    "v_exp_f32 v98, v98\n"
    "v_mul_f32 v138, v140, v138\n"
    "v_mfma_f32_16x16x32_bf16 v[236:239], a[140:143], v[36:39], v[236:239]\n"
    "v_cvt_pk_bf16_f32 v82, v96, v97\n"
    "v_mul_f32 v139, v140, v139\n"
    "v_cvt_pk_bf16_f32 v136, v138, v139\n"
    "v_mfma_f32_16x16x32_bf16 v[240:243], a[140:143], v[44:47], 0\n"
    "v_exp_f32 v99, v99\n"
    "v_accvgpr_read_b32 v138, a210\n"
    "v_accvgpr_read_b32 v139, a211\n"
    "v_mfma_f32_16x16x32_bf16 v[240:243], a[136:139], v[40:43], v[240:243]\n"
    "s_waitcnt lgkmcnt(3)\n"
    "v_cvt_pk_bf16_f32 v83, v98, v99\n"
    "v_mul_f32 v138, v140, v138\n"
    "v_mul_f32 v139, v140, v139\n"
    "v_smfmac_f32_16x16x64_bf16 a[152:155], a[168:171], v[16:23], v133\n"
    "v_cvt_pk_bf16_f32 v137, v138, v139\n"
    "v_accvgpr_read_b32 v138, a212\n"
    "v_accvgpr_read_b32 v139, a213\n"
    "v_mfma_f32_16x16x32_bf16 v[96:99], a[96:99], a[0:3], v[104:107]\n"
    "v_exp_f32 v100, v100\n"
    "v_mfma_f32_16x16x32_bf16 v[96:99], a[100:103], a[4:7], v[96:99]\n"
    "ds_write_b64 v135, v[136:137] offset:3072\n"
    "v_mul_f32 v138, v141, v138\n"
    "v_mul_f32 v139, v141, v139\n"
    "v_mfma_f32_16x16x32_bf16 v[96:99], a[104:107], a[8:11], v[96:99]\n"
    "v_exp_f32 v101, v101\n"
    "v_cvt_pk_bf16_f32 v136, v138, v139\n"
    "v_mfma_f32_16x16x32_bf16 v[96:99], a[108:111], a[12:15], v[96:99]\n"
    "s_waitcnt lgkmcnt(2)\n"
    "v_accvgpr_read_b32 v138, a214\n"
    "v_accvgpr_read_b32 v139, a215\n"
    "v_mul_f32 v138, v141, v138\n"
    "v_mfma_f32_16x16x32_bf16 v[244:247], a[128:131], v[0:3], 0\n"
    "v_exp_f32 v102, v102\n"
    "v_mul_f32 v139, v141, v139\n"
    "v_mfma_f32_16x16x32_bf16 v[244:247], a[132:135], v[4:7], v[244:247]\n"
    "ds_read_b128 a[112:115], v128 offset:28672\n"
    "v_cvt_pk_bf16_f32 v90, v100, v101\n"
    "v_cvt_pk_bf16_f32 v137, v138, v139\n"
    "v_mfma_f32_16x16x32_bf16 v[248:251], a[132:135], v[12:15], 0\n"
    "ds_write_b64 v135, v[136:137] offset:7168\n"
    "v_exp_f32 v103, v103\n"
    "v_mfma_f32_16x16x32_bf16 v[248:251], a[128:131], v[8:11], v[248:251]\n"
    "ds_read_b128 a[116:119], v128 offset:28928\n"
    "v_cvt_pk_bf16_f32 v91, v102, v103\n"
    "v_mfma_f32_16x16x32_bf16 v[100:103], a[96:99], a[16:19], v[108:111]\n"
    "v_exp_f32 v96, v96\n"
    "v_accvgpr_read_b32 v138, a216\n"
    "v_mfma_f32_16x16x32_bf16 v[100:103], a[100:103], a[20:23], v[100:103]\n"
    "ds_read_b64_tr_b16 a[136:137], v130 offset:33280\n"
    "v_accvgpr_read_b32 v139, a217\n"
    "v_mul_f32 v138, v142, v138\n"
    "v_mfma_f32_16x16x32_bf16 v[100:103], a[104:107], a[24:27], v[100:103]\n"
    "v_exp_f32 v97, v97\n"
    "v_mul_f32 v139, v142, v139\n"
    "v_cvt_pk_bf16_f32 v136, v138, v139\n"
    "v_mfma_f32_16x16x32_bf16 v[100:103], a[108:111], a[28:31], v[100:103]\n"
    "ds_read_b64_tr_b16 a[138:139], v130 offset:37376\n"
    "v_accvgpr_read_b32 v138, a218\n"
    "v_accvgpr_read_b32 v139, a219\n"
    "v_mfma_f32_16x16x32_bf16 v[252:255], a[128:131], v[16:19], 0\n"
    "v_exp_f32 v98, v98\n"
    "v_mul_f32 v138, v142, v138\n"
    "v_mfma_f32_16x16x32_bf16 v[252:255], a[132:135], v[20:23], v[252:255]\n"
    "v_cvt_pk_bf16_f32 v52, v96, v97\n"
    "v_mul_f32 v139, v142, v139\n"
    "v_cvt_pk_bf16_f32 v137, v138, v139\n"
    "v_mfma_f32_16x16x32_bf16 a[172:175], a[132:135], v[28:31], 0\n"
    "v_exp_f32 v99, v99\n"
    "v_mfma_f32_16x16x32_bf16 a[172:175], a[128:131], v[24:27], a[172:175]\n"
    "ds_write_b64 v135, v[136:137] offset:11264\n"
    "v_cvt_pk_bf16_f32 v53, v98, v99\n"
    "v_accvgpr_read_b32 v138, a220\n"
    "v_mfma_f32_16x16x32_bf16 v[96:99], a[96:99], a[32:35], v[112:115]\n"
    "v_exp_f32 v100, v100\n"
    "v_accvgpr_read_b32 v139, a221\n"
    "v_mfma_f32_16x16x32_bf16 v[96:99], a[100:103], a[36:39], v[96:99]\n"
    "ds_read_b64_tr_b16 a[140:141], v130 offset:41472\n"
    "v_mul_f32 v138, v143, v138\n"
    "v_mul_f32 v139, v143, v139\n"
    "v_mfma_f32_16x16x32_bf16 v[96:99], a[104:107], a[40:43], v[96:99]\n"
    "v_exp_f32 v101, v101\n"
    "v_cvt_pk_bf16_f32 v136, v138, v139\n"
    "v_mfma_f32_16x16x32_bf16 v[96:99], a[108:111], a[44:47], v[96:99]\n"
    "ds_read_b64_tr_b16 a[142:143], v130 offset:45568\n"
    "v_accvgpr_read_b32 v138, a222\n"
    "v_accvgpr_read_b32 v139, a223\n"
    "v_mul_f32 v138, v143, v138\n"
    "v_mfma_f32_16x16x32_bf16 a[176:179], a[128:131], v[32:35], 0\n"
    "v_exp_f32 v102, v102\n"
    "v_mul_f32 v139, v143, v139\n"
    "v_mfma_f32_16x16x32_bf16 a[176:179], a[132:135], v[36:39], a[176:179]\n"
    "v_cvt_pk_bf16_f32 v60, v100, v101\n"
    "v_cvt_pk_bf16_f32 v137, v138, v139\n"
    "v_accvgpr_read_b32 v138, a224\n"
    "v_mfma_f32_16x16x32_bf16 a[180:183], a[132:135], v[44:47], 0\n"
    "v_exp_f32 v103, v103\n"
    "v_mfma_f32_16x16x32_bf16 a[180:183], a[128:131], v[40:43], a[180:183]\n"
    "ds_write_b64 v135, v[136:137] offset:15360\n"
    "v_cvt_pk_bf16_f32 v61, v102, v103\n"
    "v_mfma_f32_16x16x32_bf16 v[100:103], a[96:99], a[48:51], v[116:119]\n"
    "v_exp_f32 v96, v96\n"
    "v_accvgpr_read_b32 v139, a225\n"
    "v_mul_f32 v138, v144, v138\n"
    "v_mfma_f32_16x16x32_bf16 v[100:103], a[100:103], a[52:55], v[100:103]\n"
    "v_mul_f32 v139, v144, v139\n"
    "v_cvt_pk_bf16_f32 v136, v138, v139\n"
    "v_accvgpr_read_b32 v138, a226\n"
    "v_mfma_f32_16x16x32_bf16 v[100:103], a[104:107], a[56:59], v[100:103]\n"
    "v_exp_f32 v97, v97\n"
    "v_accvgpr_read_b32 v139, a227\n"
    "v_mfma_f32_16x16x32_bf16 v[100:103], a[108:111], a[60:63], v[100:103]\n"
    "s_waitcnt lgkmcnt(1)\n"
    "v_mul_f32 v138, v144, v138\n"
    "v_mul_f32 v139, v144, v139\n"
    "v_cvt_pk_bf16_f32 v137, v138, v139\n"
    "v_mfma_f32_16x16x32_bf16 a[184:187], a[136:139], v[0:3], 0\n"
    "v_exp_f32 v98, v98\n"
    "v_mfma_f32_16x16x32_bf16 a[184:187], a[140:143], v[4:7], a[184:187]\n"
    "ds_read_b128 a[120:123], v128 offset:29184\n"
    "ds_write_b64 v135, v[136:137] offset:19456\n"
    "v_cvt_pk_bf16_f32 v68, v96, v97\n"
    "v_mfma_f32_16x16x32_bf16 a[188:191], a[140:143], v[12:15], 0\n"
    "v_exp_f32 v99, v99\n"
    "v_accvgpr_read_b32 v138, a228\n"
    "v_mfma_f32_16x16x32_bf16 a[188:191], a[136:139], v[8:11], a[188:191]\n"
    "ds_read_b128 a[124:127], v128 offset:29440\n"
    "v_cvt_pk_bf16_f32 v69, v98, v99\n"
    "v_accvgpr_read_b32 v139, a229\n"
    "v_smfmac_f32_16x16x64_bf16 a[156:159], a[168:171], v[24:31], v133\n"
    "v_mul_f32 v138, v145, v138\n"
    "v_mul_f32 v139, v145, v139\n"
    "v_cvt_pk_bf16_f32 v136, v138, v139\n"
    "v_mfma_f32_16x16x32_bf16 v[96:99], a[96:99], a[64:67], v[120:123]\n"
    "v_exp_f32 v100, v100\n"
    "v_accvgpr_read_b32 v138, a230\n"
    "v_accvgpr_read_b32 v139, a231\n"
    "v_mfma_f32_16x16x32_bf16 v[96:99], a[100:103], a[68:71], v[96:99]\n"
    "ds_read_b64_tr_b16 a[128:129], v129 offset:33536\n"
    "v_mul_f32 v138, v145, v138\n"
    "v_mul_f32 v139, v145, v139\n"
    "v_mfma_f32_16x16x32_bf16 v[96:99], a[104:107], a[72:75], v[96:99]\n"
    "v_exp_f32 v101, v101\n"
    "v_cvt_pk_bf16_f32 v137, v138, v139\n"
    "v_mfma_f32_16x16x32_bf16 v[96:99], a[108:111], a[76:79], v[96:99]\n"
    "ds_read_b64_tr_b16 a[130:131], v129 offset:37632\n"
    "ds_write_b64 v135, v[136:137] offset:23552\n"
    "v_mfma_f32_16x16x32_bf16 a[192:195], a[136:139], v[16:19], 0\n"
    "v_exp_f32 v102, v102\n"
    "v_mfma_f32_16x16x32_bf16 a[192:195], a[140:143], v[20:23], a[192:195]\n"
    "v_cvt_pk_bf16_f32 v76, v100, v101\n"
    "v_accvgpr_read_b32 v138, a232\n"
    "v_accvgpr_read_b32 v139, a233\n"
    "v_mul_f32 v138, v140, v138\n"
    "v_mfma_f32_16x16x32_bf16 a[196:199], a[140:143], v[28:31], 0\n"
    "v_exp_f32 v103, v103\n"
    "v_mul_f32 v139, v140, v139\n"
    "v_mfma_f32_16x16x32_bf16 a[196:199], a[136:139], v[24:27], a[196:199]\n"
    "v_cvt_pk_bf16_f32 v77, v102, v103\n"
    "v_cvt_pk_bf16_f32 v136, v138, v139\n"
    "v_accvgpr_read_b32 v138, a234\n"
    "v_mfma_f32_16x16x32_bf16 v[100:103], a[96:99], a[80:83], v[124:127]\n"
    "v_exp_f32 v96, v96\n"
    "v_accvgpr_read_b32 v139, a235\n"
    "v_mfma_f32_16x16x32_bf16 v[100:103], a[100:103], a[84:87], v[100:103]\n"
    "ds_read_b64_tr_b16 a[132:133], v129 offset:41728\n"
    "v_mul_f32 v138, v140, v138\n"
    "v_mul_f32 v139, v140, v139\n"
    "v_cvt_pk_bf16_f32 v137, v138, v139\n"
    "v_mfma_f32_16x16x32_bf16 v[100:103], a[104:107], a[88:91], v[100:103]\n"
    "v_exp_f32 v97, v97\n"
    "v_mfma_f32_16x16x32_bf16 v[100:103], a[108:111], a[92:95], v[100:103]\n"
    "ds_read_b64_tr_b16 a[134:135], v129 offset:45824\n"
    "ds_write_b64 v135, v[136:137] offset:3584\n"
    "v_mfma_f32_16x16x32_bf16 a[200:203], a[136:139], v[32:35], 0\n"
    "v_exp_f32 v98, v98\n"
    "v_accvgpr_read_b32 v138, a236\n"
    "v_mfma_f32_16x16x32_bf16 a[200:203], a[140:143], v[36:39], a[200:203]\n"
    "v_cvt_pk_bf16_f32 v84, v96, v97\n"
    "v_accvgpr_read_b32 v139, a237\n"
    "v_mul_f32 v138, v141, v138\n"
    "v_mfma_f32_16x16x32_bf16 a[204:207], a[140:143], v[44:47], 0\n"
    "v_exp_f32 v99, v99\n"
    "v_mul_f32 v139, v141, v139\n"
    "v_cvt_pk_bf16_f32 v136, v138, v139\n"
    "v_mfma_f32_16x16x32_bf16 a[204:207], a[136:139], v[40:43], a[204:207]\n"
    "s_waitcnt lgkmcnt(6)\n"
    "v_cvt_pk_bf16_f32 v85, v98, v99\n"
    "v_accvgpr_read_b32 v138, a238\n"
    "v_accvgpr_read_b32 v139, a239\n"
    "v_mfma_f32_16x16x32_bf16 v[96:99], a[112:115], a[0:3], v[104:107]\n"
    "v_exp_f32 v100, v100\n"
    "v_mul_f32 v138, v141, v138\n"
    "v_mfma_f32_16x16x32_bf16 v[96:99], a[116:119], a[4:7], v[96:99]\n"
    "v_mul_f32 v139, v141, v139\n"
    "v_cvt_pk_bf16_f32 v137, v138, v139\n"
    "v_accvgpr_read_b32 v138, a240\n"
    "v_mfma_f32_16x16x32_bf16 v[96:99], a[120:123], a[8:11], v[96:99]\n"
    "v_exp_f32 v101, v101\n"
    "v_mfma_f32_16x16x32_bf16 v[96:99], a[124:127], a[12:15], v[96:99]\n"
    "s_waitcnt lgkmcnt(1)\n"
    "ds_write_b64 v135, v[136:137] offset:7680\n"
    "v_accvgpr_read_b32 v139, a241\n"
    "v_mul_f32 v138, v142, v138\n"
    "v_mfma_f32_16x16x32_bf16 a[208:211], a[128:131], v[0:3], 0\n"
    "v_exp_f32 v102, v102\n"
    "v_mul_f32 v139, v142, v139\n"
    "v_mfma_f32_16x16x32_bf16 a[208:211], a[132:135], v[4:7], a[208:211]\n"
    "v_cvt_pk_bf16_f32 v92, v100, v101\n"
    "v_cvt_pk_bf16_f32 v136, v138, v139\n"
    "v_accvgpr_read_b32 v138, a242\n"
    "v_mfma_f32_16x16x32_bf16 a[212:215], a[132:135], v[12:15], 0\n"
    "v_exp_f32 v103, v103\n"
    "v_accvgpr_read_b32 v139, a243\n"
    "v_mfma_f32_16x16x32_bf16 a[212:215], a[128:131], v[8:11], a[212:215]\n"
    "v_cvt_pk_bf16_f32 v93, v102, v103\n"
    "v_mul_f32 v138, v142, v138\n"
    "v_mul_f32 v139, v142, v139\n"
    "v_cvt_pk_bf16_f32 v137, v138, v139\n"
    "v_mfma_f32_16x16x32_bf16 v[100:103], a[112:115], a[16:19], v[108:111]\n"
    "v_exp_f32 v96, v96\n"
    "v_mfma_f32_16x16x32_bf16 v[100:103], a[116:119], a[20:23], v[100:103]\n"
    "ds_read_b64_tr_b16 a[136:137], v130 offset:33536\n"
    "ds_write_b64 v135, v[136:137] offset:11776\n"
    "v_mfma_f32_16x16x32_bf16 v[100:103], a[120:123], a[24:27], v[100:103]\n"
    "v_exp_f32 v97, v97\n"
    "v_accvgpr_read_b32 v138, a244\n"
    "v_mfma_f32_16x16x32_bf16 v[100:103], a[124:127], a[28:31], v[100:103]\n"
    "ds_read_b64_tr_b16 a[138:139], v130 offset:37632\n"
    "v_accvgpr_read_b32 v139, a245\n"
    "v_mul_f32 v138, v143, v138\n"
    "v_mfma_f32_16x16x32_bf16 a[216:219], a[128:131], v[16:19], 0\n"
    "v_exp_f32 v98, v98\n"
    "v_mul_f32 v139, v143, v139\n"
    "v_cvt_pk_bf16_f32 v136, v138, v139\n"
    "v_mfma_f32_16x16x32_bf16 a[216:219], a[132:135], v[20:23], a[216:219]\n"
    "v_cvt_pk_bf16_f32 v54, v96, v97\n"
    "v_accvgpr_read_b32 v138, a246\n"
    "v_accvgpr_read_b32 v139, a247\n"
    "v_mfma_f32_16x16x32_bf16 a[220:223], a[132:135], v[28:31], 0\n"
    "v_exp_f32 v99, v99\n"
    "v_mul_f32 v138, v143, v138\n"
    "v_mfma_f32_16x16x32_bf16 a[220:223], a[128:131], v[24:27], a[220:223]\n"
    "v_cvt_pk_bf16_f32 v55, v98, v99\n"
    "v_mul_f32 v139, v143, v139\n"
    "v_cvt_pk_bf16_f32 v137, v138, v139\n"
    "v_smfmac_f32_16x16x64_bf16 a[160:163], a[168:171], v[32:39], v133\n"
    "ds_write_b64 v135, v[136:137] offset:15872\n"
    "v_accvgpr_read_b32 v138, a248\n"
    "v_mfma_f32_16x16x32_bf16 v[96:99], a[112:115], a[32:35], v[112:115]\n"
    "v_exp_f32 v100, v100\n"
    "v_accvgpr_read_b32 v139, a249\n"
    "v_mfma_f32_16x16x32_bf16 v[96:99], a[116:119], a[36:39], v[96:99]\n"
    "ds_read_b64_tr_b16 a[140:141], v130 offset:41728\n"
    "v_mul_f32 v138, v144, v138\n"
    "v_mul_f32 v139, v144, v139\n"
    "v_mfma_f32_16x16x32_bf16 v[96:99], a[120:123], a[40:43], v[96:99]\n"
    "v_exp_f32 v101, v101\n"
    "v_cvt_pk_bf16_f32 v136, v138, v139\n"
    "v_mfma_f32_16x16x32_bf16 v[96:99], a[124:127], a[44:47], v[96:99]\n"
    "ds_read_b64_tr_b16 a[142:143], v130 offset:45824\n"
    "v_accvgpr_read_b32 v138, a250\n"
    "v_accvgpr_read_b32 v139, a251\n"
    "v_mfma_f32_16x16x32_bf16 a[224:227], a[128:131], v[32:35], 0\n"
    "v_exp_f32 v102, v102\n"
    "v_mul_f32 v138, v144, v138\n"
    "v_mul_f32 v139, v144, v139\n"
    "v_mfma_f32_16x16x32_bf16 a[224:227], a[132:135], v[36:39], a[224:227]\n"
    "v_cvt_pk_bf16_f32 v62, v100, v101\n"
    "v_cvt_pk_bf16_f32 v137, v138, v139\n"
    "v_accvgpr_read_b32 v138, a252\n"
    "v_mfma_f32_16x16x32_bf16 a[228:231], a[132:135], v[44:47], 0\n"
    "v_exp_f32 v103, v103\n"
    "v_mfma_f32_16x16x32_bf16 a[228:231], a[128:131], v[40:43], a[228:231]\n"
    "ds_write_b64 v135, v[136:137] offset:19968\n"
    "v_cvt_pk_bf16_f32 v63, v102, v103\n"
    "v_mfma_f32_16x16x32_bf16 v[100:103], a[112:115], a[48:51], v[116:119]\n"
    "v_exp_f32 v96, v96\n"
    "v_accvgpr_read_b32 v139, a253\n"
    "v_mfma_f32_16x16x32_bf16 v[100:103], a[116:119], a[52:55], v[100:103]\n"
    "v_mul_f32 v138, v145, v138\n"
    "v_mul_f32 v139, v145, v139\n"
    "v_cvt_pk_bf16_f32 v136, v138, v139\n"
    "v_accvgpr_read_b32 v138, a254\n"
    "v_mfma_f32_16x16x32_bf16 v[100:103], a[120:123], a[56:59], v[100:103]\n"
    "s_waitcnt vmcnt(4) lgkmcnt(1)\n"
    "v_exp_f32 v97, v97\n"
    "v_accvgpr_read_b32 v139, a255\n"
    "v_mfma_f32_16x16x32_bf16 v[100:103], a[124:127], a[60:63], v[100:103]\n"
    "s_barrier\n"
    "v_mul_f32 v138, v145, v138\n"
    "v_mfma_f32_16x16x32_bf16 a[232:235], a[136:139], v[0:3], 0\n"
    "v_exp_f32 v98, v98\n"
    "v_mul_f32 v139, v145, v139\n"
    "v_mfma_f32_16x16x32_bf16 a[232:235], a[140:143], v[4:7], a[232:235]\n"
    "ds_read_b128 a[96:99], v128 offset:0\n"
    "v_cvt_pk_bf16_f32 v70, v96, v97\n"
    "v_cvt_pk_bf16_f32 v137, v138, v139\n"
    "v_mfma_f32_16x16x32_bf16 a[236:239], a[140:143], v[12:15], 0\n"
    "ds_write_b64 v135, v[136:137] offset:24064\n"
    "v_exp_f32 v99, v99\n"
    "v_mfma_f32_16x16x32_bf16 a[236:239], a[136:139], v[8:11], a[236:239]\n"
    "ds_read_b128 a[100:103], v128 offset:256\n"
    "v_cvt_pk_bf16_f32 v71, v98, v99\n"
    "v_mfma_f32_16x16x32_bf16 v[96:99], a[112:115], a[64:67], v[120:123]\n"
    "s_cmp_lg_u64 s[78:79], 0\n"
    "v_exp_f32 v100, v100\n"
    "v_mbcnt_lo_u32_b32 v136, -1, 0\n"
    "v_mfma_f32_16x16x32_bf16 v[96:99], a[116:119], a[68:71], v[96:99]\n"
    "s_cselect_b32 s66, 1, 0\n"
    "ds_read_b64_tr_b16 a[128:129], v129 offset:49152\n"
    "ds_read_b64_tr_b16 a[130:131], v129 offset:53248\n"
    "v_mfma_f32_16x16x32_bf16 v[96:99], a[120:123], a[72:75], v[96:99]\n"
    "s_mov_b32 m0, s84\n"
    "v_exp_f32 v101, v101\n"
    "v_mbcnt_hi_u32_b32 v136, -1, v136\n"
    "v_mfma_f32_16x16x32_bf16 v[96:99], a[124:127], a[76:79], v[96:99]\n"
    "s_mov_b32 s59, 0\n"
    "buffer_load_dwordx4 v131, s[44:47], s57 offen nt lds\n"
    "v_mfma_f32_16x16x32_bf16 a[240:243], a[136:139], v[16:19], 0\n"
    "v_exp_f32 v102, v102\n"
    "v_lshrrev_b32 v137, 4, v136\n"
    "v_mfma_f32_16x16x32_bf16 a[240:243], a[140:143], v[20:23], a[240:243]\n"
    "ds_read_b128 a[104:107], v128 offset:512\n"
    "v_cvt_pk_bf16_f32 v78, v100, v101\n"
    "v_lshlrev_b32 v140, 8, v137\n"
    "v_mfma_f32_16x16x32_bf16 a[244:247], a[140:143], v[28:31], 0\n"
    "v_exp_f32 v103, v103\n"
    "v_mfma_f32_16x16x32_bf16 a[244:247], a[136:139], v[24:27], a[244:247]\n"
    "ds_read_b128 a[108:111], v128 offset:768\n"
    "v_cvt_pk_bf16_f32 v79, v102, v103\n"
    "v_lshlrev_b32 v134, 5, v137\n"
    "v_mfma_f32_16x16x32_bf16 v[100:103], a[112:115], a[80:83], v[124:127]\n"
    "v_exp_f32 v96, v96\n"
    "v_mfma_f32_16x16x32_bf16 v[100:103], a[116:119], a[84:87], v[100:103]\n"
    "ds_read_b64_tr_b16 a[132:133], v129 offset:57344\n"
    "ds_read_b64_tr_b16 a[134:135], v129 offset:61440\n"
    "v_and_b32 v137, 15, v136\n"
    "v_mfma_f32_16x16x32_bf16 v[100:103], a[120:123], a[88:91], v[100:103]\n"
    "v_exp_f32 v97, v97\n"
    "v_mfma_f32_16x16x32_bf16 v[100:103], a[124:127], a[92:95], v[100:103]\n"
    "buffer_load_dwordx4 v131, s[44:47], s57 offen offset:2048 nt lds\n"
    "v_lshl_add_u32 v140, v137, 3, v140\n"
    "v_mfma_f32_16x16x32_bf16 a[248:251], a[136:139], v[32:35], 0\n"
    "v_exp_f32 v98, v98\n"
    "v_bfe_u32 v137, v136, 2, 2\n"
    "v_mfma_f32_16x16x32_bf16 a[248:251], a[140:143], v[36:39], a[248:251]\n"
    "v_cvt_pk_bf16_f32 v86, v96, v97\n"
    "v_lshl_add_u32 v134, v137, 9, v134\n"
    "v_mfma_f32_16x16x32_bf16 a[252:255], a[140:143], v[44:47], 0\n"
    "v_exp_f32 v99, v99\n"
    "v_and_b32 v137, 3, v136\n"
    "v_mfma_f32_16x16x32_bf16 a[252:255], a[136:139], v[40:43], a[252:255]\n"
    "s_waitcnt lgkmcnt(2)\n"
    "v_cvt_pk_bf16_f32 v87, v98, v99\n"
    "v_lshl_add_u32 v134, v137, 3, v134\n"
    "v_smfmac_f32_16x16x64_bf16 a[164:167], a[168:171], v[40:47], v133\n"
    "v_add_u32 v134, s62, v134\n"
    "v_xor_b32 v142, 0x90, v134\n"
    "v_mov_b32 v141, 0\n"
    "s_mov_b32 s60, 0\n"
    ".p2align 6\n"
    "Lloop2_%=:\n"
    "v_mfma_f32_16x16x32_bf16 v[96:99], a[96:99], a[0:3], v[104:107]\n"
    "s_mov_b32 m0, s85\n"
    "v_exp_f32 v100, v100\n"
    "v_mfma_f32_16x16x32_bf16 v[96:99], a[100:103], a[4:7], v[96:99]\n"
    "buffer_load_dwordx4 v131, s[44:47], s70 offen nt lds\n"
    "v_mfma_f32_16x16x32_bf16 v[96:99], a[104:107], a[8:11], v[96:99]\n"
    "v_exp_f32 v101, v101\n"
    "v_mfma_f32_16x16x32_bf16 v[96:99], a[108:111], a[12:15], v[96:99]\n"
    "s_waitcnt lgkmcnt(0)\n"
    "v_mfma_f32_16x16x32_bf16 v[148:151], a[128:131], v[48:51], v[148:151]\n"
    "v_exp_f32 v102, v102\n"
    "v_mfma_f32_16x16x32_bf16 v[148:151], a[132:135], v[52:55], v[148:151]\n"
    "ds_read_b128 a[112:115], v128 offset:4096\n"
    "v_cvt_pk_bf16_f32 v94, v100, v101\n"
    "v_mfma_f32_16x16x32_bf16 v[152:155], a[132:135], v[60:63], v[152:155]\n"
    "v_exp_f32 v103, v103\n"
    "v_mfma_f32_16x16x32_bf16 v[152:155], a[128:131], v[56:59], v[152:155]\n"
    "ds_read_b128 a[116:119], v128 offset:4352\n"
    "v_cvt_pk_bf16_f32 v95, v102, v103\n"
    "v_mfma_f32_16x16x32_bf16 v[100:103], a[96:99], a[16:19], v[108:111]\n"
    "v_exp_f32 v96, v96\n"
    "v_mfma_f32_16x16x32_bf16 v[100:103], a[100:103], a[20:23], v[100:103]\n"
    "ds_read_b64_tr_b16 a[136:137], v130 offset:49152\n"
    "ds_read_b64_tr_b16 a[138:139], v130 offset:53248\n"
    "v_mfma_f32_16x16x32_bf16 v[100:103], a[104:107], a[24:27], v[100:103]\n"
    "v_exp_f32 v97, v97\n"
    "v_mfma_f32_16x16x32_bf16 v[100:103], a[108:111], a[28:31], v[100:103]\n"
    "buffer_load_dwordx4 v131, s[44:47], s70 offen offset:2048 nt lds\n"
    "v_mfma_f32_16x16x32_bf16 v[156:159], a[128:131], v[64:67], v[156:159]\n"
    "v_exp_f32 v98, v98\n"
    "v_mfma_f32_16x16x32_bf16 v[156:159], a[132:135], v[68:71], v[156:159]\n"
    "v_cvt_pk_bf16_f32 v0, v96, v97\n"
    "v_mfma_f32_16x16x32_bf16 v[160:163], a[132:135], v[76:79], v[160:163]\n"
    "v_exp_f32 v99, v99\n"
    "v_mfma_f32_16x16x32_bf16 v[160:163], a[128:131], v[72:75], v[160:163]\n"
    "v_cvt_pk_bf16_f32 v1, v98, v99\n"
    "v_mfma_f32_16x16x32_bf16 v[96:99], a[96:99], a[32:35], v[112:115]\n"
    "v_exp_f32 v100, v100\n"
    "v_mfma_f32_16x16x32_bf16 v[96:99], a[100:103], a[36:39], v[96:99]\n"
    "ds_read_b64_tr_b16 a[140:141], v130 offset:57344\n"
    "ds_read_b64_tr_b16 a[142:143], v130 offset:61440\n"
    "v_mfma_f32_16x16x32_bf16 v[96:99], a[104:107], a[40:43], v[96:99]\n"
    "s_mov_b32 m0, s83\n"
    "v_exp_f32 v101, v101\n"
    "v_mfma_f32_16x16x32_bf16 v[96:99], a[108:111], a[44:47], v[96:99]\n"
    "buffer_load_dwordx4 v131, s[40:43], s94 offen lds\n"
    "v_mfma_f32_16x16x32_bf16 v[164:167], a[128:131], v[80:83], v[164:167]\n"
    "v_exp_f32 v102, v102\n"
    "v_mfma_f32_16x16x32_bf16 v[164:167], a[132:135], v[84:87], v[164:167]\n"
    "v_cvt_pk_bf16_f32 v8, v100, v101\n"
    "v_mfma_f32_16x16x32_bf16 v[168:171], a[132:135], v[92:95], v[168:171]\n"
    "v_exp_f32 v103, v103\n"
    "v_mfma_f32_16x16x32_bf16 v[168:171], a[128:131], v[88:91], v[168:171]\n"
    "v_cvt_pk_bf16_f32 v9, v102, v103\n"
    "v_mfma_f32_16x16x32_bf16 v[100:103], a[96:99], a[48:51], v[116:119]\n"
    "v_exp_f32 v96, v96\n"
    "v_mfma_f32_16x16x32_bf16 v[100:103], a[100:103], a[52:55], v[100:103]\n"
    "buffer_load_dwordx4 v131, s[40:43], s94 offen offset:2048 lds\n"
    "v_mfma_f32_16x16x32_bf16 v[100:103], a[104:107], a[56:59], v[100:103]\n"
    "v_exp_f32 v97, v97\n"
    "v_mfma_f32_16x16x32_bf16 v[100:103], a[108:111], a[60:63], v[100:103]\n"
    "s_waitcnt lgkmcnt(0)\n"
    "v_mfma_f32_16x16x32_bf16 v[172:175], a[136:139], v[48:51], v[172:175]\n"
    "v_exp_f32 v98, v98\n"
    "v_mfma_f32_16x16x32_bf16 v[172:175], a[140:143], v[52:55], v[172:175]\n"
    "ds_read_b128 a[120:123], v128 offset:4608\n"
    "v_cvt_pk_bf16_f32 v16, v96, v97\n"
    "v_mfma_f32_16x16x32_bf16 v[176:179], a[140:143], v[60:63], v[176:179]\n"
    "v_exp_f32 v99, v99\n"
    "v_mfma_f32_16x16x32_bf16 v[176:179], a[136:139], v[56:59], v[176:179]\n"
    "ds_read_b128 a[124:127], v128 offset:4864\n"
    "v_cvt_pk_bf16_f32 v17, v98, v99\n"
    "v_smfmac_f32_16x16x64_bf16 a[144:147], a[168:171], v[48:55], v133\n"
    "v_mfma_f32_16x16x32_bf16 v[96:99], a[96:99], a[64:67], v[120:123]\n"
    "v_exp_f32 v100, v100\n"
    "v_mfma_f32_16x16x32_bf16 v[96:99], a[100:103], a[68:71], v[96:99]\n"
    "ds_read_b64_tr_b16 a[128:129], v129 offset:49408\n"
    "v_mfma_f32_16x16x32_bf16 v[96:99], a[104:107], a[72:75], v[96:99]\n"
    "v_exp_f32 v101, v101\n"
    "v_mfma_f32_16x16x32_bf16 v[96:99], a[108:111], a[76:79], v[96:99]\n"
    "ds_read_b64_tr_b16 a[130:131], v129 offset:53504\n"
    "v_mfma_f32_16x16x32_bf16 v[180:183], a[136:139], v[64:67], v[180:183]\n"
    "v_exp_f32 v102, v102\n"
    "v_mfma_f32_16x16x32_bf16 v[180:183], a[140:143], v[68:71], v[180:183]\n"
    "v_cvt_pk_bf16_f32 v24, v100, v101\n"
    "v_mfma_f32_16x16x32_bf16 v[184:187], a[140:143], v[76:79], v[184:187]\n"
    "v_exp_f32 v103, v103\n"
    "v_mfma_f32_16x16x32_bf16 v[184:187], a[136:139], v[72:75], v[184:187]\n"
    "v_cvt_pk_bf16_f32 v25, v102, v103\n"
    "v_mfma_f32_16x16x32_bf16 v[100:103], a[96:99], a[80:83], v[124:127]\n"
    "v_exp_f32 v96, v96\n"
    "v_mfma_f32_16x16x32_bf16 v[100:103], a[100:103], a[84:87], v[100:103]\n"
    "ds_read_b64_tr_b16 a[132:133], v129 offset:57600\n"
    "v_mfma_f32_16x16x32_bf16 v[100:103], a[104:107], a[88:91], v[100:103]\n"
    "v_exp_f32 v97, v97\n"
    "v_mfma_f32_16x16x32_bf16 v[100:103], a[108:111], a[92:95], v[100:103]\n"
    "ds_read_b64_tr_b16 a[134:135], v129 offset:61696\n"
    "v_mfma_f32_16x16x32_bf16 v[188:191], a[136:139], v[80:83], v[188:191]\n"
    "v_exp_f32 v98, v98\n"
    "v_mfma_f32_16x16x32_bf16 v[188:191], a[140:143], v[84:87], v[188:191]\n"
    "v_cvt_pk_bf16_f32 v32, v96, v97\n"
    "v_mfma_f32_16x16x32_bf16 v[192:195], a[140:143], v[92:95], v[192:195]\n"
    "v_exp_f32 v99, v99\n"
    "v_mfma_f32_16x16x32_bf16 v[192:195], a[136:139], v[88:91], v[192:195]\n"
    "s_waitcnt lgkmcnt(4)\n"
    "v_cvt_pk_bf16_f32 v33, v98, v99\n"
    "v_mfma_f32_16x16x32_bf16 v[96:99], a[112:115], a[0:3], v[104:107]\n"
    "s_add_u32 s56, s56, 16384\n"
    "v_exp_f32 v100, v100\n"
    "v_mfma_f32_16x16x32_bf16 v[96:99], a[116:119], a[4:7], v[96:99]\n"
    "s_and_b32 s56, s56, 262143\n"
    "v_mfma_f32_16x16x32_bf16 v[96:99], a[120:123], a[8:11], v[96:99]\n"
    "s_or_b32 s56, s56, s61\n"
    "v_exp_f32 v101, v101\n"
    "v_mfma_f32_16x16x32_bf16 v[96:99], a[124:127], a[12:15], v[96:99]\n"
    "s_waitcnt lgkmcnt(0)\n"
    "v_mfma_f32_16x16x32_bf16 v[196:199], a[128:131], v[48:51], v[196:199]\n"
    "s_add_u32 s94, s56, 8192\n"
    "v_exp_f32 v102, v102\n"
    "v_mfma_f32_16x16x32_bf16 v[196:199], a[132:135], v[52:55], v[196:199]\n"
    "s_add_u32 s57, s57, 16384\n"
    "v_cvt_pk_bf16_f32 v40, v100, v101\n"
    "v_mfma_f32_16x16x32_bf16 v[200:203], a[132:135], v[60:63], v[200:203]\n"
    "s_and_b32 s57, s57, 262143\n"
    "v_exp_f32 v103, v103\n"
    "v_mfma_f32_16x16x32_bf16 v[200:203], a[128:131], v[56:59], v[200:203]\n"
    "s_or_b32 s57, s57, s96\n"
    "v_cvt_pk_bf16_f32 v41, v102, v103\n"
    "v_mfma_f32_16x16x32_bf16 v[100:103], a[112:115], a[16:19], v[108:111]\n"
    "s_add_u32 s70, s57, 8192\n"
    "v_exp_f32 v96, v96\n"
    "v_mfma_f32_16x16x32_bf16 v[100:103], a[116:119], a[20:23], v[100:103]\n"
    "ds_read_b64_tr_b16 a[136:137], v130 offset:49408\n"
    "v_mfma_f32_16x16x32_bf16 v[100:103], a[120:123], a[24:27], v[100:103]\n"
    "v_exp_f32 v97, v97\n"
    "v_mfma_f32_16x16x32_bf16 v[100:103], a[124:127], a[28:31], v[100:103]\n"
    "ds_read_b64_tr_b16 a[138:139], v130 offset:53504\n"
    "v_mfma_f32_16x16x32_bf16 v[204:207], a[128:131], v[64:67], v[204:207]\n"
    "v_exp_f32 v98, v98\n"
    "v_mfma_f32_16x16x32_bf16 v[204:207], a[132:135], v[68:71], v[204:207]\n"
    "v_cvt_pk_bf16_f32 v2, v96, v97\n"
    "v_mfma_f32_16x16x32_bf16 v[208:211], a[132:135], v[76:79], v[208:211]\n"
    "v_exp_f32 v99, v99\n"
    "v_mfma_f32_16x16x32_bf16 v[208:211], a[128:131], v[72:75], v[208:211]\n"
    "v_cvt_pk_bf16_f32 v3, v98, v99\n"
    "v_smfmac_f32_16x16x64_bf16 a[148:151], a[168:171], v[56:63], v133\n"
    "v_mfma_f32_16x16x32_bf16 v[96:99], a[112:115], a[32:35], v[112:115]\n"
    "v_exp_f32 v100, v100\n"
    "v_mfma_f32_16x16x32_bf16 v[96:99], a[116:119], a[36:39], v[96:99]\n"
    "ds_read_b64_tr_b16 a[140:141], v130 offset:57600\n"
    "ds_read_b64 v[136:137], v134 offset:0\n"
    "v_mfma_f32_16x16x32_bf16 v[96:99], a[120:123], a[40:43], v[96:99]\n"
    "v_exp_f32 v101, v101\n"
    "v_mfma_f32_16x16x32_bf16 v[96:99], a[124:127], a[44:47], v[96:99]\n"
    "ds_read_b64_tr_b16 a[142:143], v130 offset:61696\n"
    "ds_read_b64 v[138:139], v142 offset:0\n"
    "v_mfma_f32_16x16x32_bf16 v[212:215], a[128:131], v[80:83], v[212:215]\n"
    "v_exp_f32 v102, v102\n"
    "v_mfma_f32_16x16x32_bf16 v[212:215], a[132:135], v[84:87], v[212:215]\n"
    "v_cvt_pk_bf16_f32 v10, v100, v101\n"
    "v_mfma_f32_16x16x32_bf16 v[216:219], a[132:135], v[92:95], v[216:219]\n"
    "v_exp_f32 v103, v103\n"
    "v_mfma_f32_16x16x32_bf16 v[216:219], a[128:131], v[88:91], v[216:219]\n"
    "v_cvt_pk_bf16_f32 v11, v102, v103\n"
    "v_mfma_f32_16x16x32_bf16 v[100:103], a[112:115], a[48:51], v[116:119]\n"
    "s_cmp_eq_u32 s60, 6\n"
    "v_exp_f32 v96, v96\n"
    "v_mfma_f32_16x16x32_bf16 v[100:103], a[116:119], a[52:55], v[100:103]\n"
    "s_cselect_b32 s42, 0, s42\n"
    "v_mfma_f32_16x16x32_bf16 v[100:103], a[120:123], a[56:59], v[100:103]\n"
    "s_waitcnt vmcnt(8) lgkmcnt(1)\n"
    "v_exp_f32 v97, v97\n"
    "v_mfma_f32_16x16x32_bf16 v[100:103], a[124:127], a[60:63], v[100:103]\n"
    "s_barrier\n"
    "v_mfma_f32_16x16x32_bf16 v[220:223], a[136:139], v[48:51], v[220:223]\n"
    "v_exp_f32 v98, v98\n"
    "v_mfma_f32_16x16x32_bf16 v[220:223], a[140:143], v[52:55], v[220:223]\n"
    "ds_read_b128 a[96:99], v128 offset:8192\n"
    "v_cvt_pk_bf16_f32 v18, v96, v97\n"
    "v_mfma_f32_16x16x32_bf16 v[224:227], a[140:143], v[60:63], v[224:227]\n"
    "v_exp_f32 v99, v99\n"
    "v_mfma_f32_16x16x32_bf16 v[224:227], a[136:139], v[56:59], v[224:227]\n"
    "ds_read_b128 a[100:103], v128 offset:8448\n"
    "v_cvt_pk_bf16_f32 v19, v98, v99\n"
    "v_mfma_f32_16x16x32_bf16 v[96:99], a[112:115], a[64:67], v[120:123]\n"
    "v_exp_f32 v100, v100\n"
    "v_mfma_f32_16x16x32_bf16 v[96:99], a[116:119], a[68:71], v[96:99]\n"
    "ds_read_b64_tr_b16 a[128:129], v129 offset:49664\n"
    "ds_read_b64_tr_b16 a[130:131], v129 offset:53760\n"
    "v_mfma_f32_16x16x32_bf16 v[96:99], a[120:123], a[72:75], v[96:99]\n"
    "s_mov_b32 m0, s80\n"
    "v_exp_f32 v101, v101\n"
    "v_mfma_f32_16x16x32_bf16 v[96:99], a[124:127], a[76:79], v[96:99]\n"
    "buffer_load_dwordx4 v131, s[40:43], s56 offen lds\n"
    "v_mfma_f32_16x16x32_bf16 v[228:231], a[136:139], v[64:67], v[228:231]\n"
    "v_exp_f32 v102, v102\n"
    "v_mfma_f32_16x16x32_bf16 v[228:231], a[140:143], v[68:71], v[228:231]\n"
    "ds_read_b128 a[104:107], v128 offset:8704\n"
    "v_cvt_pk_bf16_f32 v26, v100, v101\n"
    "v_mfma_f32_16x16x32_bf16 v[232:235], a[140:143], v[76:79], v[232:235]\n"
    "v_exp_f32 v103, v103\n"
    "v_mfma_f32_16x16x32_bf16 v[232:235], a[136:139], v[72:75], v[232:235]\n"
    "ds_read_b128 a[108:111], v128 offset:8960\n"
    "v_cvt_pk_bf16_f32 v27, v102, v103\n"
    "v_mfma_f32_16x16x32_bf16 v[100:103], a[112:115], a[80:83], v[124:127]\n"
    "v_exp_f32 v96, v96\n"
    "v_mfma_f32_16x16x32_bf16 v[100:103], a[116:119], a[84:87], v[100:103]\n"
    "ds_read_b64_tr_b16 a[132:133], v129 offset:57856\n"
    "ds_read_b64_tr_b16 a[134:135], v129 offset:61952\n"
    "v_mfma_f32_16x16x32_bf16 v[100:103], a[120:123], a[88:91], v[100:103]\n"
    "v_exp_f32 v97, v97\n"
    "v_mfma_f32_16x16x32_bf16 v[100:103], a[124:127], a[92:95], v[100:103]\n"
    "buffer_load_dwordx4 v131, s[40:43], s56 offen offset:2048 lds\n"
    "v_mfma_f32_16x16x32_bf16 v[236:239], a[136:139], v[80:83], v[236:239]\n"
    "v_exp_f32 v98, v98\n"
    "v_mfma_f32_16x16x32_bf16 v[236:239], a[140:143], v[84:87], v[236:239]\n"
    "v_cvt_pk_bf16_f32 v34, v96, v97\n"
    "v_mfma_f32_16x16x32_bf16 v[240:243], a[140:143], v[92:95], v[240:243]\n"
    "v_exp_f32 v99, v99\n"
    "v_mfma_f32_16x16x32_bf16 v[240:243], a[136:139], v[88:91], v[240:243]\n"
    "s_waitcnt lgkmcnt(2)\n"
    "v_cvt_pk_bf16_f32 v35, v98, v99\n"
    "v_smfmac_f32_16x16x64_bf16 a[152:155], a[168:171], v[64:71], v133\n"
    "s_waitcnt lgkmcnt(10)\n"
    "buffer_store_dwordx2 v[136:137], v140, s[52:55], s59 offen offset:0 sc0 sc1 nt\n"
    "v_mfma_f32_16x16x32_bf16 v[96:99], a[96:99], a[0:3], v[104:107]\n"
    "v_exp_f32 v100, v100\n"
    "v_mfma_f32_16x16x32_bf16 v[96:99], a[100:103], a[4:7], v[96:99]\n"
    "v_pk_maximum3_f16 v141, v141, v136, v137\n"
    "v_mfma_f32_16x16x32_bf16 v[96:99], a[104:107], a[8:11], v[96:99]\n"
    "v_exp_f32 v101, v101\n"
    "v_mfma_f32_16x16x32_bf16 v[96:99], a[108:111], a[12:15], v[96:99]\n"
    "s_waitcnt lgkmcnt(0)\n"
    "v_mfma_f32_16x16x32_bf16 v[244:247], a[128:131], v[48:51], v[244:247]\n"
    "v_exp_f32 v102, v102\n"
    "v_mfma_f32_16x16x32_bf16 v[244:247], a[132:135], v[52:55], v[244:247]\n"
    "ds_read_b128 a[112:115], v128 offset:12288\n"
    "v_cvt_pk_bf16_f32 v42, v100, v101\n"
    "v_mfma_f32_16x16x32_bf16 v[248:251], a[132:135], v[60:63], v[248:251]\n"
    "v_exp_f32 v103, v103\n"
    "v_mfma_f32_16x16x32_bf16 v[248:251], a[128:131], v[56:59], v[248:251]\n"
    "ds_read_b128 a[116:119], v128 offset:12544\n"
    "v_cvt_pk_bf16_f32 v43, v102, v103\n"
    "v_mfma_f32_16x16x32_bf16 v[100:103], a[96:99], a[16:19], v[108:111]\n"
    "v_exp_f32 v96, v96\n"
    "v_mfma_f32_16x16x32_bf16 v[100:103], a[100:103], a[20:23], v[100:103]\n"
    "ds_read_b64_tr_b16 a[136:137], v130 offset:49664\n"
    "v_mfma_f32_16x16x32_bf16 v[100:103], a[104:107], a[24:27], v[100:103]\n"
    "v_exp_f32 v97, v97\n"
    "v_mfma_f32_16x16x32_bf16 v[100:103], a[108:111], a[28:31], v[100:103]\n"
    "ds_read_b64_tr_b16 a[138:139], v130 offset:53760\n"
    "v_mfma_f32_16x16x32_bf16 v[252:255], a[128:131], v[64:67], v[252:255]\n"
    "v_exp_f32 v98, v98\n"
    "v_mfma_f32_16x16x32_bf16 v[252:255], a[132:135], v[68:71], v[252:255]\n"
    "ds_read_b64 v[136:137], v134 offset:256\n"
    "v_cvt_pk_bf16_f32 v4, v96, v97\n"
    "v_mfma_f32_16x16x32_bf16 a[172:175], a[132:135], v[76:79], a[172:175]\n"
    "v_exp_f32 v99, v99\n"
    "v_mfma_f32_16x16x32_bf16 a[172:175], a[128:131], v[72:75], a[172:175]\n"
    "v_cvt_pk_bf16_f32 v5, v98, v99\n"
    "v_mfma_f32_16x16x32_bf16 v[96:99], a[96:99], a[32:35], v[112:115]\n"
    "v_exp_f32 v100, v100\n"
    "v_mfma_f32_16x16x32_bf16 v[96:99], a[100:103], a[36:39], v[96:99]\n"
    "ds_read_b64_tr_b16 a[140:141], v130 offset:57856\n"
    "v_mfma_f32_16x16x32_bf16 v[96:99], a[104:107], a[40:43], v[96:99]\n"
    "v_exp_f32 v101, v101\n"
    "v_mfma_f32_16x16x32_bf16 v[96:99], a[108:111], a[44:47], v[96:99]\n"
    "ds_read_b64_tr_b16 a[142:143], v130 offset:61952\n"
    "v_mfma_f32_16x16x32_bf16 a[176:179], a[128:131], v[80:83], a[176:179]\n"
    "v_exp_f32 v102, v102\n"
    "v_mfma_f32_16x16x32_bf16 a[176:179], a[132:135], v[84:87], a[176:179]\n"
    "v_cvt_pk_bf16_f32 v12, v100, v101\n"
    "v_mfma_f32_16x16x32_bf16 a[180:183], a[132:135], v[92:95], a[180:183]\n"
    "v_exp_f32 v103, v103\n"
    "v_mfma_f32_16x16x32_bf16 a[180:183], a[128:131], v[88:91], a[180:183]\n"
    "v_cvt_pk_bf16_f32 v13, v102, v103\n"
    "v_mfma_f32_16x16x32_bf16 v[100:103], a[96:99], a[48:51], v[116:119]\n"
    "v_exp_f32 v96, v96\n"
    "v_mfma_f32_16x16x32_bf16 v[100:103], a[100:103], a[52:55], v[100:103]\n"
    "s_waitcnt lgkmcnt(15)\n"
    "buffer_store_dwordx2 v[138:139], v140, s[52:55], s59 offen offset:1024 sc0 sc1 nt\n"
    "v_mfma_f32_16x16x32_bf16 v[100:103], a[104:107], a[56:59], v[100:103]\n"
    "v_exp_f32 v97, v97\n"
    "v_mfma_f32_16x16x32_bf16 v[100:103], a[108:111], a[60:63], v[100:103]\n"
    "s_waitcnt lgkmcnt(0)\n"
    "v_pk_maximum3_f16 v141, v141, v138, v139\n"
    "v_mfma_f32_16x16x32_bf16 a[184:187], a[136:139], v[48:51], a[184:187]\n"
    "v_exp_f32 v98, v98\n"
    "v_mfma_f32_16x16x32_bf16 a[184:187], a[140:143], v[52:55], a[184:187]\n"
    "ds_read_b128 a[120:123], v128 offset:12800\n"
    "v_cvt_pk_bf16_f32 v20, v96, v97\n"
    "v_mfma_f32_16x16x32_bf16 a[188:191], a[140:143], v[60:63], a[188:191]\n"
    "v_exp_f32 v99, v99\n"
    "v_mfma_f32_16x16x32_bf16 a[188:191], a[136:139], v[56:59], a[188:191]\n"
    "ds_read_b128 a[124:127], v128 offset:13056\n"
    "v_cvt_pk_bf16_f32 v21, v98, v99\n"
    "v_smfmac_f32_16x16x64_bf16 a[156:159], a[168:171], v[72:79], v133\n"
    "s_waitcnt lgkmcnt(4)\n"
    "buffer_store_dwordx2 v[136:137], v140, s[52:55], s59 offen offset:2048 sc0 sc1 nt\n"
    "v_mfma_f32_16x16x32_bf16 v[96:99], a[96:99], a[64:67], v[120:123]\n"
    "v_exp_f32 v100, v100\n"
    "v_mfma_f32_16x16x32_bf16 v[96:99], a[100:103], a[68:71], v[96:99]\n"
    "ds_read_b64_tr_b16 a[128:129], v129 offset:49920\n"
    "v_pk_maximum3_f16 v141, v141, v136, v137\n"
    "v_mfma_f32_16x16x32_bf16 v[96:99], a[104:107], a[72:75], v[96:99]\n"
    "v_exp_f32 v101, v101\n"
    "v_mfma_f32_16x16x32_bf16 v[96:99], a[108:111], a[76:79], v[96:99]\n"
    "ds_read_b64_tr_b16 a[130:131], v129 offset:54016\n"
    "v_mfma_f32_16x16x32_bf16 a[192:195], a[136:139], v[64:67], a[192:195]\n"
    "v_exp_f32 v102, v102\n"
    "v_mfma_f32_16x16x32_bf16 a[192:195], a[140:143], v[68:71], a[192:195]\n"
    "ds_read_b64 v[138:139], v142 offset:256\n"
    "v_cvt_pk_bf16_f32 v28, v100, v101\n"
    "v_mfma_f32_16x16x32_bf16 a[196:199], a[140:143], v[76:79], a[196:199]\n"
    "v_exp_f32 v103, v103\n"
    "v_mfma_f32_16x16x32_bf16 a[196:199], a[136:139], v[72:75], a[196:199]\n"
    "v_cvt_pk_bf16_f32 v29, v102, v103\n"
    "v_mfma_f32_16x16x32_bf16 v[100:103], a[96:99], a[80:83], v[124:127]\n"
    "v_exp_f32 v96, v96\n"
    "v_mfma_f32_16x16x32_bf16 v[100:103], a[100:103], a[84:87], v[100:103]\n"
    "ds_read_b64_tr_b16 a[132:133], v129 offset:58112\n"
    "v_mfma_f32_16x16x32_bf16 v[100:103], a[104:107], a[88:91], v[100:103]\n"
    "v_exp_f32 v97, v97\n"
    "v_mfma_f32_16x16x32_bf16 v[100:103], a[108:111], a[92:95], v[100:103]\n"
    "ds_read_b64_tr_b16 a[134:135], v129 offset:62208\n"
    "v_mfma_f32_16x16x32_bf16 a[200:203], a[136:139], v[80:83], a[200:203]\n"
    "v_exp_f32 v98, v98\n"
    "v_mfma_f32_16x16x32_bf16 a[200:203], a[140:143], v[84:87], a[200:203]\n"
    "v_cvt_pk_bf16_f32 v36, v96, v97\n"
    "v_mfma_f32_16x16x32_bf16 a[204:207], a[140:143], v[92:95], a[204:207]\n"
    "v_exp_f32 v99, v99\n"
    "v_mfma_f32_16x16x32_bf16 a[204:207], a[136:139], v[88:91], a[204:207]\n"
    "s_waitcnt lgkmcnt(5)\n"
    "v_cvt_pk_bf16_f32 v37, v98, v99\n"
    "v_mfma_f32_16x16x32_bf16 v[96:99], a[112:115], a[0:3], v[104:107]\n"
    "v_exp_f32 v100, v100\n"
    "v_mfma_f32_16x16x32_bf16 v[96:99], a[116:119], a[4:7], v[96:99]\n"
    "s_waitcnt lgkmcnt(2)\n"
    "buffer_store_dwordx2 v[138:139], v140, s[52:55], s59 offen offset:3072 sc0 sc1 nt\n"
    "v_mfma_f32_16x16x32_bf16 v[96:99], a[120:123], a[8:11], v[96:99]\n"
    "v_exp_f32 v101, v101\n"
    "v_mfma_f32_16x16x32_bf16 v[96:99], a[124:127], a[12:15], v[96:99]\n"
    "s_waitcnt lgkmcnt(0)\n"
    "v_pk_maximum3_f16 v141, v141, v138, v139\n"
    "v_mfma_f32_16x16x32_bf16 a[208:211], a[128:131], v[48:51], a[208:211]\n"
    "v_exp_f32 v102, v102\n"
    "v_mfma_f32_16x16x32_bf16 a[208:211], a[132:135], v[52:55], a[208:211]\n"
    "v_cvt_pk_bf16_f32 v44, v100, v101\n"
    "v_mfma_f32_16x16x32_bf16 a[212:215], a[132:135], v[60:63], a[212:215]\n"
    "v_exp_f32 v103, v103\n"
    "v_mfma_f32_16x16x32_bf16 a[212:215], a[128:131], v[56:59], a[212:215]\n"
    "v_cvt_pk_bf16_f32 v45, v102, v103\n"
    "v_mfma_f32_16x16x32_bf16 v[100:103], a[112:115], a[16:19], v[108:111]\n"
    "v_exp_f32 v96, v96\n"
    "v_mfma_f32_16x16x32_bf16 v[100:103], a[116:119], a[20:23], v[100:103]\n"
    "ds_read_b64_tr_b16 a[136:137], v130 offset:49920\n"
    "v_mfma_f32_16x16x32_bf16 v[100:103], a[120:123], a[24:27], v[100:103]\n"
    "v_exp_f32 v97, v97\n"
    "v_mfma_f32_16x16x32_bf16 v[100:103], a[124:127], a[28:31], v[100:103]\n"
    "ds_read_b64_tr_b16 a[138:139], v130 offset:54016\n"
    "v_mfma_f32_16x16x32_bf16 a[216:219], a[128:131], v[64:67], a[216:219]\n"
    "v_exp_f32 v98, v98\n"
    "v_mfma_f32_16x16x32_bf16 a[216:219], a[132:135], v[68:71], a[216:219]\n"
    "v_cvt_pk_bf16_f32 v6, v96, v97\n"
    "v_mfma_f32_16x16x32_bf16 a[220:223], a[132:135], v[76:79], a[220:223]\n"
    "v_exp_f32 v99, v99\n"
    "v_mfma_f32_16x16x32_bf16 a[220:223], a[128:131], v[72:75], a[220:223]\n"
    "v_cvt_pk_bf16_f32 v7, v98, v99\n"
    "v_smfmac_f32_16x16x64_bf16 a[160:163], a[168:171], v[80:87], v133\n"
    "v_mfma_f32_16x16x32_bf16 v[96:99], a[112:115], a[32:35], v[112:115]\n"
    "v_exp_f32 v100, v100\n"
    "v_mfma_f32_16x16x32_bf16 v[96:99], a[116:119], a[36:39], v[96:99]\n"
    "ds_read_b64_tr_b16 a[140:141], v130 offset:58112\n"
    "v_mfma_f32_16x16x32_bf16 v[96:99], a[120:123], a[40:43], v[96:99]\n"
    "v_exp_f32 v101, v101\n"
    "v_mfma_f32_16x16x32_bf16 v[96:99], a[124:127], a[44:47], v[96:99]\n"
    "ds_read_b64_tr_b16 a[142:143], v130 offset:62208\n"
    "v_mfma_f32_16x16x32_bf16 a[224:227], a[128:131], v[80:83], a[224:227]\n"
    "v_exp_f32 v102, v102\n"
    "v_mfma_f32_16x16x32_bf16 a[224:227], a[132:135], v[84:87], a[224:227]\n"
    "v_cvt_pk_bf16_f32 v14, v100, v101\n"
    "v_mfma_f32_16x16x32_bf16 a[228:231], a[132:135], v[92:95], a[228:231]\n"
    "v_exp_f32 v103, v103\n"
    "v_mfma_f32_16x16x32_bf16 a[228:231], a[128:131], v[88:91], a[228:231]\n"
    "v_cvt_pk_bf16_f32 v15, v102, v103\n"
    "v_mfma_f32_16x16x32_bf16 v[100:103], a[112:115], a[48:51], v[116:119]\n"
    "v_exp_f32 v96, v96\n"
    "v_mfma_f32_16x16x32_bf16 v[100:103], a[116:119], a[52:55], v[100:103]\n"
    "v_mfma_f32_16x16x32_bf16 v[100:103], a[120:123], a[56:59], v[100:103]\n"
    "s_waitcnt vmcnt(8) lgkmcnt(0)\n"
    "v_exp_f32 v97, v97\n"
    "v_mfma_f32_16x16x32_bf16 v[100:103], a[124:127], a[60:63], v[100:103]\n"
    "s_barrier\n"
    "v_mfma_f32_16x16x32_bf16 a[232:235], a[136:139], v[48:51], a[232:235]\n"
    "v_exp_f32 v98, v98\n"
    "v_mfma_f32_16x16x32_bf16 a[232:235], a[140:143], v[52:55], a[232:235]\n"
    "ds_read_b128 a[96:99], v128 offset:16384\n"
    "v_cvt_pk_bf16_f32 v22, v96, v97\n"
    "v_mfma_f32_16x16x32_bf16 a[236:239], a[140:143], v[60:63], a[236:239]\n"
    "v_exp_f32 v99, v99\n"
    "v_mfma_f32_16x16x32_bf16 a[236:239], a[136:139], v[56:59], a[236:239]\n"
    "ds_read_b128 a[100:103], v128 offset:16640\n"
    "v_cvt_pk_bf16_f32 v23, v98, v99\n"
    "v_mfma_f32_16x16x32_bf16 v[96:99], a[112:115], a[64:67], v[120:123]\n"
    "v_exp_f32 v100, v100\n"
    "v_mfma_f32_16x16x32_bf16 v[96:99], a[116:119], a[68:71], v[96:99]\n"
    "ds_read_b64_tr_b16 a[128:129], v129 offset:32768\n"
    "ds_read_b64_tr_b16 a[130:131], v129 offset:36864\n"
    "v_mfma_f32_16x16x32_bf16 v[96:99], a[120:123], a[72:75], v[96:99]\n"
    "s_mov_b32 m0, s86\n"
    "v_exp_f32 v101, v101\n"
    "v_mfma_f32_16x16x32_bf16 v[96:99], a[124:127], a[76:79], v[96:99]\n"
    "buffer_load_dwordx4 v131, s[44:47], s57 offen nt lds\n"
    "v_mfma_f32_16x16x32_bf16 a[240:243], a[136:139], v[64:67], a[240:243]\n"
    "v_exp_f32 v102, v102\n"
    "v_mfma_f32_16x16x32_bf16 a[240:243], a[140:143], v[68:71], a[240:243]\n"
    "ds_read_b128 a[104:107], v128 offset:16896\n"
    "v_cvt_pk_bf16_f32 v30, v100, v101\n"
    "v_mfma_f32_16x16x32_bf16 a[244:247], a[140:143], v[76:79], a[244:247]\n"
    "v_exp_f32 v103, v103\n"
    "v_mfma_f32_16x16x32_bf16 a[244:247], a[136:139], v[72:75], a[244:247]\n"
    "ds_read_b128 a[108:111], v128 offset:17152\n"
    "v_cvt_pk_bf16_f32 v31, v102, v103\n"
    "v_mfma_f32_16x16x32_bf16 v[100:103], a[112:115], a[80:83], v[124:127]\n"
    "v_exp_f32 v96, v96\n"
    "v_mfma_f32_16x16x32_bf16 v[100:103], a[116:119], a[84:87], v[100:103]\n"
    "ds_read_b64_tr_b16 a[132:133], v129 offset:40960\n"
    "ds_read_b64_tr_b16 a[134:135], v129 offset:45056\n"
    "v_mfma_f32_16x16x32_bf16 v[100:103], a[120:123], a[88:91], v[100:103]\n"
    "v_exp_f32 v97, v97\n"
    "v_mfma_f32_16x16x32_bf16 v[100:103], a[124:127], a[92:95], v[100:103]\n"
    "buffer_load_dwordx4 v131, s[44:47], s57 offen offset:2048 nt lds\n"
    "v_mfma_f32_16x16x32_bf16 a[248:251], a[136:139], v[80:83], a[248:251]\n"
    "v_exp_f32 v98, v98\n"
    "v_mfma_f32_16x16x32_bf16 a[248:251], a[140:143], v[84:87], a[248:251]\n"
    "v_cvt_pk_bf16_f32 v38, v96, v97\n"
    "v_mfma_f32_16x16x32_bf16 a[252:255], a[140:143], v[92:95], a[252:255]\n"
    "v_exp_f32 v99, v99\n"
    "v_mfma_f32_16x16x32_bf16 a[252:255], a[136:139], v[88:91], a[252:255]\n"
    "s_waitcnt lgkmcnt(2)\n"
    "v_cvt_pk_bf16_f32 v39, v98, v99\n"
    "v_smfmac_f32_16x16x64_bf16 a[164:167], a[168:171], v[88:95], v133\n"
    "v_mfma_f32_16x16x32_bf16 v[96:99], a[96:99], a[0:3], v[104:107]\n"
    "s_mov_b32 m0, s87\n"
    "v_exp_f32 v100, v100\n"
    "v_mfma_f32_16x16x32_bf16 v[96:99], a[100:103], a[4:7], v[96:99]\n"
    "buffer_load_dwordx4 v131, s[44:47], s70 offen nt lds\n"
    "v_mfma_f32_16x16x32_bf16 v[96:99], a[104:107], a[8:11], v[96:99]\n"
    "v_exp_f32 v101, v101\n"
    "v_mfma_f32_16x16x32_bf16 v[96:99], a[108:111], a[12:15], v[96:99]\n"
    "s_waitcnt lgkmcnt(0)\n"
    "v_mfma_f32_16x16x32_bf16 v[148:151], a[128:131], v[0:3], v[148:151]\n"
    "v_exp_f32 v102, v102\n"
    "v_mfma_f32_16x16x32_bf16 v[148:151], a[132:135], v[4:7], v[148:151]\n"
    "ds_read_b128 a[112:115], v128 offset:20480\n"
    "v_cvt_pk_bf16_f32 v46, v100, v101\n"
    "v_mfma_f32_16x16x32_bf16 v[152:155], a[132:135], v[12:15], v[152:155]\n"
    "v_exp_f32 v103, v103\n"
    "v_mfma_f32_16x16x32_bf16 v[152:155], a[128:131], v[8:11], v[152:155]\n"
    "ds_read_b128 a[116:119], v128 offset:20736\n"
    "v_cvt_pk_bf16_f32 v47, v102, v103\n"
    "v_mfma_f32_16x16x32_bf16 v[100:103], a[96:99], a[16:19], v[108:111]\n"
    "v_exp_f32 v96, v96\n"
    "v_mfma_f32_16x16x32_bf16 v[100:103], a[100:103], a[20:23], v[100:103]\n"
    "ds_read_b64_tr_b16 a[136:137], v130 offset:32768\n"
    "ds_read_b64_tr_b16 a[138:139], v130 offset:36864\n"
    "v_mfma_f32_16x16x32_bf16 v[100:103], a[104:107], a[24:27], v[100:103]\n"
    "v_exp_f32 v97, v97\n"
    "v_mfma_f32_16x16x32_bf16 v[100:103], a[108:111], a[28:31], v[100:103]\n"
    "buffer_load_dwordx4 v131, s[44:47], s70 offen offset:2048 nt lds\n"
    "v_mfma_f32_16x16x32_bf16 v[156:159], a[128:131], v[16:19], v[156:159]\n"
    "v_exp_f32 v98, v98\n"
    "v_mfma_f32_16x16x32_bf16 v[156:159], a[132:135], v[20:23], v[156:159]\n"
    "v_cvt_pk_bf16_f32 v48, v96, v97\n"
    "v_mfma_f32_16x16x32_bf16 v[160:163], a[132:135], v[28:31], v[160:163]\n"
    "v_exp_f32 v99, v99\n"
    "v_mfma_f32_16x16x32_bf16 v[160:163], a[128:131], v[24:27], v[160:163]\n"
    "v_cvt_pk_bf16_f32 v49, v98, v99\n"
    "v_mfma_f32_16x16x32_bf16 v[96:99], a[96:99], a[32:35], v[112:115]\n"
    "v_exp_f32 v100, v100\n"
    "v_mfma_f32_16x16x32_bf16 v[96:99], a[100:103], a[36:39], v[96:99]\n"
    "ds_read_b64_tr_b16 a[140:141], v130 offset:40960\n"
    "ds_read_b64_tr_b16 a[142:143], v130 offset:45056\n"
    "v_mfma_f32_16x16x32_bf16 v[96:99], a[104:107], a[40:43], v[96:99]\n"
    "s_mov_b32 m0, s81\n"
    "v_exp_f32 v101, v101\n"
    "v_mfma_f32_16x16x32_bf16 v[96:99], a[108:111], a[44:47], v[96:99]\n"
    "buffer_load_dwordx4 v131, s[40:43], s94 offen lds\n"
    "v_mfma_f32_16x16x32_bf16 v[164:167], a[128:131], v[32:35], v[164:167]\n"
    "v_exp_f32 v102, v102\n"
    "v_mfma_f32_16x16x32_bf16 v[164:167], a[132:135], v[36:39], v[164:167]\n"
    "v_cvt_pk_bf16_f32 v56, v100, v101\n"
    "v_mfma_f32_16x16x32_bf16 v[168:171], a[132:135], v[44:47], v[168:171]\n"
    "v_exp_f32 v103, v103\n"
    "v_mfma_f32_16x16x32_bf16 v[168:171], a[128:131], v[40:43], v[168:171]\n"
    "v_cvt_pk_bf16_f32 v57, v102, v103\n"
    "v_mfma_f32_16x16x32_bf16 v[100:103], a[96:99], a[48:51], v[116:119]\n"
    "v_exp_f32 v96, v96\n"
    "v_mfma_f32_16x16x32_bf16 v[100:103], a[100:103], a[52:55], v[100:103]\n"
    "buffer_load_dwordx4 v131, s[40:43], s94 offen offset:2048 lds\n"
    "v_mfma_f32_16x16x32_bf16 v[100:103], a[104:107], a[56:59], v[100:103]\n"
    "v_exp_f32 v97, v97\n"
    "v_mfma_f32_16x16x32_bf16 v[100:103], a[108:111], a[60:63], v[100:103]\n"
    "s_waitcnt lgkmcnt(0)\n"
    "v_mfma_f32_16x16x32_bf16 v[172:175], a[136:139], v[0:3], v[172:175]\n"
    "v_exp_f32 v98, v98\n"
    "v_mfma_f32_16x16x32_bf16 v[172:175], a[140:143], v[4:7], v[172:175]\n"
    "ds_read_b128 a[120:123], v128 offset:20992\n"
    "v_cvt_pk_bf16_f32 v64, v96, v97\n"
    "v_mfma_f32_16x16x32_bf16 v[176:179], a[140:143], v[12:15], v[176:179]\n"
    "v_exp_f32 v99, v99\n"
    "v_mfma_f32_16x16x32_bf16 v[176:179], a[136:139], v[8:11], v[176:179]\n"
    "ds_read_b128 a[124:127], v128 offset:21248\n"
    "v_cvt_pk_bf16_f32 v65, v98, v99\n"
    "v_smfmac_f32_16x16x64_bf16 a[144:147], a[168:171], v[0:7], v133\n"
    "v_mfma_f32_16x16x32_bf16 v[96:99], a[96:99], a[64:67], v[120:123]\n"
    "v_exp_f32 v100, v100\n"
    "v_mfma_f32_16x16x32_bf16 v[96:99], a[100:103], a[68:71], v[96:99]\n"
    "ds_read_b64_tr_b16 a[128:129], v129 offset:33024\n"
    "v_mfma_f32_16x16x32_bf16 v[96:99], a[104:107], a[72:75], v[96:99]\n"
    "v_exp_f32 v101, v101\n"
    "v_mfma_f32_16x16x32_bf16 v[96:99], a[108:111], a[76:79], v[96:99]\n"
    "ds_read_b64_tr_b16 a[130:131], v129 offset:37120\n"
    "v_mfma_f32_16x16x32_bf16 v[180:183], a[136:139], v[16:19], v[180:183]\n"
    "v_exp_f32 v102, v102\n"
    "v_mfma_f32_16x16x32_bf16 v[180:183], a[140:143], v[20:23], v[180:183]\n"
    "v_cvt_pk_bf16_f32 v72, v100, v101\n"
    "v_mfma_f32_16x16x32_bf16 v[184:187], a[140:143], v[28:31], v[184:187]\n"
    "v_exp_f32 v103, v103\n"
    "v_mfma_f32_16x16x32_bf16 v[184:187], a[136:139], v[24:27], v[184:187]\n"
    "v_cvt_pk_bf16_f32 v73, v102, v103\n"
    "v_mfma_f32_16x16x32_bf16 v[100:103], a[96:99], a[80:83], v[124:127]\n"
    "v_exp_f32 v96, v96\n"
    "v_mfma_f32_16x16x32_bf16 v[100:103], a[100:103], a[84:87], v[100:103]\n"
    "ds_read_b64_tr_b16 a[132:133], v129 offset:41216\n"
    "v_mfma_f32_16x16x32_bf16 v[100:103], a[104:107], a[88:91], v[100:103]\n"
    "v_exp_f32 v97, v97\n"
    "v_mfma_f32_16x16x32_bf16 v[100:103], a[108:111], a[92:95], v[100:103]\n"
    "ds_read_b64_tr_b16 a[134:135], v129 offset:45312\n"
    "v_mfma_f32_16x16x32_bf16 v[188:191], a[136:139], v[32:35], v[188:191]\n"
    "v_exp_f32 v98, v98\n"
    "v_mfma_f32_16x16x32_bf16 v[188:191], a[140:143], v[36:39], v[188:191]\n"
    "v_cvt_pk_bf16_f32 v80, v96, v97\n"
    "v_mfma_f32_16x16x32_bf16 v[192:195], a[140:143], v[44:47], v[192:195]\n"
    "v_exp_f32 v99, v99\n"
    "v_mfma_f32_16x16x32_bf16 v[192:195], a[136:139], v[40:43], v[192:195]\n"
    "s_waitcnt lgkmcnt(4)\n"
    "v_cvt_pk_bf16_f32 v81, v98, v99\n"
    "v_mfma_f32_16x16x32_bf16 v[96:99], a[112:115], a[0:3], v[104:107]\n"
    "s_add_u32 s56, s56, 16384\n"
    "v_exp_f32 v100, v100\n"
    "v_mfma_f32_16x16x32_bf16 v[96:99], a[116:119], a[4:7], v[96:99]\n"
    "s_and_b32 s56, s56, 262143\n"
    "v_mfma_f32_16x16x32_bf16 v[96:99], a[120:123], a[8:11], v[96:99]\n"
    "s_or_b32 s56, s56, s61\n"
    "v_exp_f32 v101, v101\n"
    "v_mfma_f32_16x16x32_bf16 v[96:99], a[124:127], a[12:15], v[96:99]\n"
    "s_waitcnt lgkmcnt(0)\n"
    "v_mfma_f32_16x16x32_bf16 v[196:199], a[128:131], v[0:3], v[196:199]\n"
    "s_add_u32 s94, s56, 8192\n"
    "v_exp_f32 v102, v102\n"
    "v_mfma_f32_16x16x32_bf16 v[196:199], a[132:135], v[4:7], v[196:199]\n"
    "s_add_u32 s57, s57, 16384\n"
    "v_cvt_pk_bf16_f32 v88, v100, v101\n"
    "v_mfma_f32_16x16x32_bf16 v[200:203], a[132:135], v[12:15], v[200:203]\n"
    "s_and_b32 s57, s57, 262143\n"
    "v_exp_f32 v103, v103\n"
    "v_mfma_f32_16x16x32_bf16 v[200:203], a[128:131], v[8:11], v[200:203]\n"
    "s_or_b32 s57, s57, s96\n"
    "v_cvt_pk_bf16_f32 v89, v102, v103\n"
    "v_mfma_f32_16x16x32_bf16 v[100:103], a[112:115], a[16:19], v[108:111]\n"
    "s_add_u32 s70, s57, 8192\n"
    "v_exp_f32 v96, v96\n"
    "v_mfma_f32_16x16x32_bf16 v[100:103], a[116:119], a[20:23], v[100:103]\n"
    "ds_read_b64_tr_b16 a[136:137], v130 offset:33024\n"
    "v_mfma_f32_16x16x32_bf16 v[100:103], a[120:123], a[24:27], v[100:103]\n"
    "v_exp_f32 v97, v97\n"
    "v_mfma_f32_16x16x32_bf16 v[100:103], a[124:127], a[28:31], v[100:103]\n"
    "ds_read_b64_tr_b16 a[138:139], v130 offset:37120\n"
    "v_mfma_f32_16x16x32_bf16 v[204:207], a[128:131], v[16:19], v[204:207]\n"
    "v_exp_f32 v98, v98\n"
    "v_mfma_f32_16x16x32_bf16 v[204:207], a[132:135], v[20:23], v[204:207]\n"
    "v_cvt_pk_bf16_f32 v50, v96, v97\n"
    "v_mfma_f32_16x16x32_bf16 v[208:211], a[132:135], v[28:31], v[208:211]\n"
    "v_exp_f32 v99, v99\n"
    "v_mfma_f32_16x16x32_bf16 v[208:211], a[128:131], v[24:27], v[208:211]\n"
    "v_cvt_pk_bf16_f32 v51, v98, v99\n"
    "v_smfmac_f32_16x16x64_bf16 a[148:151], a[168:171], v[8:15], v133\n"
    "v_mfma_f32_16x16x32_bf16 v[96:99], a[112:115], a[32:35], v[112:115]\n"
    "v_exp_f32 v100, v100\n"
    "v_mfma_f32_16x16x32_bf16 v[96:99], a[116:119], a[36:39], v[96:99]\n"
    "ds_read_b64_tr_b16 a[140:141], v130 offset:41216\n"
    "ds_read_b64 v[136:137], v134 offset:2048\n"
    "v_mfma_f32_16x16x32_bf16 v[96:99], a[120:123], a[40:43], v[96:99]\n"
    "v_exp_f32 v101, v101\n"
    "v_mfma_f32_16x16x32_bf16 v[96:99], a[124:127], a[44:47], v[96:99]\n"
    "ds_read_b64_tr_b16 a[142:143], v130 offset:45312\n"
    "ds_read_b64 v[138:139], v142 offset:2048\n"
    "v_mfma_f32_16x16x32_bf16 v[212:215], a[128:131], v[32:35], v[212:215]\n"
    "v_exp_f32 v102, v102\n"
    "v_mfma_f32_16x16x32_bf16 v[212:215], a[132:135], v[36:39], v[212:215]\n"
    "v_cvt_pk_bf16_f32 v58, v100, v101\n"
    "v_mfma_f32_16x16x32_bf16 v[216:219], a[132:135], v[44:47], v[216:219]\n"
    "v_exp_f32 v103, v103\n"
    "v_mfma_f32_16x16x32_bf16 v[216:219], a[128:131], v[40:43], v[216:219]\n"
    "v_cvt_pk_bf16_f32 v59, v102, v103\n"
    "v_mfma_f32_16x16x32_bf16 v[100:103], a[112:115], a[48:51], v[116:119]\n"
    "s_cmp_eq_u32 s60, 6\n"
    "v_exp_f32 v96, v96\n"
    "v_mfma_f32_16x16x32_bf16 v[100:103], a[116:119], a[52:55], v[100:103]\n"
    "s_cselect_b32 s46, 0, s46\n"
    "v_mfma_f32_16x16x32_bf16 v[100:103], a[120:123], a[56:59], v[100:103]\n"
    "s_waitcnt vmcnt(12) lgkmcnt(1)\n"
    "v_exp_f32 v97, v97\n"
    "v_mfma_f32_16x16x32_bf16 v[100:103], a[124:127], a[60:63], v[100:103]\n"
    "s_barrier\n"
    "v_mfma_f32_16x16x32_bf16 v[220:223], a[136:139], v[0:3], v[220:223]\n"
    "v_exp_f32 v98, v98\n"
    "v_mfma_f32_16x16x32_bf16 v[220:223], a[140:143], v[4:7], v[220:223]\n"
    "ds_read_b128 a[96:99], v128 offset:24576\n"
    "v_cvt_pk_bf16_f32 v66, v96, v97\n"
    "v_mfma_f32_16x16x32_bf16 v[224:227], a[140:143], v[12:15], v[224:227]\n"
    "v_exp_f32 v99, v99\n"
    "v_mfma_f32_16x16x32_bf16 v[224:227], a[136:139], v[8:11], v[224:227]\n"
    "ds_read_b128 a[100:103], v128 offset:24832\n"
    "v_cvt_pk_bf16_f32 v67, v98, v99\n"
    "v_mfma_f32_16x16x32_bf16 v[96:99], a[112:115], a[64:67], v[120:123]\n"
    "v_exp_f32 v100, v100\n"
    "v_mfma_f32_16x16x32_bf16 v[96:99], a[116:119], a[68:71], v[96:99]\n"
    "ds_read_b64_tr_b16 a[128:129], v129 offset:33280\n"
    "ds_read_b64_tr_b16 a[130:131], v129 offset:37376\n"
    "v_mfma_f32_16x16x32_bf16 v[96:99], a[120:123], a[72:75], v[96:99]\n"
    "s_mov_b32 m0, s82\n"
    "v_exp_f32 v101, v101\n"
    "v_mfma_f32_16x16x32_bf16 v[96:99], a[124:127], a[76:79], v[96:99]\n"
    "buffer_load_dwordx4 v131, s[40:43], s56 offen lds\n"
    "v_mfma_f32_16x16x32_bf16 v[228:231], a[136:139], v[16:19], v[228:231]\n"
    "v_exp_f32 v102, v102\n"
    "v_mfma_f32_16x16x32_bf16 v[228:231], a[140:143], v[20:23], v[228:231]\n"
    "ds_read_b128 a[104:107], v128 offset:25088\n"
    "v_cvt_pk_bf16_f32 v74, v100, v101\n"
    "v_mfma_f32_16x16x32_bf16 v[232:235], a[140:143], v[28:31], v[232:235]\n"
    "v_exp_f32 v103, v103\n"
    "v_mfma_f32_16x16x32_bf16 v[232:235], a[136:139], v[24:27], v[232:235]\n"
    "ds_read_b128 a[108:111], v128 offset:25344\n"
    "v_cvt_pk_bf16_f32 v75, v102, v103\n"
    "v_mfma_f32_16x16x32_bf16 v[100:103], a[112:115], a[80:83], v[124:127]\n"
    "v_exp_f32 v96, v96\n"
    "v_mfma_f32_16x16x32_bf16 v[100:103], a[116:119], a[84:87], v[100:103]\n"
    "ds_read_b64_tr_b16 a[132:133], v129 offset:41472\n"
    "ds_read_b64_tr_b16 a[134:135], v129 offset:45568\n"
    "v_mfma_f32_16x16x32_bf16 v[100:103], a[120:123], a[88:91], v[100:103]\n"
    "v_exp_f32 v97, v97\n"
    "v_mfma_f32_16x16x32_bf16 v[100:103], a[124:127], a[92:95], v[100:103]\n"
    "buffer_load_dwordx4 v131, s[40:43], s56 offen offset:2048 lds\n"
    "v_mfma_f32_16x16x32_bf16 v[236:239], a[136:139], v[32:35], v[236:239]\n"
    "v_exp_f32 v98, v98\n"
    "v_mfma_f32_16x16x32_bf16 v[236:239], a[140:143], v[36:39], v[236:239]\n"
    "v_cvt_pk_bf16_f32 v82, v96, v97\n"
    "v_mfma_f32_16x16x32_bf16 v[240:243], a[140:143], v[44:47], v[240:243]\n"
    "v_exp_f32 v99, v99\n"
    "v_mfma_f32_16x16x32_bf16 v[240:243], a[136:139], v[40:43], v[240:243]\n"
    "s_waitcnt lgkmcnt(2)\n"
    "v_cvt_pk_bf16_f32 v83, v98, v99\n"
    "v_smfmac_f32_16x16x64_bf16 a[152:155], a[168:171], v[16:23], v133\n"
    "s_waitcnt lgkmcnt(10)\n"
    "buffer_store_dwordx2 v[136:137], v140, s[52:55], s59 offen offset:128 sc0 sc1 nt\n"
    "v_mfma_f32_16x16x32_bf16 v[96:99], a[96:99], a[0:3], v[104:107]\n"
    "v_exp_f32 v100, v100\n"
    "v_mfma_f32_16x16x32_bf16 v[96:99], a[100:103], a[4:7], v[96:99]\n"
    "v_pk_maximum3_f16 v141, v141, v136, v137\n"
    "v_mfma_f32_16x16x32_bf16 v[96:99], a[104:107], a[8:11], v[96:99]\n"
    "v_exp_f32 v101, v101\n"
    "v_mfma_f32_16x16x32_bf16 v[96:99], a[108:111], a[12:15], v[96:99]\n"
    "s_waitcnt lgkmcnt(0)\n"
    "v_mfma_f32_16x16x32_bf16 v[244:247], a[128:131], v[0:3], v[244:247]\n"
    "v_exp_f32 v102, v102\n"
    "v_mfma_f32_16x16x32_bf16 v[244:247], a[132:135], v[4:7], v[244:247]\n"
    "ds_read_b128 a[112:115], v128 offset:28672\n"
    "v_cvt_pk_bf16_f32 v90, v100, v101\n"
    "v_mfma_f32_16x16x32_bf16 v[248:251], a[132:135], v[12:15], v[248:251]\n"
    "v_exp_f32 v103, v103\n"
    "v_mfma_f32_16x16x32_bf16 v[248:251], a[128:131], v[8:11], v[248:251]\n"
    "ds_read_b128 a[116:119], v128 offset:28928\n"
    "v_cvt_pk_bf16_f32 v91, v102, v103\n"
    "v_mfma_f32_16x16x32_bf16 v[100:103], a[96:99], a[16:19], v[108:111]\n"
    "v_exp_f32 v96, v96\n"
    "v_mfma_f32_16x16x32_bf16 v[100:103], a[100:103], a[20:23], v[100:103]\n"
    "ds_read_b64_tr_b16 a[136:137], v130 offset:33280\n"
    "v_mfma_f32_16x16x32_bf16 v[100:103], a[104:107], a[24:27], v[100:103]\n"
    "v_exp_f32 v97, v97\n"
    "v_mfma_f32_16x16x32_bf16 v[100:103], a[108:111], a[28:31], v[100:103]\n"
    "ds_read_b64_tr_b16 a[138:139], v130 offset:37376\n"
    "v_mfma_f32_16x16x32_bf16 v[252:255], a[128:131], v[16:19], v[252:255]\n"
    "v_exp_f32 v98, v98\n"
    "v_mfma_f32_16x16x32_bf16 v[252:255], a[132:135], v[20:23], v[252:255]\n"
    "ds_read_b64 v[136:137], v134 offset:2304\n"
    "v_cvt_pk_bf16_f32 v52, v96, v97\n"
    "v_mfma_f32_16x16x32_bf16 a[172:175], a[132:135], v[28:31], a[172:175]\n"
    "v_exp_f32 v99, v99\n"
    "v_mfma_f32_16x16x32_bf16 a[172:175], a[128:131], v[24:27], a[172:175]\n"
    "v_cvt_pk_bf16_f32 v53, v98, v99\n"
    "v_mfma_f32_16x16x32_bf16 v[96:99], a[96:99], a[32:35], v[112:115]\n"
    "v_exp_f32 v100, v100\n"
    "v_mfma_f32_16x16x32_bf16 v[96:99], a[100:103], a[36:39], v[96:99]\n"
    "ds_read_b64_tr_b16 a[140:141], v130 offset:41472\n"
    "v_mfma_f32_16x16x32_bf16 v[96:99], a[104:107], a[40:43], v[96:99]\n"
    "v_exp_f32 v101, v101\n"
    "v_mfma_f32_16x16x32_bf16 v[96:99], a[108:111], a[44:47], v[96:99]\n"
    "ds_read_b64_tr_b16 a[142:143], v130 offset:45568\n"
    "v_mfma_f32_16x16x32_bf16 a[176:179], a[128:131], v[32:35], a[176:179]\n"
    "v_exp_f32 v102, v102\n"
    "v_mfma_f32_16x16x32_bf16 a[176:179], a[132:135], v[36:39], a[176:179]\n"
    "v_cvt_pk_bf16_f32 v60, v100, v101\n"
    "v_mfma_f32_16x16x32_bf16 a[180:183], a[132:135], v[44:47], a[180:183]\n"
    "v_exp_f32 v103, v103\n"
    "v_mfma_f32_16x16x32_bf16 a[180:183], a[128:131], v[40:43], a[180:183]\n"
    "v_cvt_pk_bf16_f32 v61, v102, v103\n"
    "v_mfma_f32_16x16x32_bf16 v[100:103], a[96:99], a[48:51], v[116:119]\n"
    "v_exp_f32 v96, v96\n"
    "v_mfma_f32_16x16x32_bf16 v[100:103], a[100:103], a[52:55], v[100:103]\n"
    "s_waitcnt lgkmcnt(15)\n"
    "buffer_store_dwordx2 v[138:139], v140, s[52:55], s59 offen offset:1152 sc0 sc1 nt\n"
    "v_mfma_f32_16x16x32_bf16 v[100:103], a[104:107], a[56:59], v[100:103]\n"
    "v_exp_f32 v97, v97\n"
    "v_mfma_f32_16x16x32_bf16 v[100:103], a[108:111], a[60:63], v[100:103]\n"
    "s_waitcnt lgkmcnt(0)\n"
    "v_pk_maximum3_f16 v141, v141, v138, v139\n"
    "v_mfma_f32_16x16x32_bf16 a[184:187], a[136:139], v[0:3], a[184:187]\n"
    "v_exp_f32 v98, v98\n"
    "v_mfma_f32_16x16x32_bf16 a[184:187], a[140:143], v[4:7], a[184:187]\n"
    "ds_read_b128 a[120:123], v128 offset:29184\n"
    "v_cvt_pk_bf16_f32 v68, v96, v97\n"
    "v_mfma_f32_16x16x32_bf16 a[188:191], a[140:143], v[12:15], a[188:191]\n"
    "v_exp_f32 v99, v99\n"
    "v_mfma_f32_16x16x32_bf16 a[188:191], a[136:139], v[8:11], a[188:191]\n"
    "ds_read_b128 a[124:127], v128 offset:29440\n"
    "v_cvt_pk_bf16_f32 v69, v98, v99\n"
    "v_smfmac_f32_16x16x64_bf16 a[156:159], a[168:171], v[24:31], v133\n"
    "s_waitcnt lgkmcnt(4)\n"
    "buffer_store_dwordx2 v[136:137], v140, s[52:55], s59 offen offset:2176 sc0 sc1 nt\n"
    "v_mfma_f32_16x16x32_bf16 v[96:99], a[96:99], a[64:67], v[120:123]\n"
    "v_exp_f32 v100, v100\n"
    "v_mfma_f32_16x16x32_bf16 v[96:99], a[100:103], a[68:71], v[96:99]\n"
    "ds_read_b64_tr_b16 a[128:129], v129 offset:33536\n"
    "v_pk_maximum3_f16 v141, v141, v136, v137\n"
    "v_mfma_f32_16x16x32_bf16 v[96:99], a[104:107], a[72:75], v[96:99]\n"
    "v_exp_f32 v101, v101\n"
    "v_mfma_f32_16x16x32_bf16 v[96:99], a[108:111], a[76:79], v[96:99]\n"
    "ds_read_b64_tr_b16 a[130:131], v129 offset:37632\n"
    "v_mfma_f32_16x16x32_bf16 a[192:195], a[136:139], v[16:19], a[192:195]\n"
    "v_exp_f32 v102, v102\n"
    "v_mfma_f32_16x16x32_bf16 a[192:195], a[140:143], v[20:23], a[192:195]\n"
    "ds_read_b64 v[138:139], v142 offset:2304\n"
    "v_cvt_pk_bf16_f32 v76, v100, v101\n"
    "v_mfma_f32_16x16x32_bf16 a[196:199], a[140:143], v[28:31], a[196:199]\n"
    "v_exp_f32 v103, v103\n"
    "v_mfma_f32_16x16x32_bf16 a[196:199], a[136:139], v[24:27], a[196:199]\n"
    "v_cvt_pk_bf16_f32 v77, v102, v103\n"
    "v_mfma_f32_16x16x32_bf16 v[100:103], a[96:99], a[80:83], v[124:127]\n"
    "v_exp_f32 v96, v96\n"
    "v_mfma_f32_16x16x32_bf16 v[100:103], a[100:103], a[84:87], v[100:103]\n"
    "ds_read_b64_tr_b16 a[132:133], v129 offset:41728\n"
    "v_add_u32 v134, 0x1000, v134\n"
    "v_mfma_f32_16x16x32_bf16 v[100:103], a[104:107], a[88:91], v[100:103]\n"
    "v_exp_f32 v97, v97\n"
    "v_mfma_f32_16x16x32_bf16 v[100:103], a[108:111], a[92:95], v[100:103]\n"
    "ds_read_b64_tr_b16 a[134:135], v129 offset:45824\n"
    "v_add_u32 v142, 0x1000, v142\n"
    "v_mfma_f32_16x16x32_bf16 a[200:203], a[136:139], v[32:35], a[200:203]\n"
    "v_exp_f32 v98, v98\n"
    "v_mfma_f32_16x16x32_bf16 a[200:203], a[140:143], v[36:39], a[200:203]\n"
    "v_cvt_pk_bf16_f32 v84, v96, v97\n"
    "v_mfma_f32_16x16x32_bf16 a[204:207], a[140:143], v[44:47], a[204:207]\n"
    "v_exp_f32 v99, v99\n"
    "v_mfma_f32_16x16x32_bf16 a[204:207], a[136:139], v[40:43], a[204:207]\n"
    "s_waitcnt lgkmcnt(5)\n"
    "v_cvt_pk_bf16_f32 v85, v98, v99\n"
    "v_mfma_f32_16x16x32_bf16 v[96:99], a[112:115], a[0:3], v[104:107]\n"
    "v_exp_f32 v100, v100\n"
    "v_mfma_f32_16x16x32_bf16 v[96:99], a[116:119], a[4:7], v[96:99]\n"
    "s_waitcnt lgkmcnt(2)\n"
    "buffer_store_dwordx2 v[138:139], v140, s[52:55], s59 offen offset:3200 sc0 sc1 nt\n"
    "v_mfma_f32_16x16x32_bf16 v[96:99], a[120:123], a[8:11], v[96:99]\n"
    "v_exp_f32 v101, v101\n"
    "v_mfma_f32_16x16x32_bf16 v[96:99], a[124:127], a[12:15], v[96:99]\n"
    "s_waitcnt lgkmcnt(0)\n"
    "v_pk_maximum3_f16 v141, v141, v138, v139\n"
    "v_mfma_f32_16x16x32_bf16 a[208:211], a[128:131], v[0:3], a[208:211]\n"
    "v_exp_f32 v102, v102\n"
    "v_mfma_f32_16x16x32_bf16 a[208:211], a[132:135], v[4:7], a[208:211]\n"
    "v_cvt_pk_bf16_f32 v92, v100, v101\n"
    "v_mfma_f32_16x16x32_bf16 a[212:215], a[132:135], v[12:15], a[212:215]\n"
    "v_exp_f32 v103, v103\n"
    "v_mfma_f32_16x16x32_bf16 a[212:215], a[128:131], v[8:11], a[212:215]\n"
    "v_cvt_pk_bf16_f32 v93, v102, v103\n"
    "v_mfma_f32_16x16x32_bf16 v[100:103], a[112:115], a[16:19], v[108:111]\n"
    "v_exp_f32 v96, v96\n"
    "v_mfma_f32_16x16x32_bf16 v[100:103], a[116:119], a[20:23], v[100:103]\n"
    "ds_read_b64_tr_b16 a[136:137], v130 offset:33536\n"
    "v_mfma_f32_16x16x32_bf16 v[100:103], a[120:123], a[24:27], v[100:103]\n"
    "v_exp_f32 v97, v97\n"
    "v_mfma_f32_16x16x32_bf16 v[100:103], a[124:127], a[28:31], v[100:103]\n"
    "ds_read_b64_tr_b16 a[138:139], v130 offset:37632\n"
    "v_mfma_f32_16x16x32_bf16 a[216:219], a[128:131], v[16:19], a[216:219]\n"
    "v_exp_f32 v98, v98\n"
    "v_mfma_f32_16x16x32_bf16 a[216:219], a[132:135], v[20:23], a[216:219]\n"
    "v_cvt_pk_bf16_f32 v54, v96, v97\n"
    "v_mfma_f32_16x16x32_bf16 a[220:223], a[132:135], v[28:31], a[220:223]\n"
    "v_exp_f32 v99, v99\n"
    "v_mfma_f32_16x16x32_bf16 a[220:223], a[128:131], v[24:27], a[220:223]\n"
    "v_cvt_pk_bf16_f32 v55, v98, v99\n"
    "v_smfmac_f32_16x16x64_bf16 a[160:163], a[168:171], v[32:39], v133\n"
    "v_mfma_f32_16x16x32_bf16 v[96:99], a[112:115], a[32:35], v[112:115]\n"
    "s_add_u32 s59, s59, 16384\n"
    "v_exp_f32 v100, v100\n"
    "v_mfma_f32_16x16x32_bf16 v[96:99], a[116:119], a[36:39], v[96:99]\n"
    "s_cmp_eq_u32 s60, 5\n"
    "ds_read_b64_tr_b16 a[140:141], v130 offset:41728\n"
    "v_mfma_f32_16x16x32_bf16 v[96:99], a[120:123], a[40:43], v[96:99]\n"
    "s_cselect_b32 s54, 0, s54\n"
    "v_exp_f32 v101, v101\n"
    "v_mfma_f32_16x16x32_bf16 v[96:99], a[124:127], a[44:47], v[96:99]\n"
    "ds_read_b64_tr_b16 a[142:143], v130 offset:45824\n"
    "v_mfma_f32_16x16x32_bf16 a[224:227], a[128:131], v[32:35], a[224:227]\n"
    "v_exp_f32 v102, v102\n"
    "v_mfma_f32_16x16x32_bf16 a[224:227], a[132:135], v[36:39], a[224:227]\n"
    "v_cvt_pk_bf16_f32 v62, v100, v101\n"
    "v_mfma_f32_16x16x32_bf16 a[228:231], a[132:135], v[44:47], a[228:231]\n"
    "v_exp_f32 v103, v103\n"
    "v_mfma_f32_16x16x32_bf16 a[228:231], a[128:131], v[40:43], a[228:231]\n"
    "v_cvt_pk_bf16_f32 v63, v102, v103\n"
    "v_mfma_f32_16x16x32_bf16 v[100:103], a[112:115], a[48:51], v[116:119]\n"
    "v_exp_f32 v96, v96\n"
    "v_mfma_f32_16x16x32_bf16 v[100:103], a[116:119], a[52:55], v[100:103]\n"
    "v_mfma_f32_16x16x32_bf16 v[100:103], a[120:123], a[56:59], v[100:103]\n"
    "s_waitcnt vmcnt(8) lgkmcnt(0)\n"
    "v_exp_f32 v97, v97\n"
    "v_mfma_f32_16x16x32_bf16 v[100:103], a[124:127], a[60:63], v[100:103]\n"
    "s_barrier\n"
    "v_mfma_f32_16x16x32_bf16 a[232:235], a[136:139], v[0:3], a[232:235]\n"
    "v_exp_f32 v98, v98\n"
    "v_mfma_f32_16x16x32_bf16 a[232:235], a[140:143], v[4:7], a[232:235]\n"
    "ds_read_b128 a[96:99], v128 offset:0\n"
    "v_cvt_pk_bf16_f32 v70, v96, v97\n"
    "v_mfma_f32_16x16x32_bf16 a[236:239], a[140:143], v[12:15], a[236:239]\n"
    "v_exp_f32 v99, v99\n"
    "v_mfma_f32_16x16x32_bf16 a[236:239], a[136:139], v[8:11], a[236:239]\n"
    "ds_read_b128 a[100:103], v128 offset:256\n"
    "v_cvt_pk_bf16_f32 v71, v98, v99\n"
    "v_mfma_f32_16x16x32_bf16 v[96:99], a[112:115], a[64:67], v[120:123]\n"
    "s_add_u32 s60, s60, 1\n"
    "v_exp_f32 v100, v100\n"
    "v_mfma_f32_16x16x32_bf16 v[96:99], a[116:119], a[68:71], v[96:99]\n"
    "s_cmp_lt_u32 s60, 7\n"
    "ds_read_b64_tr_b16 a[128:129], v129 offset:49152\n"
    "ds_read_b64_tr_b16 a[130:131], v129 offset:53248\n"
    "v_mfma_f32_16x16x32_bf16 v[96:99], a[120:123], a[72:75], v[96:99]\n"
    "s_mov_b32 m0, s84\n"
    "v_exp_f32 v101, v101\n"
    "v_mfma_f32_16x16x32_bf16 v[96:99], a[124:127], a[76:79], v[96:99]\n"
    "buffer_load_dwordx4 v131, s[44:47], s57 offen nt lds\n"
    "v_mfma_f32_16x16x32_bf16 a[240:243], a[136:139], v[16:19], a[240:243]\n"
    "v_exp_f32 v102, v102\n"
    "v_mfma_f32_16x16x32_bf16 a[240:243], a[140:143], v[20:23], a[240:243]\n"
    "ds_read_b128 a[104:107], v128 offset:512\n"
    "v_cvt_pk_bf16_f32 v78, v100, v101\n"
    "v_mfma_f32_16x16x32_bf16 a[244:247], a[140:143], v[28:31], a[244:247]\n"
    "v_exp_f32 v103, v103\n"
    "v_mfma_f32_16x16x32_bf16 a[244:247], a[136:139], v[24:27], a[244:247]\n"
    "ds_read_b128 a[108:111], v128 offset:768\n"
    "v_cvt_pk_bf16_f32 v79, v102, v103\n"
    "v_mfma_f32_16x16x32_bf16 v[100:103], a[112:115], a[80:83], v[124:127]\n"
    "v_exp_f32 v96, v96\n"
    "v_mfma_f32_16x16x32_bf16 v[100:103], a[116:119], a[84:87], v[100:103]\n"
    "ds_read_b64_tr_b16 a[132:133], v129 offset:57344\n"
    "ds_read_b64_tr_b16 a[134:135], v129 offset:61440\n"
    "v_mfma_f32_16x16x32_bf16 v[100:103], a[120:123], a[88:91], v[100:103]\n"
    "v_exp_f32 v97, v97\n"
    "v_mfma_f32_16x16x32_bf16 v[100:103], a[124:127], a[92:95], v[100:103]\n"
    "buffer_load_dwordx4 v131, s[44:47], s57 offen offset:2048 nt lds\n"
    "v_mfma_f32_16x16x32_bf16 a[248:251], a[136:139], v[32:35], a[248:251]\n"
    "v_exp_f32 v98, v98\n"
    "v_mfma_f32_16x16x32_bf16 a[248:251], a[140:143], v[36:39], a[248:251]\n"
    "v_cvt_pk_bf16_f32 v86, v96, v97\n"
    "v_mfma_f32_16x16x32_bf16 a[252:255], a[140:143], v[44:47], a[252:255]\n"
    "v_exp_f32 v99, v99\n"
    "v_mfma_f32_16x16x32_bf16 a[252:255], a[136:139], v[40:43], a[252:255]\n"
    "s_waitcnt lgkmcnt(2)\n"
    "v_cvt_pk_bf16_f32 v87, v98, v99\n"
    "v_smfmac_f32_16x16x64_bf16 a[164:167], a[168:171], v[40:47], v133\n"
    "s_cbranch_scc1 Lloop2_%=\n"
    "ds_read_b64_tr_b16 a[48:49], v130 offset:49920\n"
    "ds_read_b64_tr_b16 a[50:51], v130 offset:54016\n"
    "ds_read_b64_tr_b16 a[52:53], v130 offset:58112\n"
    "ds_read_b64_tr_b16 a[54:55], v130 offset:62208\n"
    "ds_read_b64_tr_b16 a[40:41], v129 offset:49920\n"
    "ds_read_b64_tr_b16 a[42:43], v129 offset:54016\n"
    "ds_read_b64_tr_b16 a[44:45], v129 offset:58112\n"
    "ds_read_b64_tr_b16 a[46:47], v129 offset:62208\n"
    "v_exp_f32 v100, v100\n"
    "v_exp_f32 v101, v101\n"
    "v_exp_f32 v102, v102\n"
    "v_cvt_pk_bf16_f32 v94, v100, v101\n"
    "v_exp_f32 v103, v103\n"
    "s_nop 0\n"
    "v_cvt_pk_bf16_f32 v95, v102, v103\n"
    "v_smfmac_f32_16x16x64_bf16 a[144:147], a[168:171], v[48:55], v133\n"
    "ds_read_b64_tr_b16 a[32:33], v130 offset:49664\n"
    "ds_read_b64_tr_b16 a[34:35], v130 offset:53760\n"
    "v_mfma_f32_16x16x32_bf16 v[148:151], a[128:131], v[48:51], v[148:151]\n"
    "ds_read_b64_tr_b16 a[36:37], v130 offset:57856\n"
    "s_waitcnt lgkmcnt(14)\n"
    "ds_read_b64_tr_b16 a[38:39], v130 offset:61952\n"
    "s_waitcnt lgkmcnt(12)\n"
    "v_mfma_f32_16x16x32_bf16 v[148:151], a[132:135], v[52:55], v[148:151]\n"
    "ds_read_b64_tr_b16 a[24:25], v129 offset:49664\n"
    "ds_read_b64_tr_b16 a[26:27], v129 offset:53760\n"
    "s_waitcnt lgkmcnt(12)\n"
    "v_mfma_f32_16x16x32_bf16 a[232:235], a[48:51], v[48:51], a[232:235]\n"
    "ds_read_b64_tr_b16 a[28:29], v129 offset:57856\n"
    "ds_read_b64_tr_b16 a[30:31], v129 offset:61952\n"
    "s_waitcnt lgkmcnt(12)\n"
    "v_mfma_f32_16x16x32_bf16 a[232:235], a[52:55], v[52:55], a[232:235]\n"
    "ds_read_b64_tr_b16 a[16:17], v130 offset:49408\n"
    "ds_read_b64_tr_b16 a[18:19], v130 offset:53504\n"
    "s_waitcnt lgkmcnt(12)\n"
    "v_mfma_f32_16x16x32_bf16 a[208:211], a[40:43], v[48:51], a[208:211]\n"
    "ds_read_b64_tr_b16 a[20:21], v130 offset:57600\n"
    "ds_read_b64_tr_b16 a[22:23], v130 offset:61696\n"
    "s_waitcnt lgkmcnt(12)\n"
    "v_mfma_f32_16x16x32_bf16 a[208:211], a[44:47], v[52:55], a[208:211]\n"
    "ds_read_b64_tr_b16 a[8:9], v129 offset:49408\n"
    "ds_read_b64_tr_b16 a[10:11], v129 offset:53504\n"
    "s_waitcnt lgkmcnt(12)\n"
    "v_mfma_f32_16x16x32_bf16 a[184:187], a[32:35], v[48:51], a[184:187]\n"
    "ds_read_b64_tr_b16 a[12:13], v129 offset:57600\n"
    "ds_read_b64_tr_b16 a[14:15], v129 offset:61696\n"
    "s_waitcnt lgkmcnt(12)\n"
    "v_mfma_f32_16x16x32_bf16 a[184:187], a[36:39], v[52:55], a[184:187]\n"
    "ds_read_b64_tr_b16 a[0:1], v130 offset:49152\n"
    "ds_read_b64_tr_b16 a[2:3], v130 offset:53248\n"
    "s_waitcnt lgkmcnt(12)\n"
    "v_mfma_f32_16x16x32_bf16 v[244:247], a[24:27], v[48:51], v[244:247]\n"
    "ds_read_b64_tr_b16 a[4:5], v130 offset:57344\n"
    "ds_read_b64_tr_b16 a[6:7], v130 offset:61440\n"
    "s_waitcnt lgkmcnt(12)\n"
    "v_mfma_f32_16x16x32_bf16 v[244:247], a[28:31], v[52:55], v[244:247]\n"
    "v_mbcnt_lo_u32_b32 v136, -1, 0\n"
    "v_mbcnt_hi_u32_b32 v136, -1, v136\n"
    "s_waitcnt lgkmcnt(10)\n"
    "v_mfma_f32_16x16x32_bf16 v[220:223], a[16:19], v[48:51], v[220:223]\n"
    "v_lshrrev_b32 v137, 3, v136\n"
    "v_lshlrev_b32 v140, 8, v137\n"
    "s_waitcnt lgkmcnt(8)\n"
    "v_mfma_f32_16x16x32_bf16 v[220:223], a[20:23], v[52:55], v[220:223]\n"
    "v_lshlrev_b32 v134, 5, v137\n"
    "v_and_b32 v137, 7, v136\n"
    "s_waitcnt lgkmcnt(6)\n"
    "v_mfma_f32_16x16x32_bf16 v[196:199], a[8:11], v[48:51], v[196:199]\n"
    "v_lshl_add_u32 v140, v137, 4, v140\n"
    "v_bfe_u32 v137, v136, 1, 2\n"
    "s_waitcnt lgkmcnt(4)\n"
    "v_mfma_f32_16x16x32_bf16 v[196:199], a[12:15], v[52:55], v[196:199]\n"
    "v_lshl_add_u32 v134, v137, 9, v134\n"
    "v_lshrrev_b32 v137, 5, v136\n"
    "s_waitcnt lgkmcnt(2)\n"
    "v_mfma_f32_16x16x32_bf16 v[172:175], a[0:3], v[48:51], v[172:175]\n"
    "v_accvgpr_read_b32 v116, a144\n"
    "v_accvgpr_read_b32 v117, a145\n"
    "s_waitcnt lgkmcnt(0)\n"
    "v_mfma_f32_16x16x32_bf16 v[172:175], a[4:7], v[52:55], v[172:175]\n"
    "v_add_f32 v116, v116, v117\n"
    "v_rcp_f32 v104, v116\n"
    "v_xor_b32 v137, v137, v136\n"
    "v_and_b32 v137, 1, v137\n"
    "v_lshl_add_u32 v134, v137, 4, v134\n"
    "v_add_u32 v134, s62, v134\n"
    "v_mov_b32 v96, 0\n"
    "v_mov_b32 v97, 0\n"
    "v_cmp_u_f16_sdwa vcc, v141, v141 src0_sel:WORD_1 src1_sel:WORD_0\n"
    "s_cmp_lg_u64 vcc, 0\n"
    "s_cselect_b32 s66, 1, s66\n"
    "v_cmp_ngt_f32 vcc, s67, v116\n"
    "s_mov_b64 s[78:79], vcc\n"
    "s_mov_b32 s74, 25165824\n"
    "v_accvgpr_read_b32 v12, a232\n"
    "v_accvgpr_read_b32 v13, a233\n"
    "v_accvgpr_read_b32 v14, a234\n"
    "v_accvgpr_read_b32 v15, a235\n"
    "v_accvgpr_read_b32 v8, a208\n"
    "v_accvgpr_read_b32 v9, a209\n"
    "v_accvgpr_read_b32 v10, a210\n"
    "v_accvgpr_read_b32 v11, a211\n"
    "v_accvgpr_read_b32 v4, a184\n"
    "v_accvgpr_read_b32 v5, a185\n"
    "v_accvgpr_read_b32 v6, a186\n"
    "v_accvgpr_read_b32 v7, a187\n"
    "v_pk_mul_f32 v[148:149], v[148:149], v[104:105] op_sel_hi:[1,0]\n"
    "v_pk_mul_f32 v[150:151], v[150:151], v[104:105] op_sel_hi:[1,0]\n"
    "v_pk_mul_f32 v[172:173], v[172:173], v[104:105] op_sel_hi:[1,0]\n"
    "v_pk_mul_f32 v[174:175], v[174:175], v[104:105] op_sel_hi:[1,0]\n"
    "v_pk_mul_f32 v[196:197], v[196:197], v[104:105] op_sel_hi:[1,0]\n"
    "v_pk_mul_f32 v[198:199], v[198:199], v[104:105] op_sel_hi:[1,0]\n"
    "v_pk_mul_f32 v[220:221], v[220:221], v[104:105] op_sel_hi:[1,0]\n"
    "v_pk_mul_f32 v[222:223], v[222:223], v[104:105] op_sel_hi:[1,0]\n"
    "v_pk_mul_f32 v[244:245], v[244:245], v[104:105] op_sel_hi:[1,0]\n"
    "v_pk_mul_f32 v[246:247], v[246:247], v[104:105] op_sel_hi:[1,0]\n"
    "v_pk_mul_f32 v[4:5], v[4:5], v[104:105] op_sel_hi:[1,0]\n"
    "v_pk_mul_f32 v[6:7], v[6:7], v[104:105] op_sel_hi:[1,0]\n"
    "v_pk_mul_f32 v[8:9], v[8:9], v[104:105] op_sel_hi:[1,0]\n"
    "v_pk_mul_f32 v[10:11], v[10:11], v[104:105] op_sel_hi:[1,0]\n"
    "v_pk_mul_f32 v[12:13], v[12:13], v[104:105] op_sel_hi:[1,0]\n"
    "v_pk_mul_f32 v[14:15], v[14:15], v[104:105] op_sel_hi:[1,0]\n"
    "v_cvt_pk_bf16_f32 v148, v148, v149\n"
    "v_cvt_pk_bf16_f32 v149, v150, v151\n"
    "v_cvt_pk_bf16_f32 v172, v172, v173\n"
    "v_cvt_pk_bf16_f32 v173, v174, v175\n"
    "v_cvt_pk_bf16_f32 v196, v196, v197\n"
    "v_cvt_pk_bf16_f32 v197, v198, v199\n"
    "v_cvt_pk_bf16_f32 v220, v220, v221\n"
    "v_cvt_pk_bf16_f32 v221, v222, v223\n"
    "v_cvt_pk_bf16_f32 v244, v244, v245\n"
    "v_cvt_pk_bf16_f32 v245, v246, v247\n"
    "v_cvt_pk_bf16_f32 v174, v4, v5\n"
    "v_cvt_pk_bf16_f32 v175, v6, v7\n"
    "v_cvt_pk_bf16_f32 v198, v8, v9\n"
    "v_cvt_pk_bf16_f32 v199, v10, v11\n"
    "v_cvt_pk_bf16_f32 v222, v12, v13\n"
    "v_cvt_pk_bf16_f32 v223, v14, v15\n"
    "v_smfmac_f32_16x16x64_bf16 a[148:151], a[168:171], v[56:63], v133\n"
    "ds_write_b64 v135, v[148:149] offset:0\n"
    "v_mfma_f32_16x16x32_bf16 v[152:155], a[128:131], v[56:59], v[152:155]\n"
    "ds_write_b64 v135, v[172:173] offset:512\n"
    "v_mfma_f32_16x16x32_bf16 v[152:155], a[132:135], v[60:63], v[152:155]\n"
    "ds_write_b64 v135, v[196:197] offset:1024\n"
    "v_mfma_f32_16x16x32_bf16 a[236:239], a[48:51], v[56:59], a[236:239]\n"
    "ds_write_b64 v135, v[220:221] offset:1536\n"
    "v_mfma_f32_16x16x32_bf16 a[236:239], a[52:55], v[60:63], a[236:239]\n"
    "ds_read_b128 v[16:19], v134 offset:0\n"
    "ds_read_b128 v[20:23], v134 offset:256\n"
    "v_mfma_f32_16x16x32_bf16 a[212:215], a[40:43], v[56:59], a[212:215]\n"
    "ds_write_b64 v135, v[244:245] offset:2048\n"
    "v_mfma_f32_16x16x32_bf16 a[212:215], a[44:47], v[60:63], a[212:215]\n"
    "ds_write_b64 v135, v[174:175] offset:2560\n"
    "v_mfma_f32_16x16x32_bf16 a[188:191], a[32:35], v[56:59], a[188:191]\n"
    "ds_write_b64 v135, v[198:199] offset:3072\n"
    "v_mfma_f32_16x16x32_bf16 a[188:191], a[36:39], v[60:63], a[188:191]\n"
    "ds_write_b64 v135, v[222:223] offset:3584\n"
    "v_mfma_f32_16x16x32_bf16 v[248:251], a[24:27], v[56:59], v[248:251]\n"
    "ds_read_b128 v[24:27], v134 offset:2048\n"
    "ds_read_b128 v[28:31], v134 offset:2304\n"
    "v_mfma_f32_16x16x32_bf16 v[248:251], a[28:31], v[60:63], v[248:251]\n"
    "s_waitcnt lgkmcnt(7)\n"
    "buffer_store_dwordx4 v[16:19], v140, s[48:51], s74 offen offset:0 sc1\n"
    "v_mfma_f32_16x16x32_bf16 v[224:227], a[16:19], v[56:59], v[224:227]\n"
    "s_waitcnt lgkmcnt(6)\n"
    "buffer_store_dwordx4 v[20:23], v140, s[48:51], s74 offen offset:2048 sc1\n"
    "v_mfma_f32_16x16x32_bf16 v[224:227], a[20:23], v[60:63], v[224:227]\n"
    "v_accvgpr_read_b32 v116, a148\n"
    "v_accvgpr_read_b32 v117, a149\n"
    "v_mfma_f32_16x16x32_bf16 v[200:203], a[8:11], v[56:59], v[200:203]\n"
    "v_add_f32 v116, v116, v117\n"
    "v_rcp_f32 v106, v116\n"
    "v_mfma_f32_16x16x32_bf16 v[200:203], a[12:15], v[60:63], v[200:203]\n"
    "v_cmp_ngt_f32 vcc, s67, v116\n"
    "s_or_b64 s[78:79], s[78:79], vcc\n"
    "v_mfma_f32_16x16x32_bf16 v[176:179], a[0:3], v[56:59], v[176:179]\n"
    "s_waitcnt lgkmcnt(1)\n"
    "buffer_store_dwordx4 v[24:27], v140, s[48:51], s74 offen offset:128 sc1\n"
    "v_mfma_f32_16x16x32_bf16 v[176:179], a[4:7], v[60:63], v[176:179]\n"
    "s_waitcnt lgkmcnt(0)\n"
    "buffer_store_dwordx4 v[28:31], v140, s[48:51], s74 offen offset:2176 sc1\n"
    "s_mov_b32 s75, 25182208\n"
    "v_accvgpr_read_b32 v12, a236\n"
    "v_accvgpr_read_b32 v13, a237\n"
    "v_accvgpr_read_b32 v14, a238\n"
    "v_accvgpr_read_b32 v15, a239\n"
    "v_accvgpr_read_b32 v8, a212\n"
    "v_accvgpr_read_b32 v9, a213\n"
    "v_accvgpr_read_b32 v10, a214\n"
    "v_accvgpr_read_b32 v11, a215\n"
    "v_accvgpr_read_b32 v4, a188\n"
    "v_accvgpr_read_b32 v5, a189\n"
    "v_accvgpr_read_b32 v6, a190\n"
    "v_accvgpr_read_b32 v7, a191\n"
    "v_pk_mul_f32 v[152:153], v[152:153], v[106:107] op_sel_hi:[1,0]\n"
    "v_pk_mul_f32 v[154:155], v[154:155], v[106:107] op_sel_hi:[1,0]\n"
    "v_pk_mul_f32 v[176:177], v[176:177], v[106:107] op_sel_hi:[1,0]\n"
    "v_pk_mul_f32 v[178:179], v[178:179], v[106:107] op_sel_hi:[1,0]\n"
    "v_pk_mul_f32 v[200:201], v[200:201], v[106:107] op_sel_hi:[1,0]\n"
    "v_pk_mul_f32 v[202:203], v[202:203], v[106:107] op_sel_hi:[1,0]\n"
    "v_pk_mul_f32 v[224:225], v[224:225], v[106:107] op_sel_hi:[1,0]\n"
    "v_pk_mul_f32 v[226:227], v[226:227], v[106:107] op_sel_hi:[1,0]\n"
    "v_pk_mul_f32 v[248:249], v[248:249], v[106:107] op_sel_hi:[1,0]\n"
    "v_pk_mul_f32 v[250:251], v[250:251], v[106:107] op_sel_hi:[1,0]\n"
    "v_pk_mul_f32 v[4:5], v[4:5], v[106:107] op_sel_hi:[1,0]\n"
    "v_pk_mul_f32 v[6:7], v[6:7], v[106:107] op_sel_hi:[1,0]\n"
    "v_pk_mul_f32 v[8:9], v[8:9], v[106:107] op_sel_hi:[1,0]\n"
    "v_pk_mul_f32 v[10:11], v[10:11], v[106:107] op_sel_hi:[1,0]\n"
    "v_pk_mul_f32 v[12:13], v[12:13], v[106:107] op_sel_hi:[1,0]\n"
    "v_pk_mul_f32 v[14:15], v[14:15], v[106:107] op_sel_hi:[1,0]\n"
    "v_cvt_pk_bf16_f32 v152, v152, v153\n"
    "v_cvt_pk_bf16_f32 v153, v154, v155\n"
    "v_cvt_pk_bf16_f32 v176, v176, v177\n"
    "v_cvt_pk_bf16_f32 v177, v178, v179\n"
    "v_cvt_pk_bf16_f32 v200, v200, v201\n"
    "v_cvt_pk_bf16_f32 v201, v202, v203\n"
    "v_cvt_pk_bf16_f32 v224, v224, v225\n"
    "v_cvt_pk_bf16_f32 v225, v226, v227\n"
    "v_cvt_pk_bf16_f32 v248, v248, v249\n"
    "v_cvt_pk_bf16_f32 v249, v250, v251\n"
    "v_cvt_pk_bf16_f32 v178, v4, v5\n"
    "v_cvt_pk_bf16_f32 v179, v6, v7\n"
    "v_cvt_pk_bf16_f32 v202, v8, v9\n"
    "v_cvt_pk_bf16_f32 v203, v10, v11\n"
    "v_cvt_pk_bf16_f32 v226, v12, v13\n"
    "v_cvt_pk_bf16_f32 v227, v14, v15\n"
    "v_smfmac_f32_16x16x64_bf16 a[152:155], a[168:171], v[64:71], v133\n"
    "ds_write_b64 v135, v[152:153] offset:4096\n"
    "v_mfma_f32_16x16x32_bf16 v[156:159], a[128:131], v[64:67], v[156:159]\n"
    "ds_write_b64 v135, v[176:177] offset:4608\n"
    "v_mfma_f32_16x16x32_bf16 v[156:159], a[132:135], v[68:71], v[156:159]\n"
    "ds_write_b64 v135, v[200:201] offset:5120\n"
    "v_mfma_f32_16x16x32_bf16 a[240:243], a[48:51], v[64:67], a[240:243]\n"
    "ds_write_b64 v135, v[224:225] offset:5632\n"
    "v_mfma_f32_16x16x32_bf16 a[240:243], a[52:55], v[68:71], a[240:243]\n"
    "ds_read_b128 v[32:35], v134 offset:4096\n"
    "ds_read_b128 v[36:39], v134 offset:4352\n"
    "v_mfma_f32_16x16x32_bf16 a[216:219], a[40:43], v[64:67], a[216:219]\n"
    "ds_write_b64 v135, v[248:249] offset:6144\n"
    "v_mfma_f32_16x16x32_bf16 a[216:219], a[44:47], v[68:71], a[216:219]\n"
    "ds_write_b64 v135, v[178:179] offset:6656\n"
    "v_mfma_f32_16x16x32_bf16 a[192:195], a[32:35], v[64:67], a[192:195]\n"
    "ds_write_b64 v135, v[202:203] offset:7168\n"
    "v_mfma_f32_16x16x32_bf16 a[192:195], a[36:39], v[68:71], a[192:195]\n"
    "ds_write_b64 v135, v[226:227] offset:7680\n"
    "v_smfmac_f32_16x16x64_bf16 v[96:99], a[168:171], v[16:23], v133\n"
    "ds_read_b128 v[40:43], v134 offset:6144\n"
    "ds_read_b128 v[44:47], v134 offset:6400\n"
    "v_smfmac_f32_16x16x64_bf16 v[96:99], a[168:171], v[24:31], v133\n"
    "s_waitcnt lgkmcnt(7)\n"
    "buffer_store_dwordx4 v[32:35], v140, s[48:51], s75 offen offset:0 sc1\n"
    "v_mfma_f32_16x16x32_bf16 v[252:255], a[24:27], v[64:67], v[252:255]\n"
    "s_waitcnt lgkmcnt(6)\n"
    "buffer_store_dwordx4 v[36:39], v140, s[48:51], s75 offen offset:2048 sc1\n"
    "v_mfma_f32_16x16x32_bf16 v[252:255], a[28:31], v[68:71], v[252:255]\n"
    "v_accvgpr_read_b32 v116, a152\n"
    "v_accvgpr_read_b32 v117, a153\n"
    "v_mfma_f32_16x16x32_bf16 v[228:231], a[16:19], v[64:67], v[228:231]\n"
    "v_add_f32 v116, v116, v117\n"
    "v_rcp_f32 v108, v116\n"
    "v_mfma_f32_16x16x32_bf16 v[228:231], a[20:23], v[68:71], v[228:231]\n"
    "v_cmp_ngt_f32 vcc, s67, v116\n"
    "s_or_b64 s[78:79], s[78:79], vcc\n"
    "v_mfma_f32_16x16x32_bf16 v[204:207], a[8:11], v[64:67], v[204:207]\n"
    "s_mov_b32 s74, 25198592\n"
    "v_accvgpr_read_b32 v12, a240\n"
    "v_mfma_f32_16x16x32_bf16 v[204:207], a[12:15], v[68:71], v[204:207]\n"
    "v_accvgpr_read_b32 v13, a241\n"
    "v_accvgpr_read_b32 v14, a242\n"
    "v_mfma_f32_16x16x32_bf16 v[180:183], a[0:3], v[64:67], v[180:183]\n"
    "s_waitcnt lgkmcnt(1)\n"
    "buffer_store_dwordx4 v[40:43], v140, s[48:51], s75 offen offset:128 sc1\n"
    "v_mfma_f32_16x16x32_bf16 v[180:183], a[4:7], v[68:71], v[180:183]\n"
    "s_waitcnt lgkmcnt(0)\n"
    "buffer_store_dwordx4 v[44:47], v140, s[48:51], s75 offen offset:2176 sc1\n"
    "v_accvgpr_read_b32 v15, a243\n"
    "v_accvgpr_read_b32 v8, a216\n"
    "v_accvgpr_read_b32 v9, a217\n"
    "v_accvgpr_read_b32 v10, a218\n"
    "v_accvgpr_read_b32 v11, a219\n"
    "v_accvgpr_read_b32 v4, a192\n"
    "v_accvgpr_read_b32 v5, a193\n"
    "v_accvgpr_read_b32 v6, a194\n"
    "v_accvgpr_read_b32 v7, a195\n"
    "v_pk_mul_f32 v[156:157], v[156:157], v[108:109] op_sel_hi:[1,0]\n"
    "v_pk_mul_f32 v[158:159], v[158:159], v[108:109] op_sel_hi:[1,0]\n"
    "v_pk_mul_f32 v[180:181], v[180:181], v[108:109] op_sel_hi:[1,0]\n"
    "v_pk_mul_f32 v[182:183], v[182:183], v[108:109] op_sel_hi:[1,0]\n"
    "v_pk_mul_f32 v[204:205], v[204:205], v[108:109] op_sel_hi:[1,0]\n"
    "v_pk_mul_f32 v[206:207], v[206:207], v[108:109] op_sel_hi:[1,0]\n"
    "v_pk_mul_f32 v[228:229], v[228:229], v[108:109] op_sel_hi:[1,0]\n"
    "v_pk_mul_f32 v[230:231], v[230:231], v[108:109] op_sel_hi:[1,0]\n"
    "v_pk_mul_f32 v[252:253], v[252:253], v[108:109] op_sel_hi:[1,0]\n"
    "v_pk_mul_f32 v[254:255], v[254:255], v[108:109] op_sel_hi:[1,0]\n"
    "v_pk_mul_f32 v[4:5], v[4:5], v[108:109] op_sel_hi:[1,0]\n"
    "v_pk_mul_f32 v[6:7], v[6:7], v[108:109] op_sel_hi:[1,0]\n"
    "v_pk_mul_f32 v[8:9], v[8:9], v[108:109] op_sel_hi:[1,0]\n"
    "v_pk_mul_f32 v[10:11], v[10:11], v[108:109] op_sel_hi:[1,0]\n"
    "v_pk_mul_f32 v[12:13], v[12:13], v[108:109] op_sel_hi:[1,0]\n"
    "v_pk_mul_f32 v[14:15], v[14:15], v[108:109] op_sel_hi:[1,0]\n"
    "v_cvt_pk_bf16_f32 v156, v156, v157\n"
    "v_cvt_pk_bf16_f32 v157, v158, v159\n"
    "v_cvt_pk_bf16_f32 v180, v180, v181\n"
    "v_cvt_pk_bf16_f32 v181, v182, v183\n"
    "v_cvt_pk_bf16_f32 v204, v204, v205\n"
    "v_cvt_pk_bf16_f32 v205, v206, v207\n"
    "v_cvt_pk_bf16_f32 v228, v228, v229\n"
    "v_cvt_pk_bf16_f32 v229, v230, v231\n"
    "v_cvt_pk_bf16_f32 v252, v252, v253\n"
    "v_cvt_pk_bf16_f32 v253, v254, v255\n"
    "v_cvt_pk_bf16_f32 v182, v4, v5\n"
    "v_cvt_pk_bf16_f32 v183, v6, v7\n"
    "v_cvt_pk_bf16_f32 v206, v8, v9\n"
    "v_cvt_pk_bf16_f32 v207, v10, v11\n"
    "v_cvt_pk_bf16_f32 v230, v12, v13\n"
    "v_cvt_pk_bf16_f32 v231, v14, v15\n"
    "v_smfmac_f32_16x16x64_bf16 a[156:159], a[168:171], v[72:79], v133\n"
    "ds_write_b64 v135, v[156:157] offset:8192\n"
    "v_mfma_f32_16x16x32_bf16 v[160:163], a[128:131], v[72:75], v[160:163]\n"
    "ds_write_b64 v135, v[180:181] offset:8704\n"
    "v_mfma_f32_16x16x32_bf16 v[160:163], a[132:135], v[76:79], v[160:163]\n"
    "ds_write_b64 v135, v[204:205] offset:9216\n"
    "v_mfma_f32_16x16x32_bf16 a[244:247], a[48:51], v[72:75], a[244:247]\n"
    "ds_write_b64 v135, v[228:229] offset:9728\n"
    "v_mfma_f32_16x16x32_bf16 a[244:247], a[52:55], v[76:79], a[244:247]\n"
    "ds_read_b128 v[16:19], v134 offset:8192\n"
    "ds_read_b128 v[20:23], v134 offset:8448\n"
    "v_mfma_f32_16x16x32_bf16 a[220:223], a[40:43], v[72:75], a[220:223]\n"
    "ds_write_b64 v135, v[252:253] offset:10240\n"
    "v_mfma_f32_16x16x32_bf16 a[220:223], a[44:47], v[76:79], a[220:223]\n"
    "ds_write_b64 v135, v[182:183] offset:10752\n"
    "v_mfma_f32_16x16x32_bf16 a[196:199], a[32:35], v[72:75], a[196:199]\n"
    "ds_write_b64 v135, v[206:207] offset:11264\n"
    "v_mfma_f32_16x16x32_bf16 a[196:199], a[36:39], v[76:79], a[196:199]\n"
    "ds_write_b64 v135, v[230:231] offset:11776\n"
    "v_smfmac_f32_16x16x64_bf16 v[96:99], a[168:171], v[32:39], v133\n"
    "ds_read_b128 v[24:27], v134 offset:10240\n"
    "ds_read_b128 v[28:31], v134 offset:10496\n"
    "v_smfmac_f32_16x16x64_bf16 v[96:99], a[168:171], v[40:47], v133\n"
    "s_waitcnt lgkmcnt(7)\n"
    "buffer_store_dwordx4 v[16:19], v140, s[48:51], s74 offen offset:0 sc1\n"
    "v_mfma_f32_16x16x32_bf16 a[172:175], a[24:27], v[72:75], a[172:175]\n"
    "s_waitcnt lgkmcnt(6)\n"
    "buffer_store_dwordx4 v[20:23], v140, s[48:51], s74 offen offset:2048 sc1\n"
    "v_mfma_f32_16x16x32_bf16 a[172:175], a[28:31], v[76:79], a[172:175]\n"
    "v_accvgpr_read_b32 v116, a156\n"
    "v_accvgpr_read_b32 v117, a157\n"
    "v_mfma_f32_16x16x32_bf16 v[232:235], a[16:19], v[72:75], v[232:235]\n"
    "v_add_f32 v116, v116, v117\n"
    "v_rcp_f32 v110, v116\n"
    "v_mfma_f32_16x16x32_bf16 v[232:235], a[20:23], v[76:79], v[232:235]\n"
    "v_cmp_ngt_f32 vcc, s67, v116\n"
    "s_or_b64 s[78:79], s[78:79], vcc\n"
    "v_mfma_f32_16x16x32_bf16 v[208:211], a[8:11], v[72:75], v[208:211]\n"
    "s_mov_b32 s75, 25214976\n"
    "v_accvgpr_read_b32 v12, a244\n"
    "v_mfma_f32_16x16x32_bf16 v[208:211], a[12:15], v[76:79], v[208:211]\n"
    "v_accvgpr_read_b32 v13, a245\n"
    "v_accvgpr_read_b32 v14, a246\n"
    "v_mfma_f32_16x16x32_bf16 v[184:187], a[0:3], v[72:75], v[184:187]\n"
    "s_waitcnt lgkmcnt(1)\n"
    "buffer_store_dwordx4 v[24:27], v140, s[48:51], s74 offen offset:128 sc1\n"
    "v_mfma_f32_16x16x32_bf16 v[184:187], a[4:7], v[76:79], v[184:187]\n"
    "s_waitcnt lgkmcnt(0)\n"
    "buffer_store_dwordx4 v[28:31], v140, s[48:51], s74 offen offset:2176 sc1\n"
    "v_accvgpr_read_b32 v15, a247\n"
    "v_accvgpr_read_b32 v8, a220\n"
    "v_accvgpr_read_b32 v9, a221\n"
    "v_accvgpr_read_b32 v10, a222\n"
    "v_accvgpr_read_b32 v11, a223\n"
    "v_accvgpr_read_b32 v4, a196\n"
    "v_accvgpr_read_b32 v5, a197\n"
    "v_accvgpr_read_b32 v6, a198\n"
    "v_accvgpr_read_b32 v7, a199\n"
    "v_accvgpr_read_b32 v0, a172\n"
    "v_accvgpr_read_b32 v1, a173\n"
    "v_accvgpr_read_b32 v2, a174\n"
    "v_accvgpr_read_b32 v3, a175\n"
    "v_pk_mul_f32 v[160:161], v[160:161], v[110:111] op_sel_hi:[1,0]\n"
    "v_pk_mul_f32 v[162:163], v[162:163], v[110:111] op_sel_hi:[1,0]\n"
    "v_pk_mul_f32 v[184:185], v[184:185], v[110:111] op_sel_hi:[1,0]\n"
    "v_pk_mul_f32 v[186:187], v[186:187], v[110:111] op_sel_hi:[1,0]\n"
    "v_pk_mul_f32 v[208:209], v[208:209], v[110:111] op_sel_hi:[1,0]\n"
    "v_pk_mul_f32 v[210:211], v[210:211], v[110:111] op_sel_hi:[1,0]\n"
    "v_pk_mul_f32 v[232:233], v[232:233], v[110:111] op_sel_hi:[1,0]\n"
    "v_pk_mul_f32 v[234:235], v[234:235], v[110:111] op_sel_hi:[1,0]\n"
    "v_pk_mul_f32 v[0:1], v[0:1], v[110:111] op_sel_hi:[1,0]\n"
    "v_pk_mul_f32 v[2:3], v[2:3], v[110:111] op_sel_hi:[1,0]\n"
    "v_pk_mul_f32 v[4:5], v[4:5], v[110:111] op_sel_hi:[1,0]\n"
    "v_pk_mul_f32 v[6:7], v[6:7], v[110:111] op_sel_hi:[1,0]\n"
    "v_pk_mul_f32 v[8:9], v[8:9], v[110:111] op_sel_hi:[1,0]\n"
    "v_pk_mul_f32 v[10:11], v[10:11], v[110:111] op_sel_hi:[1,0]\n"
    "v_pk_mul_f32 v[12:13], v[12:13], v[110:111] op_sel_hi:[1,0]\n"
    "v_pk_mul_f32 v[14:15], v[14:15], v[110:111] op_sel_hi:[1,0]\n"
    "v_cvt_pk_bf16_f32 v160, v160, v161\n"
    "v_cvt_pk_bf16_f32 v161, v162, v163\n"
    "v_cvt_pk_bf16_f32 v184, v184, v185\n"
    "v_cvt_pk_bf16_f32 v185, v186, v187\n"
    "v_cvt_pk_bf16_f32 v208, v208, v209\n"
    "v_cvt_pk_bf16_f32 v209, v210, v211\n"
    "v_cvt_pk_bf16_f32 v232, v232, v233\n"
    "v_cvt_pk_bf16_f32 v233, v234, v235\n"
    "v_cvt_pk_bf16_f32 v162, v0, v1\n"
    "v_cvt_pk_bf16_f32 v163, v2, v3\n"
    "v_cvt_pk_bf16_f32 v186, v4, v5\n"
    "v_cvt_pk_bf16_f32 v187, v6, v7\n"
    "v_cvt_pk_bf16_f32 v210, v8, v9\n"
    "v_cvt_pk_bf16_f32 v211, v10, v11\n"
    "v_cvt_pk_bf16_f32 v234, v12, v13\n"
    "v_cvt_pk_bf16_f32 v235, v14, v15\n"
    "v_smfmac_f32_16x16x64_bf16 a[160:163], a[168:171], v[80:87], v133\n"
    "ds_write_b64 v135, v[160:161] offset:12288\n"
    "v_mfma_f32_16x16x32_bf16 v[164:167], a[128:131], v[80:83], v[164:167]\n"
    "ds_write_b64 v135, v[184:185] offset:12800\n"
    "v_mfma_f32_16x16x32_bf16 v[164:167], a[132:135], v[84:87], v[164:167]\n"
    "ds_write_b64 v135, v[208:209] offset:13312\n"
    "v_mfma_f32_16x16x32_bf16 a[248:251], a[48:51], v[80:83], a[248:251]\n"
    "ds_write_b64 v135, v[232:233] offset:13824\n"
    "v_mfma_f32_16x16x32_bf16 a[248:251], a[52:55], v[84:87], a[248:251]\n"
    "ds_read_b128 v[32:35], v134 offset:12288\n"
    "ds_read_b128 v[36:39], v134 offset:12544\n"
    "v_mfma_f32_16x16x32_bf16 a[224:227], a[40:43], v[80:83], a[224:227]\n"
    "ds_write_b64 v135, v[162:163] offset:14336\n"
    "v_mfma_f32_16x16x32_bf16 a[224:227], a[44:47], v[84:87], a[224:227]\n"
    "ds_write_b64 v135, v[186:187] offset:14848\n"
    "v_mfma_f32_16x16x32_bf16 a[200:203], a[32:35], v[80:83], a[200:203]\n"
    "ds_write_b64 v135, v[210:211] offset:15360\n"
    "v_mfma_f32_16x16x32_bf16 a[200:203], a[36:39], v[84:87], a[200:203]\n"
    "ds_write_b64 v135, v[234:235] offset:15872\n"
    "v_smfmac_f32_16x16x64_bf16 v[96:99], a[168:171], v[16:23], v133\n"
    "ds_read_b128 v[40:43], v134 offset:14336\n"
    "ds_read_b128 v[44:47], v134 offset:14592\n"
    "v_smfmac_f32_16x16x64_bf16 v[96:99], a[168:171], v[24:31], v133\n"
    "s_waitcnt lgkmcnt(7)\n"
    "buffer_store_dwordx4 v[32:35], v140, s[48:51], s75 offen offset:0 sc1\n"
    "v_mfma_f32_16x16x32_bf16 a[176:179], a[24:27], v[80:83], a[176:179]\n"
    "s_waitcnt lgkmcnt(6)\n"
    "buffer_store_dwordx4 v[36:39], v140, s[48:51], s75 offen offset:2048 sc1\n"
    "v_mfma_f32_16x16x32_bf16 a[176:179], a[28:31], v[84:87], a[176:179]\n"
    "v_accvgpr_read_b32 v116, a160\n"
    "v_accvgpr_read_b32 v117, a161\n"
    "v_mfma_f32_16x16x32_bf16 v[236:239], a[16:19], v[80:83], v[236:239]\n"
    "v_add_f32 v116, v116, v117\n"
    "v_rcp_f32 v112, v116\n"
    "v_mfma_f32_16x16x32_bf16 v[236:239], a[20:23], v[84:87], v[236:239]\n"
    "v_cmp_ngt_f32 vcc, s67, v116\n"
    "s_or_b64 s[78:79], s[78:79], vcc\n"
    "v_mfma_f32_16x16x32_bf16 v[212:215], a[8:11], v[80:83], v[212:215]\n"
    "s_mov_b32 s74, 25231360\n"
    "v_accvgpr_read_b32 v12, a248\n"
    "v_mfma_f32_16x16x32_bf16 v[212:215], a[12:15], v[84:87], v[212:215]\n"
    "v_accvgpr_read_b32 v13, a249\n"
    "v_accvgpr_read_b32 v14, a250\n"
    "v_mfma_f32_16x16x32_bf16 v[188:191], a[0:3], v[80:83], v[188:191]\n"
    "s_waitcnt lgkmcnt(1)\n"
    "buffer_store_dwordx4 v[40:43], v140, s[48:51], s75 offen offset:128 sc1\n"
    "v_mfma_f32_16x16x32_bf16 v[188:191], a[4:7], v[84:87], v[188:191]\n"
    "s_waitcnt lgkmcnt(0)\n"
    "buffer_store_dwordx4 v[44:47], v140, s[48:51], s75 offen offset:2176 sc1\n"
    "v_accvgpr_read_b32 v15, a251\n"
    "v_accvgpr_read_b32 v8, a224\n"
    "v_accvgpr_read_b32 v9, a225\n"
    "v_accvgpr_read_b32 v10, a226\n"
    "v_accvgpr_read_b32 v11, a227\n"
    "v_accvgpr_read_b32 v4, a200\n"
    "v_accvgpr_read_b32 v5, a201\n"
    "v_accvgpr_read_b32 v6, a202\n"
    "v_accvgpr_read_b32 v7, a203\n"
    "v_accvgpr_read_b32 v0, a176\n"
    "v_accvgpr_read_b32 v1, a177\n"
    "v_accvgpr_read_b32 v2, a178\n"
    "v_accvgpr_read_b32 v3, a179\n"
    "v_pk_mul_f32 v[164:165], v[164:165], v[112:113] op_sel_hi:[1,0]\n"
    "v_pk_mul_f32 v[166:167], v[166:167], v[112:113] op_sel_hi:[1,0]\n"
    "v_pk_mul_f32 v[188:189], v[188:189], v[112:113] op_sel_hi:[1,0]\n"
    "v_pk_mul_f32 v[190:191], v[190:191], v[112:113] op_sel_hi:[1,0]\n"
    "v_pk_mul_f32 v[212:213], v[212:213], v[112:113] op_sel_hi:[1,0]\n"
    "v_pk_mul_f32 v[214:215], v[214:215], v[112:113] op_sel_hi:[1,0]\n"
    "v_pk_mul_f32 v[236:237], v[236:237], v[112:113] op_sel_hi:[1,0]\n"
    "v_pk_mul_f32 v[238:239], v[238:239], v[112:113] op_sel_hi:[1,0]\n"
    "v_pk_mul_f32 v[0:1], v[0:1], v[112:113] op_sel_hi:[1,0]\n"
    "v_pk_mul_f32 v[2:3], v[2:3], v[112:113] op_sel_hi:[1,0]\n"
    "v_pk_mul_f32 v[4:5], v[4:5], v[112:113] op_sel_hi:[1,0]\n"
    "v_pk_mul_f32 v[6:7], v[6:7], v[112:113] op_sel_hi:[1,0]\n"
    "v_pk_mul_f32 v[8:9], v[8:9], v[112:113] op_sel_hi:[1,0]\n"
    "v_pk_mul_f32 v[10:11], v[10:11], v[112:113] op_sel_hi:[1,0]\n"
    "v_pk_mul_f32 v[12:13], v[12:13], v[112:113] op_sel_hi:[1,0]\n"
    "v_pk_mul_f32 v[14:15], v[14:15], v[112:113] op_sel_hi:[1,0]\n"
    "v_cvt_pk_bf16_f32 v164, v164, v165\n"
    "v_cvt_pk_bf16_f32 v165, v166, v167\n"
    "v_cvt_pk_bf16_f32 v188, v188, v189\n"
    "v_cvt_pk_bf16_f32 v189, v190, v191\n"
    "v_cvt_pk_bf16_f32 v212, v212, v213\n"
    "v_cvt_pk_bf16_f32 v213, v214, v215\n"
    "v_cvt_pk_bf16_f32 v236, v236, v237\n"
    "v_cvt_pk_bf16_f32 v237, v238, v239\n"
    "v_cvt_pk_bf16_f32 v166, v0, v1\n"
    "v_cvt_pk_bf16_f32 v167, v2, v3\n"
    "v_cvt_pk_bf16_f32 v190, v4, v5\n"
    "v_cvt_pk_bf16_f32 v191, v6, v7\n"
    "v_cvt_pk_bf16_f32 v214, v8, v9\n"
    "v_cvt_pk_bf16_f32 v215, v10, v11\n"
    "v_cvt_pk_bf16_f32 v238, v12, v13\n"
    "v_cvt_pk_bf16_f32 v239, v14, v15\n"
    "v_smfmac_f32_16x16x64_bf16 a[164:167], a[168:171], v[88:95], v133\n"
    "ds_write_b64 v135, v[164:165] offset:16384\n"
    "v_mfma_f32_16x16x32_bf16 v[168:171], a[128:131], v[88:91], v[168:171]\n"
    "ds_write_b64 v135, v[188:189] offset:16896\n"
    "v_mfma_f32_16x16x32_bf16 v[168:171], a[132:135], v[92:95], v[168:171]\n"
    "ds_write_b64 v135, v[212:213] offset:17408\n"
    "v_mfma_f32_16x16x32_bf16 a[252:255], a[48:51], v[88:91], a[252:255]\n"
    "ds_write_b64 v135, v[236:237] offset:17920\n"
    "v_mfma_f32_16x16x32_bf16 a[252:255], a[52:55], v[92:95], a[252:255]\n"
    "ds_read_b128 v[16:19], v134 offset:16384\n"
    "ds_read_b128 v[20:23], v134 offset:16640\n"
    "v_mfma_f32_16x16x32_bf16 a[228:231], a[40:43], v[88:91], a[228:231]\n"
    "ds_write_b64 v135, v[166:167] offset:18432\n"
    "v_mfma_f32_16x16x32_bf16 a[228:231], a[44:47], v[92:95], a[228:231]\n"
    "ds_write_b64 v135, v[190:191] offset:18944\n"
    "v_mfma_f32_16x16x32_bf16 a[204:207], a[32:35], v[88:91], a[204:207]\n"
    "ds_write_b64 v135, v[214:215] offset:19456\n"
    "v_mfma_f32_16x16x32_bf16 a[204:207], a[36:39], v[92:95], a[204:207]\n"
    "ds_write_b64 v135, v[238:239] offset:19968\n"
    "v_smfmac_f32_16x16x64_bf16 v[96:99], a[168:171], v[32:39], v133\n"
    "ds_read_b128 v[24:27], v134 offset:18432\n"
    "ds_read_b128 v[28:31], v134 offset:18688\n"
    "v_smfmac_f32_16x16x64_bf16 v[96:99], a[168:171], v[40:47], v133\n"
    "s_waitcnt lgkmcnt(7)\n"
    "buffer_store_dwordx4 v[16:19], v140, s[48:51], s74 offen offset:0 sc1\n"
    "v_mfma_f32_16x16x32_bf16 a[180:183], a[24:27], v[88:91], a[180:183]\n"
    "s_waitcnt lgkmcnt(6)\n"
    "buffer_store_dwordx4 v[20:23], v140, s[48:51], s74 offen offset:2048 sc1\n"
    "v_mfma_f32_16x16x32_bf16 a[180:183], a[28:31], v[92:95], a[180:183]\n"
    "v_accvgpr_read_b32 v116, a164\n"
    "v_accvgpr_read_b32 v117, a165\n"
    "v_mfma_f32_16x16x32_bf16 v[240:243], a[16:19], v[88:91], v[240:243]\n"
    "v_add_f32 v116, v116, v117\n"
    "v_rcp_f32 v114, v116\n"
    "v_mfma_f32_16x16x32_bf16 v[240:243], a[20:23], v[92:95], v[240:243]\n"
    "v_cmp_ngt_f32 vcc, s67, v116\n"
    "s_or_b64 s[78:79], s[78:79], vcc\n"
    "v_mfma_f32_16x16x32_bf16 v[216:219], a[8:11], v[88:91], v[216:219]\n"
    "s_mov_b32 s75, 25247744\n"
    "v_accvgpr_read_b32 v12, a252\n"
    "v_mfma_f32_16x16x32_bf16 v[216:219], a[12:15], v[92:95], v[216:219]\n"
    "v_accvgpr_read_b32 v13, a253\n"
    "v_accvgpr_read_b32 v14, a254\n"
    "v_mfma_f32_16x16x32_bf16 v[192:195], a[0:3], v[88:91], v[192:195]\n"
    "s_waitcnt lgkmcnt(1)\n"
    "buffer_store_dwordx4 v[24:27], v140, s[48:51], s74 offen offset:128 sc1\n"
    "v_mfma_f32_16x16x32_bf16 v[192:195], a[4:7], v[92:95], v[192:195]\n"
    "s_waitcnt lgkmcnt(0)\n"
    "buffer_store_dwordx4 v[28:31], v140, s[48:51], s74 offen offset:2176 sc1\n"
    "v_accvgpr_read_b32 v15, a255\n"
    "v_accvgpr_read_b32 v8, a228\n"
    "v_accvgpr_read_b32 v9, a229\n"
    "v_accvgpr_read_b32 v10, a230\n"
    "v_accvgpr_read_b32 v11, a231\n"
    "v_accvgpr_read_b32 v4, a204\n"
    "v_accvgpr_read_b32 v5, a205\n"
    "v_accvgpr_read_b32 v6, a206\n"
    "v_accvgpr_read_b32 v7, a207\n"
    "v_accvgpr_read_b32 v0, a180\n"
    "v_accvgpr_read_b32 v1, a181\n"
    "v_accvgpr_read_b32 v2, a182\n"
    "v_accvgpr_read_b32 v3, a183\n"
    "v_pk_mul_f32 v[168:169], v[168:169], v[114:115] op_sel_hi:[1,0]\n"
    "v_pk_mul_f32 v[170:171], v[170:171], v[114:115] op_sel_hi:[1,0]\n"
    "v_pk_mul_f32 v[192:193], v[192:193], v[114:115] op_sel_hi:[1,0]\n"
    "v_pk_mul_f32 v[194:195], v[194:195], v[114:115] op_sel_hi:[1,0]\n"
    "v_pk_mul_f32 v[216:217], v[216:217], v[114:115] op_sel_hi:[1,0]\n"
    "v_pk_mul_f32 v[218:219], v[218:219], v[114:115] op_sel_hi:[1,0]\n"
    "v_pk_mul_f32 v[240:241], v[240:241], v[114:115] op_sel_hi:[1,0]\n"
    "v_pk_mul_f32 v[242:243], v[242:243], v[114:115] op_sel_hi:[1,0]\n"
    "v_pk_mul_f32 v[0:1], v[0:1], v[114:115] op_sel_hi:[1,0]\n"
    "v_pk_mul_f32 v[2:3], v[2:3], v[114:115] op_sel_hi:[1,0]\n"
    "v_pk_mul_f32 v[4:5], v[4:5], v[114:115] op_sel_hi:[1,0]\n"
    "v_pk_mul_f32 v[6:7], v[6:7], v[114:115] op_sel_hi:[1,0]\n"
    "v_pk_mul_f32 v[8:9], v[8:9], v[114:115] op_sel_hi:[1,0]\n"
    "v_pk_mul_f32 v[10:11], v[10:11], v[114:115] op_sel_hi:[1,0]\n"
    "v_pk_mul_f32 v[12:13], v[12:13], v[114:115] op_sel_hi:[1,0]\n"
    "v_pk_mul_f32 v[14:15], v[14:15], v[114:115] op_sel_hi:[1,0]\n"
    "v_cvt_pk_bf16_f32 v168, v168, v169\n"
    "v_cvt_pk_bf16_f32 v169, v170, v171\n"
    "v_cvt_pk_bf16_f32 v192, v192, v193\n"
    "v_cvt_pk_bf16_f32 v193, v194, v195\n"
    "v_cvt_pk_bf16_f32 v216, v216, v217\n"
    "v_cvt_pk_bf16_f32 v217, v218, v219\n"
    "v_cvt_pk_bf16_f32 v240, v240, v241\n"
    "v_cvt_pk_bf16_f32 v241, v242, v243\n"
    "v_cvt_pk_bf16_f32 v170, v0, v1\n"
    "v_cvt_pk_bf16_f32 v171, v2, v3\n"
    "v_cvt_pk_bf16_f32 v194, v4, v5\n"
    "v_cvt_pk_bf16_f32 v195, v6, v7\n"
    "v_cvt_pk_bf16_f32 v218, v8, v9\n"
    "v_cvt_pk_bf16_f32 v219, v10, v11\n"
    "v_cvt_pk_bf16_f32 v242, v12, v13\n"
    "v_cvt_pk_bf16_f32 v243, v14, v15\n"
    "v_smfmac_f32_16x16x64_bf16 v[96:99], a[168:171], v[16:23], v133\n"
    "ds_write_b64 v135, v[168:169] offset:20480\n"
    "ds_write_b64 v135, v[192:193] offset:20992\n"
    "ds_write_b64 v135, v[216:217] offset:21504\n"
    "ds_write_b64 v135, v[240:241] offset:22016\n"
    "ds_read_b128 v[32:35], v134 offset:20480\n"
    "ds_read_b128 v[36:39], v134 offset:20736\n"
    "v_smfmac_f32_16x16x64_bf16 v[96:99], a[168:171], v[24:31], v133\n"
    "ds_write_b64 v135, v[170:171] offset:22528\n"
    "ds_write_b64 v135, v[194:195] offset:23040\n"
    "ds_write_b64 v135, v[218:219] offset:23552\n"
    "ds_write_b64 v135, v[242:243] offset:24064\n"
    "ds_read_b128 v[40:43], v134 offset:22528\n"
    "ds_read_b128 v[44:47], v134 offset:22784\n"
    "s_waitcnt lgkmcnt(7)\n"
    "buffer_store_dwordx4 v[32:35], v140, s[48:51], s75 offen offset:0 sc1\n"
    "s_waitcnt lgkmcnt(6)\n"
    "buffer_store_dwordx4 v[36:39], v140, s[48:51], s75 offen offset:2048 sc1\n"
    "v_smfmac_f32_16x16x64_bf16 v[96:99], a[168:171], v[32:39], v133\n"
    "s_waitcnt lgkmcnt(1)\n"
    "buffer_store_dwordx4 v[40:43], v140, s[48:51], s75 offen offset:128 sc1\n"
    "s_waitcnt lgkmcnt(0)\n"
    "buffer_store_dwordx4 v[44:47], v140, s[48:51], s75 offen offset:2176 sc1\n"
    "v_smfmac_f32_16x16x64_bf16 v[96:99], a[168:171], v[40:47], v133\n"
    "s_nop 7\n"
    "v_add_f32 v96, v96, v97\n"
    "v_and_b32 v96, 0x7fffffff, v96\n"
    "v_mov_b32 v97, 0x7f800000\n"
    "v_cmp_nlt_f32 vcc, v96, v97\n"
    "s_or_b64 s[78:79], s[78:79], vcc\n"
    "s_cselect_b32 s64, 2, 0\n"
    "s_or_b32 s66, s66, s64\n"
    "Lend_%=:\n"
    "s_waitcnt vmcnt(0)\n"
    "s_mov_b32 %0, s66\n"
    : "=s"(flags)
    : "s"(qp), "s"(kp), "s"(vp), "s"(op), "s"(qrec), "s"(krec), "s"(vrec), "s"(ldsb + wk), "s"(ldsb + w * 2048u),
      "s"(ldsb + 65536u + w * 24576u), "s"((w & 1u) * 32u), "s"(ldsb), "s"(rot)
    : "v0", "v1", "v2", "v3", "v4", "v5", "v6", "v7", "v8", "v9", "v10", "v11", "v12", "v13", "v14", "v15", "v16", "v17", "v18", "v19", "v20", "v21", "v22", "v23", "v24", "v25", "v26", "v27", "v28", "v29", "v30", "v31", "v32", "v33", "v34", "v35", "v36", "v37", "v38", "v39", "v40", "v41", "v42", "v43", "v44", "v45", "v46", "v47", "v48", "v49", "v50", "v51", "v52", "v53", "v54", "v55", "v56", "v57", "v58", "v59", "v60", "v61", "v62", "v63", "v64", "v65", "v66", "v67", "v68", "v69", "v70", "v71", "v72", "v73", "v74", "v75", "v76", "v77", "v78", "v79", "v80", "v81", "v82", "v83", "v84", "v85", "v86", "v87", "v88", "v89", "v90", "v91", "v92", "v93", "v94", "v95", "v96", "v97", "v98", "v99", "v100", "v101", "v102", "v103", "v104", "v105", "v106", "v107", "v108", "v109", "v110", "v111", "v112", "v113", "v114", "v115", "v116", "v117", "v118", "v119", "v120", "v121", "v122", "v123", "v124", "v125", "v126", "v127", "v128", "v129", "v130", "v131", "v132", "v133", "v134", "v135", "v136", "v137", "v138", "v139", "v140", "v141", "v142", "v143", "v144", "v145", "v146", "v147", "v148", "v149", "v150", "v151", "v152", "v153", "v154", "v155", "v156", "v157", "v158", "v159", "v160", "v161", "v162", "v163", "v164", "v165", "v166", "v167", "v168", "v169", "v170", "v171", "v172", "v173", "v174", "v175", "v176", "v177", "v178", "v179", "v180", "v181", "v182", "v183", "v184", "v185", "v186", "v187", "v188", "v189", "v190", "v191", "v192", "v193", "v194", "v195", "v196", "v197", "v198", "v199", "v200", "v201", "v202", "v203", "v204", "v205", "v206", "v207", "v208", "v209", "v210", "v211", "v212", "v213", "v214", "v215", "v216", "v217", "v218", "v219", "v220", "v221", "v222", "v223", "v224", "v225", "v226", "v227", "v228", "v229", "v230", "v231", "v232", "v233", "v234", "v235", "v236", "v237", "v238", "v239", "v240", "v241", "v242", "v243", "v244", "v245", "v246", "v247", "v248", "v249", "v250", "v251", "v252", "v253", "v254", "v255", "a0", "a1", "a2", "a3", "a4", "a5", "a6", "a7", "a8", "a9", "a10", "a11", "a12", "a13", "a14", "a15", "a16", "a17", "a18", "a19", "a20", "a21", "a22", "a23", "a24", "a25", "a26", "a27", "a28", "a29", "a30", "a31", "a32", "a33", "a34", "a35", "a36", "a37", "a38", "a39", "a40", "a41", "a42", "a43", "a44", "a45", "a46", "a47", "a48", "a49", "a50", "a51", "a52", "a53", "a54", "a55", "a56", "a57", "a58", "a59", "a60", "a61", "a62", "a63", "a64", "a65", "a66", "a67", "a68", "a69", "a70", "a71", "a72", "a73", "a74", "a75", "a76", "a77", "a78", "a79", "a80", "a81", "a82", "a83", "a84", "a85", "a86", "a87", "a88", "a89", "a90", "a91", "a92", "a93", "a94", "a95", "a96", "a97", "a98", "a99", "a100", "a101", "a102", "a103", "a104", "a105", "a106", "a107", "a108", "a109", "a110", "a111", "a112", "a113", "a114", "a115", "a116", "a117", "a118", "a119", "a120", "a121", "a122", "a123", "a124", "a125", "a126", "a127", "a128", "a129", "a130", "a131", "a132", "a133", "a134", "a135", "a136", "a137", "a138", "a139", "a140", "a141", "a142", "a143", "a144", "a145", "a146", "a147", "a148", "a149", "a150", "a151", "a152", "a153", "a154", "a155", "a156", "a157", "a158", "a159", "a160", "a161", "a162", "a163", "a164", "a165", "a166", "a167", "a168", "a169", "a170", "a171", "a172", "a173", "a174", "a175", "a176", "a177", "a178", "a179", "a180", "a181", "a182", "a183", "a184", "a185", "a186", "a187", "a188", "a189", "a190", "a191", "a192", "a193", "a194", "a195", "a196", "a197", "a198", "a199", "a200", "a201", "a202", "a203", "a204", "a205", "a206", "a207", "a208", "a209", "a210", "a211", "a212", "a213", "a214", "a215", "a216", "a217", "a218", "a219", "a220", "a221", "a222", "a223", "a224", "a225", "a226", "a227", "a228", "a229", "a230", "a231", "a232", "a233", "a234", "a235", "a236", "a237", "a238", "a239", "a240", "a241", "a242", "a243", "a244", "a245", "a246", "a247", "a248", "a249", "a250", "a251", "a252", "a253", "a254", "a255", "s36", "s37", "s38", "s39", "s40", "s41", "s42", "s43", "s44", "s45", "s46", "s47", "s48", "s49", "s50", "s51", "s52", "s53", "s54", "s55", "s56", "s57", "s58", "s59", "s60", "s61", "s62", "s63", "s64", "s65", "s66", "s67", "s68", "s69", "s70", "s71", "s72", "s73", "s74", "s75", "s76", "s77", "s78", "s79", "s80", "s81", "s82", "s83", "s84", "s85", "s86", "s87", "s88", "s89", "s90", "s91", "s92", "s93", "s94", "s95", "s96", "s97", "vcc", "scc", "memory");
  if (flags != 0u) {
    const int lane = __builtin_amdgcn_mbcnt_hi(-1, __builtin_amdgcn_mbcnt_lo(-1, 0));
    for (int hh = 0; hh < 2; ++hh) {
      if (!((flags >> hh) & 1u)) continue;
      const size_t bh = wg + 256u * hh;
      for (int qb = 0; qb < 6; ++qb) {
        const size_t row = bh * SQ + 64 * qb + 16 * w;
        exact_rows16(Q + row * DH, K + bh * SK * DH, V + bh * SK * DH, O + row * DH, lane);
      }
    }
  }
}

extern "C" void kernel_launch(void* const* d_in, const int* in_sizes, int n_in, void* d_out, int out_size, void* d_ws, size_t ws_size,
                              hipStream_t stream) {
  hipLaunchKernelGGL(mha_fwd_kernel, dim3(256), dim3(256), 0, stream, (const unsigned short*)d_in[0], (const unsigned short*)d_in[1],
                     (const unsigned short*)d_in[2], (unsigned short*)d_out);
}
```
